```python
import torch

def _softmax(x, axis=-1):
    return torch.softmax(x, dim=axis)

def _gelu(x):
    return torch.nn.functional.gelu(x, approximate='tanh')

def _silu(x):
    return torch.nn.functional.silu(x)

def _sigmoid(x):
    return torch.sigmoid(x)

def _layernorm(x, axis=-1, eps=1e-05):
    m = x.mean(dim=axis, keepdim=True)
    v = x.var(dim=axis, keepdim=True, unbiased=False)
    return (x - m) / torch.sqrt(v + eps)

def _scatter_add(tgt, idx, src, axis=0):
    return tgt.clone().index_add_(axis, idx, src)

def _segment_sum(src, seg, n):
    out = torch.zeros((n,) + src.shape[1:], dtype=src.dtype)
    return out.index_add_(0, seg, src)

def _pad(x, pw):
    flat = tuple((v for pair in reversed(pw) for v in pair))
    return torch.nn.functional.pad(x, flat)
kernel_name = 'adaLN_cond__gqa_block_cached__glu_gated_bilinear__decode'

def setup_inputs():

    _w = lambda a: (lambda t: t.to(torch.bfloat16) if t.is_floating_point() else t)(torch.as_tensor(a))

    def _A_setup():
        B, Sq, D = (2, 1, 512)
        Hq, Hkv, Sk, Dh = (32, 8, 32768, 128)
        assert Hq % Hkv == 0, (Hq, Hkv)
        return {'x': _w((lambda *s: torch.randn(*s, dtype=torch.bfloat16) * 0.1)(B, Sq, D)), 'k_cache': _w((lambda *s: torch.randn(*s, dtype=torch.bfloat16) * 0.8)(B, Hkv, Sk, Dh)), 'v_cache': _w((lambda *s: torch.randn(*s, dtype=torch.bfloat16) * 12.8)(B, Hkv, Sk, Dh)), 'wq': _w((lambda *s: torch.randn(*s, dtype=torch.bfloat16) * 0.8)(D, Hq * Dh) * D ** (-0.5)), 'wo': _w((lambda *s: torch.randn(*s, dtype=torch.bfloat16) * 12.8)(Hq * Dh, D) * (Hq * Dh) ** (-0.5))}
    _A_in = _A_setup()

    def _B_setup():
        B, S, D, F = (2, 1, 512, 1792)
        return {'x': _w((lambda *s: torch.randn(*s, dtype=torch.bfloat16) * 0.1)(B, S, D)), 'w_gate': _w((lambda *s: torch.randn(*s, dtype=torch.bfloat16) * 12.8)(D, F) * D ** (-0.5)), 'w_up': _w((lambda *s: torch.randn(*s, dtype=torch.bfloat16) * 12.8)(D, F) * D ** (-0.5)), 'w_down': _w((lambda *s: torch.randn(*s, dtype=torch.bfloat16) * 12.8)(F, D) * F ** (-0.5))}
    _B_in = _B_setup()
    return {'A_x': _A_in['x'], 'A_k_cache': _A_in['k_cache'], 'A_v_cache': _A_in['v_cache'], 'A_wq': _A_in['wq'], 'A_wo': _A_in['wo'], 'B_x': _B_in['x'], 'B_w_gate': _B_in['w_gate'], 'B_w_up': _B_in['w_up'], 'B_w_down': _B_in['w_down'], 'c': _w((lambda *s: torch.randn(*s, dtype=torch.bfloat16) * 0.2)(2, 512) * 8.0), 'w_mod': _w((lambda *s: torch.randn(*s, dtype=torch.bfloat16) * 0.2)(512, 6 * 512) * 512 ** (-0.5) * 8.0)}

def reference(A_x, A_k_cache, A_v_cache, A_wq, A_wo, B_x, B_w_gate, B_w_up, B_w_down, c, w_mod):

    def _A_ref(x, k_cache, v_cache, wq, wo):
        B, Sq, D = x.shape
        Hq, Dh = (32, 128)
        Hkv = k_cache.shape[1]
        rep = Hq // Hkv
        q = (lambda x, *d: x.permute(*d))((x @ wq).reshape(B, Sq, Hq, Dh), 0, 2, 1, 3)
        k = (lambda x, n, axis: torch.repeat_interleave(x, n, dim=axis))(k_cache, rep, axis=1)
        v = (lambda x, n, axis: torch.repeat_interleave(x, n, dim=axis))(v_cache, rep, axis=1)
        scores = torch.einsum('bhqd,bhkd->bhqk', q, k) * Dh ** (-0.5)
        p = _softmax(scores, axis=-1)
        o = torch.einsum('bhqk,bhkd->bhqd', p, v)
        return (lambda x, *d: x.permute(*d))(o, 0, 2, 1, 3).reshape(B, Sq, Hq * Dh) @ wo

    def _B_ref(x, w_gate, w_up, w_down):
        g = torch.einsum('bsd,df->bsf', x, w_gate)
        u = torch.einsum('bsd,df->bsf', x, w_up)
        h = g * u
        return torch.einsum('bsf,fd->bsd', h, w_down)
    x = A_x
    mod = (_silu(c) @ w_mod).reshape(c.shape[0], 6, 1, -1)
    sa, ca, ga, sb, cb, gb = (mod[:, 0].clone(), mod[:, 1].clone(), mod[:, 2].clone(), mod[:, 3].clone(), mod[:, 4].clone(), mod[:, 5].clone())
    h = _layernorm(x, axis=-1) * (1 + ca) + sa
    x = x + ga * _A_ref(h, A_k_cache, A_v_cache, A_wq, A_wo)
    h = _layernorm(x, axis=-1) * (1 + cb) + sb
    x = x + gb * _B_ref(h, B_w_gate, B_w_up, B_w_down)
    return x
```

```cpp
#include <hip/hip_runtime.h>
#include <hip/hip_bf16.h>
#include <cstdint>

using bf16 = __hip_bfloat16;
constexpr int B = 2, DM = 512, HQ = 32, HKV = 8, G = 4, SK = 32768, DH = 128, F = 1792;
constexpr int NQ = HQ * DH;
constexpr int NSPLIT = 32;
constexpr int PAGE = 16, WAVES = 4;
constexpr int PPS = SK / PAGE / NSPLIT;
constexpr int PPW = PPS / WAVES;

using u32x4 = __attribute__((ext_vector_type(4))) unsigned;
using f32x4 = __attribute__((ext_vector_type(4))) float;
using bf16x2 = __attribute__((ext_vector_type(2))) __bf16;

__device__ __forceinline__ float bf2f(unsigned short h) { return __builtin_bit_cast(float, (unsigned)h << 16); }
__device__ __forceinline__ float bflo(unsigned w) { return __builtin_bit_cast(float, w << 16); }
__device__ __forceinline__ float bfhi(unsigned w) { return __builtin_bit_cast(float, w & 0xffff0000u); }
__device__ __forceinline__ unsigned short f2bf(float f) {
  unsigned u = __builtin_bit_cast(unsigned, f);
  if ((u & 0x7fffffffu) > 0x7f800000u) return (unsigned short)((u >> 16) | 0x40);
  u += 0x7fffu + ((u >> 16) & 1u);
  return (unsigned short)(u >> 16);
}
__device__ __forceinline__ float rbf(float f) { return f; }
__device__ __forceinline__ float dot2(unsigned k, unsigned q, float acc) {
  return __builtin_amdgcn_fdot2_f32_bf16(__builtin_bit_cast(bf16x2, k), __builtin_bit_cast(bf16x2, q), acc, false);
}
template <int CTRL> __device__ __forceinline__ float dpp(float x) {
  return __builtin_bit_cast(float, __builtin_amdgcn_mov_dpp(__builtin_bit_cast(int, x), CTRL, 0xf, 0xf, true));
}
constexpr int XOR1 = 0xB1, XOR2 = 0x4E, XOR7 = 0x141, XOR8 = 0x128, ROR4 = 0x124;
__device__ __forceinline__ float xrow16_max(float x) {
  auto s = __builtin_amdgcn_permlane16_swap(__float_as_uint(x), __float_as_uint(x), false, false);
  x = fmaxf(__uint_as_float(s[0]), __uint_as_float(s[1]));
  auto t = __builtin_amdgcn_permlane32_swap(__float_as_uint(x), __float_as_uint(x), false, false);
  return fmaxf(__uint_as_float(t[0]), __uint_as_float(t[1]));
}
__device__ __forceinline__ float xrow16_sum(float x) {
  auto s = __builtin_amdgcn_permlane16_swap(__float_as_uint(x), __float_as_uint(x), false, false);
  x = __uint_as_float(s[0]) + __uint_as_float(s[1]);
  auto t = __builtin_amdgcn_permlane32_swap(__float_as_uint(x), __float_as_uint(x), false, false);
  return __uint_as_float(t[0]) + __uint_as_float(t[1]);
}
__device__ __forceinline__ float wave_sum(float x) {
#pragma unroll
  for (int o = 32; o >= 1; o >>= 1) x += __shfl_xor(x, o);
  return x;
}

constexpr size_t WS_MOD = 0;
constexpr size_t WS_Q = 32768;
constexpr size_t WS_O = 65536;
constexpr size_t WS_X1 = 131072;
constexpr size_t WS_H = 196608;
constexpr size_t WS_PML = 262144;
constexpr size_t WS_PO = 1048576;

template <int K>
__device__ __forceinline__ void gemv32(const float* a_lds, const bf16* W, int N, int n0, float* red  , float* res  ) {
  const int tid = threadIdx.x, lane = tid & 63, wave = tid >> 6;
  const int cl = lane & 3, rl = lane >> 2;
  float acc[2][8];
#pragma unroll
  for (int j = 0; j < 8; ++j) { acc[0][j] = 0.f; acc[1][j] = 0.f; }
  for (int k0 = wave * 16; k0 < K; k0 += 64) {
    const int k = k0 + rl;
    u32x4 w = *reinterpret_cast<const u32x4*>(W + (size_t)k * N + n0 + cl * 8);
    const float a0 = a_lds[k], a1 = a_lds[K + k];
#pragma unroll
    for (int j = 0; j < 4; ++j) {
      const float lo = bflo(w[j]), hi = bfhi(w[j]);
      acc[0][2 * j] = fmaf(a0, lo, acc[0][2 * j]); acc[0][2 * j + 1] = fmaf(a0, hi, acc[0][2 * j + 1]);
      acc[1][2 * j] = fmaf(a1, lo, acc[1][2 * j]); acc[1][2 * j + 1] = fmaf(a1, hi, acc[1][2 * j + 1]);
    }
  }
#pragma unroll
  for (int bb = 0; bb < 2; ++bb)
#pragma unroll
    for (int j = 0; j < 8; ++j) {
      float v = acc[bb][j];
      v += __shfl_xor(v, 4); v += __shfl_xor(v, 8); v += __shfl_xor(v, 16); v += __shfl_xor(v, 32);
      acc[bb][j] = v;
    }
  if (rl == 0) {
#pragma unroll
    for (int bb = 0; bb < 2; ++bb)
#pragma unroll
      for (int j = 0; j < 8; ++j) red[(wave * 2 + bb) * 32 + cl * 8 + j] = acc[bb][j];
  }
  __syncthreads();
  if (tid < 64) {
    const int bb = tid >> 5, c = tid & 31;
    res[bb * 32 + c] = red[(0 * 2 + bb) * 32 + c] + red[(1 * 2 + bb) * 32 + c] + red[(2 * 2 + bb) * 32 + c] + red[(3 * 2 + bb) * 32 + c];
  }
  __syncthreads();
}

__global__ void __launch_bounds__(256) k_mod(const bf16* c, const bf16* w_mod, float* mod) {
  __shared__ float a[2 * DM]; __shared__ float red[4 * 2 * 32]; __shared__ float res[64];
  for (int i = threadIdx.x; i < 2 * DM; i += 256) { float v = __bfloat162float(c[i]); a[i] = rbf(v / (1.f + __expf(-v))); }
  __syncthreads();
  gemv32<DM>(a, w_mod, 6 * DM, blockIdx.x * 32, red, res);
  if (threadIdx.x < 64) mod[(threadIdx.x >> 5) * 6 * DM + blockIdx.x * 32 + (threadIdx.x & 31)] = rbf(res[threadIdx.x]);
}

__device__ __forceinline__ void ln_mod(const float* x, float* h, const float* mod, int shift_idx, int scale_idx) {
  const int tid = threadIdx.x, lane = tid & 63, wave = tid >> 6;
  if (wave < 2) {
    const float* xr = x + wave * DM;
    float s = 0.f;
    for (int i = lane; i < DM; i += 64) s += xr[i];
    const float m = rbf(wave_sum(s) * (1.f / DM));
    float v = 0.f;
    const float mu = wave_sum(s) * (1.f / DM);
    for (int i = lane; i < DM; i += 64) { float d = xr[i] - mu; v += d * d; }
    v = rbf(wave_sum(v) * (1.f / DM));
    const float sd = rbf(sqrtf(rbf(v + 1e-5f)));
    for (int i = lane; i < DM; i += 64) {
      float t = rbf(rbf(xr[i] - m) / sd);
      t = rbf(t * rbf(1.f + mod[wave * 6 * DM + scale_idx * DM + i]));
      h[wave * DM + i] = rbf(t + mod[wave * 6 * DM + shift_idx * DM + i]);
    }
  }
  __syncthreads();
}

__global__ void __launch_bounds__(256) k_q(const bf16* x, const float* mod, const bf16* wq, bf16* q) {
  __shared__ float xs[2 * DM]; __shared__ float a[2 * DM]; __shared__ float red[4 * 2 * 32]; __shared__ float res[64];
  for (int i = threadIdx.x; i < 2 * DM; i += 256) xs[i] = __bfloat162float(x[i]);
  __syncthreads();
  ln_mod(xs, a, mod, 0, 1);
  gemv32<DM>(a, wq, NQ, blockIdx.x * 32, red, res);
  if (threadIdx.x < 64) q[(threadIdx.x >> 5) * NQ + blockIdx.x * 32 + (threadIdx.x & 31)] = __float2bfloat16(res[threadIdx.x]);
}

constexpr int MRG_REC = 4 + DH;
__global__ void __launch_bounds__(256) k_attn(const bf16* q, const bf16* kc, const bf16* vc, float* pml, float* po) {
  __shared__ __attribute__((aligned(16))) float lds[WAVES * 64 + WAVES * 4 * MRG_REC];
  const int s = blockIdx.x, kvh = blockIdx.y, b = blockIdx.z;
  const int tid = threadIdx.x, lane = tid & 63, wave = __builtin_amdgcn_readfirstlane(tid >> 6), r = lane >> 4, c = lane & 15;
  constexpr float C2 = 0.088388347648318440f * 1.4426950408889634f;
  u32x4 qpk[4];
#pragma unroll
  for (int g = 0; g < 4; ++g) qpk[g] = *reinterpret_cast<const u32x4*>(q + ((size_t)b * HQ + kvh * G + g) * DH + 8 * c);
  const size_t base = ((size_t)(b * HKV + kvh) * SK + (size_t)s * PPS * PAGE) * DH + (size_t)r * DH + 8 * c;
  const bf16* kb = kc + base; const bf16* vb = vc + base;
  u32x4 Kn[4], Vn[4];
#define ISSUE(i_) do { const size_t off_ = (size_t)(wave + 4 * (i_)) * PAGE * DH; \
    _Pragma("unroll") for (int t_ = 0; t_ < 4; ++t_) { Kn[t_] = __builtin_nontemporal_load(reinterpret_cast<const u32x4*>(kb + off_ + t_ * 4 * DH)); Vn[t_] = __builtin_nontemporal_load(reinterpret_cast<const u32x4*>(vb + off_ + t_ * 4 * DH)); } } while (0)
  float m = -INFINITY, l = 0.f, acc[32];
#pragma unroll
  for (int k = 0; k < 32; ++k) acc[k] = 0.f;
  ISSUE(0);
  for (int i = 0; i < PPW; ++i) {
    u32x4 Kc[4], Vc[4];
#pragma unroll
    for (int t = 0; t < 4; ++t) { Kc[t] = Kn[t]; Vc[t] = Vn[t]; }
    if (i + 1 < PPW) ISSUE(i + 1);
    float x[16];
#pragma unroll
    for (int t = 0; t < 4; ++t)
#pragma unroll
      for (int g = 0; g < 4; ++g) { float d = 0.f;
#pragma unroll
        for (int k = 0; k < 4; ++k) d = dot2(Kc[t][k], qpk[g][k], d);
        x[t * 4 + g] = d; }
#define TR_STEP(HALF, CTRL, BIT) _Pragma("unroll") for (int h = 0; h < (HALF); ++h) { \
      const float lo_ = x[h], hi_ = x[h + (HALF)]; const float t1_ = lo_ + dpp<CTRL>(lo_), t2_ = hi_ + dpp<CTRL>(hi_); \
      x[h] = (c & (BIT)) ? t2_ : t1_; }
    TR_STEP(8, XOR8, 8) TR_STEP(4, XOR7, 4) TR_STEP(2, XOR2, 2) TR_STEP(1, XOR1, 1)
#undef TR_STEP
    const float sc = x[0] * C2;
    float pm = sc; pm = fmaxf(pm, dpp<ROR4>(pm)); pm = fmaxf(pm, dpp<XOR8>(pm)); pm = xrow16_max(pm);
    const float mn = fmaxf(m, pm);
    const float alpha = __builtin_amdgcn_exp2f(m - mn), p = __builtin_amdgcn_exp2f(sc - mn);
    l = fmaf(l, alpha, p); m = mn;
#pragma unroll
    for (int q4 = 0; q4 < 4; ++q4) {
      const float al = q4 == 0 ? dpp<0x00>(alpha) : q4 == 1 ? dpp<0x55>(alpha) : q4 == 2 ? dpp<0xAA>(alpha) : dpp<0xFF>(alpha);
#pragma unroll
      for (int k = 0; k < 8; ++k) acc[q4 * 8 + k] *= al; }
    float* prow = lds + (wave * 4 + r) * 16;
    prow[c] = p;
    asm volatile("s_waitcnt lgkmcnt(0)" ::: "memory");
    f32x4 pr[4];
#pragma unroll
    for (int u = 0; u < 4; ++u) pr[u] = *reinterpret_cast<const f32x4*>(prow + 4 * u);
#pragma unroll
    for (int t = 0; t < 4; ++t) {
      float vf[8];
#pragma unroll
      for (int k = 0; k < 4; ++k) { vf[2 * k] = bflo(Vc[t][k]); vf[2 * k + 1] = bfhi(Vc[t][k]); }
#pragma unroll
      for (int g = 0; g < 4; ++g) { const float pg_ = pr[t][g];
#pragma unroll
        for (int k = 0; k < 8; ++k) acc[g * 8 + k] = fmaf(pg_, vf[k], acc[g * 8 + k]); } }
  }
#undef ISSUE
#pragma unroll
  for (int k = 0; k < 32; ++k) acc[k] = xrow16_sum(acc[k]);
  { float t = l; t += dpp<ROR4>(t); t += dpp<XOR8>(t); l = xrow16_sum(t); }
  float* mrg = lds + WAVES * 64;
  if (r == 0) {
#pragma unroll
    for (int g = 0; g < 4; ++g) {
      float* rec = mrg + (wave * 4 + g) * MRG_REC;
#pragma unroll
      for (int k = 0; k < 8; k += 4) *reinterpret_cast<f32x4*>(rec + 4 + 8 * c + k) = f32x4{acc[g * 8 + k], acc[g * 8 + k + 1], acc[g * 8 + k + 2], acc[g * 8 + k + 3]};
      if (c == g) { rec[0] = m; rec[1] = l; } } }
  __syncthreads();
  { const int g = wave, d0 = 2 * lane;
    const size_t row = (size_t)b * HQ + kvh * G + g;
    float M = -INFINITY;
#pragma unroll
    for (int w = 0; w < WAVES; ++w) M = fmaxf(M, mrg[(w * 4 + g) * MRG_REC]);
    float L = 0.f, o0 = 0.f, o1 = 0.f;
#pragma unroll
    for (int w = 0; w < WAVES; ++w) { const float* rec = mrg + (w * 4 + g) * MRG_REC; const float lw = rec[1];
      const float wt = __builtin_amdgcn_exp2f(rec[0] - M);
      L = fmaf(wt, lw, L); o0 = fmaf(wt, rec[4 + d0], o0); o1 = fmaf(wt, rec[4 + d0 + 1], o1); }
    const size_t slot = row * NSPLIT + s;
    *reinterpret_cast<float2*>(po + slot * DH + d0) = make_float2(o0, o1);
    if (lane == 0) *reinterpret_cast<float2*>(pml + 2 * slot) = make_float2(M, L);
  }
}

__global__ void __launch_bounds__(64) k_combine(const float* pml, const float* po, float* o) {
  const size_t row = blockIdx.x; const int d0 = 2 * threadIdx.x;
  const float* pm = pml + row * NSPLIT * 2; const float* pp = po + row * NSPLIT * DH;
  float M = -INFINITY;
  for (int s = 0; s < NSPLIT; ++s) M = fmaxf(M, pm[2 * s]);
  float L = 0.f, o0 = 0.f, o1 = 0.f;
  for (int s = 0; s < NSPLIT; ++s) { const float w = __builtin_amdgcn_exp2f(pm[2 * s] - M);
    const float2 a = *reinterpret_cast<const float2*>(pp + (size_t)s * DH + d0);
    L = fmaf(w, pm[2 * s + 1], L); o0 = fmaf(w, a.x, o0); o1 = fmaf(w, a.y, o1); }
  const float inv = 1.f / L;
  o[row * DH + d0] = rbf(o0 * inv); o[row * DH + d0 + 1] = rbf(o1 * inv);
}

__global__ void __launch_bounds__(256) k_wo(const float* o, const bf16* wo, const bf16* x, const float* mod, float* x1) {
  __shared__ float a[2 * NQ]; __shared__ float red[4 * 2 * 32]; __shared__ float res[64];
  for (int i = threadIdx.x; i < 2 * NQ; i += 256) a[i] = o[i];
  __syncthreads();
  gemv32<NQ>(a, wo, DM, blockIdx.x * 32, red, res);
  if (threadIdx.x < 64) { const int bb = threadIdx.x >> 5, n = blockIdx.x * 32 + (threadIdx.x & 31);
    x1[bb * DM + n] = rbf(__bfloat162float(x[bb * DM + n]) + rbf(mod[bb * 6 * DM + 2 * DM + n] * rbf(res[threadIdx.x]))); }
}

__global__ void __launch_bounds__(256) k_gu(const float* x1, const float* mod, const bf16* wg, const bf16* wu, float* hh) {
  __shared__ float xs[2 * DM]; __shared__ float a[2 * DM]; __shared__ float red[4 * 2 * 32]; __shared__ float res[64]; __shared__ float res2[64];
  for (int i = threadIdx.x; i < 2 * DM; i += 256) xs[i] = x1[i];
  __syncthreads();
  ln_mod(xs, a, mod, 3, 4);
  gemv32<DM>(a, wg, F, blockIdx.x * 32, red, res);
  gemv32<DM>(a, wu, F, blockIdx.x * 32, red, res2);
  if (threadIdx.x < 64) hh[(threadIdx.x >> 5) * F + blockIdx.x * 32 + (threadIdx.x & 31)] = rbf(rbf(res[threadIdx.x]) * rbf(res2[threadIdx.x]));
}

__global__ void __launch_bounds__(256) k_down(const float* hh, const bf16* wd, const float* x1, const float* mod, bf16* out) {
  __shared__ float a[2 * F]; __shared__ float red[4 * 2 * 32]; __shared__ float res[64];
  for (int i = threadIdx.x; i < 2 * F; i += 256) a[i] = hh[i];
  __syncthreads();
  gemv32<F>(a, wd, DM, blockIdx.x * 32, red, res);
  if (threadIdx.x < 64) { const int bb = threadIdx.x >> 5, n = blockIdx.x * 32 + (threadIdx.x & 31);
    out[bb * DM + n] = __float2bfloat16(x1[bb * DM + n] + rbf(mod[bb * 6 * DM + 5 * DM + n] * rbf(res[threadIdx.x]))); }
}

extern "C" void kernel_launch(void* const* d_in, const int* in_sizes, int n_in, void* d_out, int out_size, void* d_ws, size_t ws_size, hipStream_t stream) {
  const bf16* A_x = (const bf16*)d_in[0]; const bf16* kc = (const bf16*)d_in[1]; const bf16* vc = (const bf16*)d_in[2];
  const bf16* wq = (const bf16*)d_in[3]; const bf16* wo = (const bf16*)d_in[4];
  const bf16* wg = (const bf16*)d_in[6]; const bf16* wu = (const bf16*)d_in[7]; const bf16* wd = (const bf16*)d_in[8];
  const bf16* c = (const bf16*)d_in[9]; const bf16* w_mod = (const bf16*)d_in[10];
  char* ws = (char*)d_ws;
  float* mod = (float*)(ws + WS_MOD); bf16* q = (bf16*)(ws + WS_Q); float* o = (float*)(ws + WS_O); float* x1 = (float*)(ws + WS_X1);
  float* hh = (float*)(ws + WS_H); float* pml = (float*)(ws + WS_PML); float* po = (float*)(ws + WS_PO);
  k_mod<<<6 * DM / 32, 256, 0, stream>>>(c, w_mod, mod);
  k_q<<<NQ / 32, 256, 0, stream>>>(A_x, mod, wq, q);
  k_attn<<<dim3(NSPLIT, HKV, B), 256, 0, stream>>>(q, kc, vc, pml, po);
  k_combine<<<B * HQ, 64, 0, stream>>>(pml, po, o);
  k_wo<<<DM / 32, 256, 0, stream>>>(o, wo, A_x, mod, x1);
  k_gu<<<F / 32, 256, 0, stream>>>(x1, mod, wg, wu, hh);
  k_down<<<DM / 32, 256, 0, stream>>>(hh, wd, x1, mod, (bf16*)d_out);
}
```

```cpp
#include <hip/hip_runtime.h>
#include <hip/hip_bf16.h>
#include <cstdint>

using bf16 = __hip_bfloat16;
typedef unsigned long long u64;
constexpr int B = 2, DM = 512, HQ = 32, HKV = 8, G = 4, SK = 32768, DH = 128, F = 1792;
constexpr int NQ = HQ * DH;
constexpr int NWG = 256, NT = 512, NW = 8;
constexpr int NSPLIT = 16, PAGE = 16;
constexpr int PPS = SK / PAGE / NSPLIT;
constexpr int PPW = PPS / NW;
constexpr int NGU = F / 8;
constexpr unsigned SPIN_CAP = 1u << 22;

using u32x4 = __attribute__((ext_vector_type(4))) unsigned;
using f32x4 = __attribute__((ext_vector_type(4))) float;
using bf16x2 = __attribute__((ext_vector_type(2))) __bf16;

constexpr size_t WS_G1 = 0;
constexpr size_t WS_G2 = 32768;
constexpr size_t WS_G5 = 65536;
constexpr size_t WS_G3 = 1u << 20;
constexpr size_t WS_G4 = 3u << 20;
constexpr size_t WS_G6 = 5u << 20;
constexpr size_t WS_CLR = 7u << 20;
constexpr size_t WS_TS = 8u << 20;

__device__ __forceinline__ float bf2f(unsigned h) { return __builtin_bit_cast(float, h << 16); }
__device__ __forceinline__ float bflo(unsigned w) { return __builtin_bit_cast(float, w << 16); }
__device__ __forceinline__ float bfhi(unsigned w) { return __builtin_bit_cast(float, w & 0xffff0000u); }
__device__ __forceinline__ unsigned f2bf(float f) {
  unsigned u = __builtin_bit_cast(unsigned, f);
  if ((u & 0x7fffffffu) > 0x7f800000u) return ((u >> 16) | 0x40) & 0xffffu;
  u += 0x7fffu + ((u >> 16) & 1u);
  return u >> 16;
}
__device__ __forceinline__ float rbf(float f) { return bf2f(f2bf(f)); }
__device__ __forceinline__ float dot2(unsigned k, unsigned q, float acc) {
  return __builtin_amdgcn_fdot2_f32_bf16(__builtin_bit_cast(bf16x2, k), __builtin_bit_cast(bf16x2, q), acc, false);
}
template <int CTRL> __device__ __forceinline__ float dpp(float x) {
  return __builtin_bit_cast(float, __builtin_amdgcn_mov_dpp(__builtin_bit_cast(int, x), CTRL, 0xf, 0xf, true));
}
constexpr int XOR1 = 0xB1, XOR2 = 0x4E, XOR7 = 0x141, XOR8 = 0x128, ROR4 = 0x124;
__device__ __forceinline__ float xrow16_max(float x) {
  auto s = __builtin_amdgcn_permlane16_swap(__float_as_uint(x), __float_as_uint(x), false, false);
  x = fmaxf(__uint_as_float(s[0]), __uint_as_float(s[1]));
  auto t = __builtin_amdgcn_permlane32_swap(__float_as_uint(x), __float_as_uint(x), false, false);
  return fmaxf(__uint_as_float(t[0]), __uint_as_float(t[1]));
}
__device__ __forceinline__ float xrow16_sum(float x) {
  auto s = __builtin_amdgcn_permlane16_swap(__float_as_uint(x), __float_as_uint(x), false, false);
  x = __uint_as_float(s[0]) + __uint_as_float(s[1]);
  auto t = __builtin_amdgcn_permlane32_swap(__float_as_uint(x), __float_as_uint(x), false, false);
  return __uint_as_float(t[0]) + __uint_as_float(t[1]);
}
__device__ __forceinline__ float wave_sum(float x) {
#pragma unroll
  for (int o = 32; o >= 1; o >>= 1) x += __shfl_xor(x, o);
  return x;
}
__device__ __forceinline__ void bar() { asm volatile("s_waitcnt lgkmcnt(0)\n\ts_barrier" ::: "memory"); }
__device__ __forceinline__ void put(u64* p, unsigned tag, unsigned v) {
  __hip_atomic_store(p, ((u64)tag << 32) | v, __ATOMIC_RELAXED, __HIP_MEMORY_SCOPE_AGENT);
}
__device__ __forceinline__ unsigned poll1(const u64* p, unsigned tag) {
  u64 x;
  for (unsigned n = 0;; ++n) {
    x = __hip_atomic_load(p, __ATOMIC_RELAXED, __HIP_MEMORY_SCOPE_AGENT);
    if ((unsigned)(x >> 32) == tag || n > SPIN_CAP) break;
    __builtin_amdgcn_s_sleep(1);
  }
  return (unsigned)x;
}
__device__ __forceinline__ void poll2(const u64* p0, const u64* p1, unsigned tag, unsigned& v0, unsigned& v1) {
  u64 x, y;
  for (unsigned n = 0;; ++n) {
    x = __hip_atomic_load(p0, __ATOMIC_RELAXED, __HIP_MEMORY_SCOPE_AGENT);
    y = __hip_atomic_load(p1, __ATOMIC_RELAXED, __HIP_MEMORY_SCOPE_AGENT);
    if (((unsigned)(x >> 32) == tag && (unsigned)(y >> 32) == tag) || n > SPIN_CAP) break;
    __builtin_amdgcn_s_sleep(1);
  }
  v0 = (unsigned)x; v1 = (unsigned)y;
}

#define STAMP(i) do { } while (0)

constexpr int L_A = 0;
constexpr int L_X = 1024;
constexpr int L_RED = 2048;
constexpr int L_RES = 2304;
constexpr int L_Q = 2368;
constexpr int L_P = 2624;
constexpr int L_PART = 3136;
constexpr int L_WST = 3776;
constexpr int L_BIG = L_WST + 10240;
constexpr int L_TOTAL = L_BIG + 8192;

__device__ __forceinline__ void ln_mod(float* lds, int tid, float x0, float x1, unsigned sh, unsigned sc) {
  const int lane = tid & 63;
  lds[L_X + tid] = x0; lds[L_X + 512 + tid] = x1;
  bar();
  float hv[2];
#pragma unroll
  for (int bb = 0; bb < 2; ++bb) {
    float v[8], s = 0.f;
#pragma unroll
    for (int j = 0; j < 8; ++j) { v[j] = lds[L_X + bb * 512 + lane + 64 * j]; s += v[j]; }
    const float mu = wave_sum(s) * (1.f / DM);
    float q = 0.f;
#pragma unroll
    for (int j = 0; j < 8; ++j) { const float d = v[j] - mu; q += d * d; }
    const float var = rbf(wave_sum(q) * (1.f / DM));
    const float sd = rbf(sqrtf(rbf(var + 1e-5f)));
    const float xx = bb ? x1 : x0;
    float t = rbf(rbf(xx - rbf(mu)) / sd);
    const float scv = bb ? bfhi(sc) : bflo(sc), shv = bb ? bfhi(sh) : bflo(sh);
    t = rbf(t * rbf(1.f + scv));
    hv[bb] = rbf(t + shv);
  }
  lds[L_A + tid] = hv[0]; lds[L_A + 512 + tid] = hv[1];
  bar();
}

__device__ __forceinline__ unsigned gemv16(float* lds, int tid, const u32x4* wv) {
  const int lane = tid & 63, wave = tid >> 6;
  float acc[16];
#pragma unroll
  for (int j = 0; j < 16; ++j) acc[j] = 0.f;
#pragma unroll
  for (int i = 0; i < 2; ++i) {
    const int row = (tid >> 1) + 256 * i;
    const float a0 = lds[L_A + row], a1 = lds[L_A + 512 + row];
#pragma unroll
    for (int j = 0; j < 4; ++j) {
      const float lo = bflo(wv[i][j]), hi = bfhi(wv[i][j]);
      acc[2 * j] = fmaf(a0, lo, acc[2 * j]); acc[2 * j + 1] = fmaf(a0, hi, acc[2 * j + 1]);
      acc[8 + 2 * j] = fmaf(a1, lo, acc[8 + 2 * j]); acc[8 + 2 * j + 1] = fmaf(a1, hi, acc[8 + 2 * j + 1]);
    }
  }
#pragma unroll
  for (int j = 0; j < 16; ++j) {
    float v = acc[j];
    v += __shfl_xor(v, 2); v += __shfl_xor(v, 4); v += __shfl_xor(v, 8); v += __shfl_xor(v, 16); v += __shfl_xor(v, 32);
    acc[j] = v;
  }
  if (lane < 2) {
#pragma unroll
    for (int j = 0; j < 16; ++j) lds[L_RED + wave * 32 + lane * 16 + j] = acc[j];
  }
  bar();
  unsigned r = 0;
  if (tid < 16) {
    const int half = tid >> 3, j = tid & 7;
    float s0 = 0.f, s1 = 0.f;
#pragma unroll
    for (int ww = 0; ww < 8; ++ww) { s0 += lds[L_RED + ww * 32 + half * 16 + j]; s1 += lds[L_RED + ww * 32 + half * 16 + 8 + j]; }
    r = f2bf(s0) | (f2bf(s1) << 16);
  }
  return r;
}

__global__ void __launch_bounds__(NT, 2) mega(const bf16* __restrict__ x, const bf16* __restrict__ kc, const bf16* __restrict__ vc,
                                              const bf16* __restrict__ wq, const bf16* __restrict__ wo, const bf16* __restrict__ wg,
                                              const bf16* __restrict__ wu, const bf16* __restrict__ wd, const bf16* __restrict__ c,
                                              const bf16* __restrict__ w_mod, bf16* __restrict__ out, char* __restrict__ ws) {
  extern __shared__ __attribute__((aligned(16))) float lds[];
  const int tid = threadIdx.x, lane = tid & 63, wave = __builtin_amdgcn_readfirstlane(tid >> 6);
  const int w = blockIdx.x;
  const unsigned tag = 1u;
  u64* g1 = (u64*)(ws + WS_G1); u64* g2 = (u64*)(ws + WS_G2); u64* g3 = (u64*)(ws + WS_G3);
  u64* g4 = (u64*)(ws + WS_G4); u64* g5 = (u64*)(ws + WS_G5); u64* g6 = (u64*)(ws + WS_G6);
  u64* ts = (u64*)(ws + WS_TS); (void)ts;
  const int pair = w >> 4, b = pair >> 3, kvh = pair & 7, s = w & 15;
  const int r = lane >> 4, cc = lane & 15;
  STAMP(0);
  const unsigned short* cs = (const unsigned short*)c; const unsigned short* xs = (const unsigned short*)x;
  const float c0 = bf2f(cs[tid]), c1 = bf2f(cs[512 + tid]);
  const float x0 = bf2f(xs[tid]), x1 = bf2f(xs[512 + tid]);
  u32x4 wm[2], wqv[2];
  const bool has_mod = w < 192;
#pragma unroll
  for (int i = 0; i < 2; ++i) {
    const int row = (tid >> 1) + 256 * i;
    wm[i] = has_mod ? *reinterpret_cast<const u32x4*>(w_mod + (size_t)row * (6 * DM) + w * 16 + (tid & 1) * 8) : u32x4{0, 0, 0, 0};
    wqv[i] = *reinterpret_cast<const u32x4*>(wq + (size_t)row * NQ + w * 16 + (tid & 1) * 8);
  }
  const size_t base = ((size_t)(b * HKV + kvh) * SK + (size_t)s * PPS * PAGE) * DH + (size_t)r * DH + 8 * cc;
  const bf16* kb = kc + base; const bf16* vb = vc + base;
  u32x4 KA[4], VA[4], KB[4], VB[4];
#define ISSUE(KX, VX, i_) do { const size_t off_ = (size_t)(wave + NW * (i_)) * PAGE * DH; \
    _Pragma("unroll") for (int t_ = 0; t_ < 4; ++t_) { KX[t_] = __builtin_nontemporal_load(reinterpret_cast<const u32x4*>(kb + off_ + t_ * 4 * DH)); VX[t_] = __builtin_nontemporal_load(reinterpret_cast<const u32x4*>(vb + off_ + t_ * 4 * DH)); } } while (0)
  ISSUE(KA, VA, 0);
  ISSUE(KB, VB, 1);
  u32x4 wt[5];
  wt[0] = *reinterpret_cast<const u32x4*>(wo + (size_t)(16 * w + (tid >> 6)) * DM + 8 * lane);
  wt[1] = *reinterpret_cast<const u32x4*>(wo + (size_t)(16 * w + 8 + (tid >> 6)) * DM + 8 * lane);
  const bool has_gu = w < NGU;
  wt[2] = has_gu ? *reinterpret_cast<const u32x4*>(wg + (size_t)tid * F + 8 * w) : u32x4{0, 0, 0, 0};
  wt[3] = has_gu ? *reinterpret_cast<const u32x4*>(wu + (size_t)tid * F + 8 * w) : u32x4{0, 0, 0, 0};
  wt[4] = has_gu ? *reinterpret_cast<const u32x4*>(wd + (size_t)(8 * w + (tid >> 6)) * DM + 8 * lane) : u32x4{0, 0, 0, 0};
  lds[L_A + tid] = rbf(c0 / (1.f + __expf(-c0))); lds[L_A + 512 + tid] = rbf(c1 / (1.f + __expf(-c1)));
  bar();
  { const unsigned rr = gemv16(lds, tid, wm);
    if (has_mod && tid < 16) put(g1 + w * 16 + tid, tag, rr); }
  STAMP(1);
  { unsigned sh, sc; poll2(g1 + tid, g1 + 512 + tid, tag, sh, sc);
    STAMP(2);
    ln_mod(lds, tid, x0, x1, sh, sc); }
  { const unsigned rr = gemv16(lds, tid, wqv);
    if (tid < 16) put(g2 + w * 16 + tid, tag, rr); }
  STAMP(3);
#pragma unroll
  for (int i = 0; i < 5; ++i) *reinterpret_cast<u32x4*>(lds + L_WST + (i * 512 + tid) * 4) = wt[i];
  { const unsigned qq = poll1(g2 + kvh * 512 + tid, tag);
    ((unsigned short*)(lds + L_Q))[tid] = (unsigned short)(b ? (qq >> 16) : (qq & 0xffffu)); }
  bar();
  STAMP(4);
  u32x4 qpk[4];
#pragma unroll
  for (int g = 0; g < 4; ++g) qpk[g] = *reinterpret_cast<const u32x4*>((const unsigned short*)(lds + L_Q) + g * DH + 8 * cc);
  constexpr float C2 = 0.088388347648318440f * 1.4426950408889634f;
  float m = -INFINITY, l = 0.f, acc[32];
#pragma unroll
  for (int k = 0; k < 32; ++k) acc[k] = 0.f;
  float* prow = lds + L_P + (wave * 4 + r) * 16;
#define TR_STEP(HALF, CTRL, BIT) _Pragma("unroll") for (int h = 0; h < (HALF); ++h) { \
      const float lo_ = xx[h], hi_ = xx[h + (HALF)]; const float t1_ = lo_ + dpp<CTRL>(lo_), t2_ = hi_ + dpp<CTRL>(hi_); \
      xx[h] = (cc & (BIT)) ? t2_ : t1_; }
#define PROCESS(KX, VX) do { \
    float xx[16]; \
    _Pragma("unroll") for (int t = 0; t < 4; ++t) _Pragma("unroll") for (int g = 0; g < 4; ++g) { float d = 0.f; \
        _Pragma("unroll") for (int k = 0; k < 4; ++k) d = dot2(KX[t][k], qpk[g][k], d); xx[t * 4 + g] = d; } \
    TR_STEP(8, XOR8, 8) TR_STEP(4, XOR7, 4) TR_STEP(2, XOR2, 2) TR_STEP(1, XOR1, 1) \
    const float sc = xx[0] * C2; \
    float pm = sc; pm = fmaxf(pm, dpp<ROR4>(pm)); pm = fmaxf(pm, dpp<XOR8>(pm)); pm = xrow16_max(pm); \
    const float mn = fmaxf(m, pm); \
    const float alpha = __builtin_amdgcn_exp2f(m - mn), p = __builtin_amdgcn_exp2f(sc - mn); \
    l = fmaf(l, alpha, p); m = mn; \
    _Pragma("unroll") for (int q4 = 0; q4 < 4; ++q4) { \
      const float al = q4 == 0 ? dpp<0x00>(alpha) : q4 == 1 ? dpp<0x55>(alpha) : q4 == 2 ? dpp<0xAA>(alpha) : dpp<0xFF>(alpha); \
      _Pragma("unroll") for (int k = 0; k < 8; ++k) acc[q4 * 8 + k] *= al; } \
    prow[cc] = p; \
    asm volatile("s_waitcnt lgkmcnt(0)" ::: "memory"); \
    f32x4 pr[4]; \
    _Pragma("unroll") for (int u = 0; u < 4; ++u) pr[u] = *reinterpret_cast<const f32x4*>(prow + 4 * u); \
    _Pragma("unroll") for (int t = 0; t < 4; ++t) { \
      float vf[8]; \
      _Pragma("unroll") for (int k = 0; k < 4; ++k) { vf[2 * k] = bflo(VX[t][k]); vf[2 * k + 1] = bfhi(VX[t][k]); } \
      _Pragma("unroll") for (int g = 0; g < 4; ++g) { const float pg_ = pr[t][g]; \
        _Pragma("unroll") for (int k = 0; k < 8; ++k) acc[g * 8 + k] = fmaf(pg_, vf[k], acc[g * 8 + k]); } } \
  } while (0)
  for (int i = 0; i < PPW; i += 2) {
    PROCESS(KA, VA);
    if (i + 2 < PPW) ISSUE(KA, VA, i + 2);
    PROCESS(KB, VB);
    if (i + 3 < PPW) ISSUE(KB, VB, i + 3);
  }
#undef PROCESS
#undef TR_STEP
#undef ISSUE
  STAMP(5);
#pragma unroll
  for (int k = 0; k < 32; ++k) acc[k] = xrow16_sum(acc[k]);
  { float t = l; t += dpp<ROR4>(t); t += dpp<XOR8>(t); l = xrow16_sum(t); }
  float* mrg = lds + L_BIG;
  if (r == 0) {
#pragma unroll
    for (int g = 0; g < 4; ++g) {
      float* rec = mrg + (wave * 4 + g) * 132;
#pragma unroll
      for (int k = 0; k < 8; k += 4) *reinterpret_cast<f32x4*>(rec + 4 + 8 * cc + k) = f32x4{acc[g * 8 + k], acc[g * 8 + k + 1], acc[g * 8 + k + 2], acc[g * 8 + k + 3]};
      if (cc == g) { rec[0] = m; rec[1] = l; } } }
  bar();
  { const int g = tid >> 7, d = tid & 127;
    float M = -INFINITY;
#pragma unroll
    for (int ww = 0; ww < NW; ++ww) M = fmaxf(M, mrg[(ww * 4 + g) * 132]);
    float L = 0.f, o0 = 0.f;
#pragma unroll
    for (int ww = 0; ww < NW; ++ww) { const float* rec = mrg + (ww * 4 + g) * 132;
      const float wgt = __builtin_amdgcn_exp2f(rec[0] - M);
      L = fmaf(wgt, rec[1], L); o0 = fmaf(wgt, rec[4 + d], o0); }
    u64* dst = g3 + (size_t)((kvh * 4 + g) * 8 + (d >> 4)) * 576 + (b * 16 + s) * 18;
    put(dst + 2 + (d & 15), tag, __float_as_uint(o0));
    if ((d & 15) == 0) { put(dst, tag, __float_as_uint(M)); put(dst + 1, tag, __float_as_uint(L)); }
  }
  STAMP(6);
  { const unsigned v0 = poll1(g3 + (size_t)w * 576 + tid, tag);
    lds[L_PART + tid] = __uint_as_float(v0);
    if (tid < 64) lds[L_PART + 512 + tid] = __uint_as_float(poll1(g3 + (size_t)w * 576 + 512 + tid, tag)); }
  bar();
  STAMP(7);
  if (tid < 32) {
    const int bb = tid >> 4, d = tid & 15;
    const float* pp = lds + L_PART + bb * 16 * 18;
    float M = -INFINITY;
#pragma unroll
    for (int ss = 0; ss < 16; ++ss) M = fmaxf(M, pp[ss * 18]);
    float L = 0.f, o0 = 0.f;
#pragma unroll
    for (int ss = 0; ss < 16; ++ss) { const float wgt = __builtin_amdgcn_exp2f(pp[ss * 18] - M);
      L = fmaf(wgt, pp[ss * 18 + 1], L); o0 = fmaf(wgt, pp[ss * 18 + 2 + d], o0); }
    lds[L_RES + tid] = rbf(o0 / L);
  }
  bar();
  float* red2 = lds + L_BIG;
  { const u32x4 w0 = *reinterpret_cast<const u32x4*>(lds + L_WST + (0 * 512 + tid) * 4), w1 = *reinterpret_cast<const u32x4*>(lds + L_WST + (1 * 512 + tid) * 4);
#pragma unroll
    for (int bb = 0; bb < 2; ++bb) {
      const float a0 = lds[L_RES + bb * 16 + wave], a1 = lds[L_RES + bb * 16 + 8 + wave];
      float v[8];
#pragma unroll
      for (int j = 0; j < 4; ++j) { v[2 * j] = fmaf(a1, bflo(w1[j]), a0 * bflo(w0[j])); v[2 * j + 1] = fmaf(a1, bfhi(w1[j]), a0 * bfhi(w0[j])); }
      *reinterpret_cast<f32x4*>(red2 + (wave * 2 + bb) * 512 + 8 * lane) = f32x4{v[0], v[1], v[2], v[3]};
      *reinterpret_cast<f32x4*>(red2 + (wave * 2 + bb) * 512 + 8 * lane + 4) = f32x4{v[4], v[5], v[6], v[7]};
    } }
  bar();
  { float s0 = 0.f, s1 = 0.f;
#pragma unroll
    for (int ww = 0; ww < NW; ++ww) { s0 += red2[(ww * 2 + 0) * 512 + tid]; s1 += red2[(ww * 2 + 1) * 512 + tid]; }
    u64* dst = g4 + (size_t)(tid >> 1) * 1024 + w * 4 + (tid & 1) * 2;
    put(dst, tag, __float_as_uint(s0)); put(dst + 1, tag, __float_as_uint(s1)); }
  STAMP(8);
  float x1own0 = 0.f, x1own1 = 0.f;
  { unsigned v0, v1; poll2(g4 + (size_t)w * 1024 + 2 * tid, g4 + (size_t)w * 1024 + 2 * tid + 1, tag, v0, v1);
    float s0 = __uint_as_float(v0), s1 = __uint_as_float(v1);
#pragma unroll
    for (int o = 2; o <= 32; o <<= 1) { s0 += __shfl_xor(s0, o); s1 += __shfl_xor(s1, o); }
    if (lane < 2) { lds[L_RED + wave * 4 + lane * 2] = s0; lds[L_RED + wave * 4 + lane * 2 + 1] = s1; }
    bar();
    STAMP(9);
    if (tid < 2) {
      float a0 = 0.f, a1 = 0.f;
#pragma unroll
      for (int ww = 0; ww < NW; ++ww) { a0 += lds[L_RED + ww * 4 + tid * 2]; a1 += lds[L_RED + ww * 4 + tid * 2 + 1]; }
      const int n = 2 * w + tid;
      const unsigned ga = poll1(g1 + 1024 + n, tag);
      x1own0 = rbf(bf2f(xs[n]) + rbf(bflo(ga) * rbf(a0)));
      x1own1 = rbf(bf2f(xs[512 + n]) + rbf(bfhi(ga) * rbf(a1)));
      put(g5 + n, tag, f2bf(x1own0) | (f2bf(x1own1) << 16));
    } }
  { unsigned sh, sc; poll2(g1 + 1536 + tid, g1 + 2048 + tid, tag, sh, sc);
    const unsigned xv = poll1(g5 + tid, tag);
    STAMP(10);
    bar();
    ln_mod(lds, tid, bflo(xv), bfhi(xv), sh, sc); }
  if (has_gu) {
    const u32x4 wgv = *reinterpret_cast<const u32x4*>(lds + L_WST + (2 * 512 + tid) * 4), wuv = *reinterpret_cast<const u32x4*>(lds + L_WST + (3 * 512 + tid) * 4);
    const float a0 = lds[L_A + tid], a1 = lds[L_A + 512 + tid];
    float v[32];
#pragma unroll
    for (int j = 0; j < 4; ++j) {
      v[2 * j] = a0 * bflo(wgv[j]); v[2 * j + 1] = a0 * bfhi(wgv[j]); v[8 + 2 * j] = a0 * bflo(wuv[j]); v[8 + 2 * j + 1] = a0 * bfhi(wuv[j]);
      v[16 + 2 * j] = a1 * bflo(wgv[j]); v[16 + 2 * j + 1] = a1 * bfhi(wgv[j]); v[24 + 2 * j] = a1 * bflo(wuv[j]); v[24 + 2 * j + 1] = a1 * bfhi(wuv[j]);
    }
#define RS(HALF, MASK) _Pragma("unroll") for (int h = 0; h < (HALF); ++h) { const bool up = (lane & (MASK)) != 0; \
      const float send = up ? v[h] : v[h + (HALF)], keep = up ? v[h + (HALF)] : v[h]; v[h] = keep + __shfl_xor(send, (MASK)); }
    RS(16, 32) RS(8, 16) RS(4, 8) RS(2, 4) RS(1, 2)
#undef RS
    v[0] += __shfl_xor(v[0], 1);
    if ((lane & 1) == 0) lds[L_RED + wave * 32 + (lane >> 1)] = v[0];
    bar();
    if (tid < 16) {
      const int bb = tid >> 3, j = tid & 7;
      float gs = 0.f, us = 0.f;
#pragma unroll
      for (int ww = 0; ww < NW; ++ww) { gs += lds[L_RED + ww * 32 + bb * 16 + j]; us += lds[L_RED + ww * 32 + bb * 16 + 8 + j]; }
      lds[L_RES + tid] = rbf(rbf(gs) * rbf(us));
    }
    bar();
    { const u32x4 w0 = *reinterpret_cast<const u32x4*>(lds + L_WST + (4 * 512 + tid) * 4);
#pragma unroll
      for (int bb = 0; bb < 2; ++bb) {
        const float a = lds[L_RES + bb * 8 + wave];
        *reinterpret_cast<f32x4*>(red2 + (wave * 2 + bb) * 512 + 8 * lane) = f32x4{a * bflo(w0[0]), a * bfhi(w0[0]), a * bflo(w0[1]), a * bfhi(w0[1])};
        *reinterpret_cast<f32x4*>(red2 + (wave * 2 + bb) * 512 + 8 * lane + 4) = f32x4{a * bflo(w0[2]), a * bfhi(w0[2]), a * bflo(w0[3]), a * bfhi(w0[3])};
      } }
    bar();
    { float s0 = 0.f, s1 = 0.f;
#pragma unroll
      for (int ww = 0; ww < NW; ++ww) { s0 += red2[(ww * 2 + 0) * 512 + tid]; s1 += red2[(ww * 2 + 1) * 512 + tid]; }
      u64* dst = g6 + (size_t)(tid >> 1) * 1024 + w * 4 + (tid & 1) * 2;
      put(dst, tag, __float_as_uint(s0)); put(dst + 1, tag, __float_as_uint(s1)); }
  }
  STAMP(11);
  { float s0 = 0.f, s1 = 0.f;
    if (tid < 2 * NGU) { unsigned v0, v1; poll2(g6 + (size_t)w * 1024 + 2 * tid, g6 + (size_t)w * 1024 + 2 * tid + 1, tag, v0, v1);
      s0 = __uint_as_float(v0); s1 = __uint_as_float(v1); }
#pragma unroll
    for (int o = 2; o <= 32; o <<= 1) { s0 += __shfl_xor(s0, o); s1 += __shfl_xor(s1, o); }
    bar();
    if (lane < 2) { lds[L_RED + wave * 4 + lane * 2] = s0; lds[L_RED + wave * 4 + lane * 2 + 1] = s1; }
    bar();
    if (tid < 2) {
      float a0 = 0.f, a1 = 0.f;
#pragma unroll
      for (int ww = 0; ww < NW; ++ww) { a0 += lds[L_RED + ww * 4 + tid * 2]; a1 += lds[L_RED + ww * 4 + tid * 2 + 1]; }
      const int n = 2 * w + tid;
      const unsigned gb = poll1(g1 + 2560 + n, tag);
      out[n] = __float2bfloat16(x1own0 + rbf(bflo(gb) * rbf(a0)));
      out[512 + n] = __float2bfloat16(x1own1 + rbf(bfhi(gb) * rbf(a1)));
    } }
  STAMP(12);
}

extern "C" void kernel_launch(void* const* d_in, const int* in_sizes, int n_in, void* d_out, int out_size, void* d_ws, size_t ws_size, hipStream_t stream) {
  (void)hipFuncSetAttribute((const void*)mega, hipFuncAttributeMaxDynamicSharedMemorySize, L_TOTAL * 4);
  hipMemsetAsync(d_ws, 0, WS_CLR, stream);
  mega<<<NWG, NT, L_TOTAL * 4, stream>>>((const bf16*)d_in[0], (const bf16*)d_in[1], (const bf16*)d_in[2], (const bf16*)d_in[3], (const bf16*)d_in[4],
                                        (const bf16*)d_in[6], (const bf16*)d_in[7], (const bf16*)d_in[8], (const bf16*)d_in[9], (const bf16*)d_in[10],
                                        (bf16*)d_out, (char*)d_ws);
}
```

```cpp
#include <hip/hip_runtime.h>
#include <hip/hip_bf16.h>
#include <cstdint>

using bf16 = __hip_bfloat16;
typedef unsigned long long u64;
constexpr int B = 2, DM = 512, HQ = 32, HKV = 8, G = 4, SK = 32768, DH = 128, F = 1792;
constexpr int NQ = HQ * DH;
constexpr int NWG = 256, NT = 512, NW = 8;
constexpr int NSPLIT = 16, PAGE = 16;
constexpr int PPS = SK / PAGE / NSPLIT;
constexpr int PPW = PPS / NW;
constexpr int NGU = F / 8;
constexpr unsigned SPIN_CAP = 1u << 22;

using u32x4 = __attribute__((ext_vector_type(4))) unsigned;
using f32x4 = __attribute__((ext_vector_type(4))) float;
using bf16x2 = __attribute__((ext_vector_type(2))) __bf16;

constexpr size_t WS_G1 = 0;
constexpr size_t WS_G2 = 32768;
constexpr size_t WS_G5 = 65536;
constexpr size_t WS_G3 = 1u << 20;
constexpr size_t WS_G4 = 3u << 20;
constexpr size_t WS_G6 = 5u << 20;
constexpr size_t WS_EPOCH = 7u << 20;
constexpr size_t WS_TS = 8u << 20;

__device__ __forceinline__ float bf2f(unsigned h) { return __builtin_bit_cast(float, h << 16); }
__device__ __forceinline__ float bflo(unsigned w) { return __builtin_bit_cast(float, w << 16); }
__device__ __forceinline__ float bfhi(unsigned w) { return __builtin_bit_cast(float, w & 0xffff0000u); }
__device__ __forceinline__ unsigned f2bf(float f) {
  unsigned u = __builtin_bit_cast(unsigned, f);
  if ((u & 0x7fffffffu) > 0x7f800000u) return ((u >> 16) | 0x40) & 0xffffu;
  u += 0x7fffu + ((u >> 16) & 1u);
  return u >> 16;
}
__device__ __forceinline__ float rbf(float f) { return bf2f(f2bf(f)); }
__device__ __forceinline__ float dot2(unsigned k, unsigned q, float acc) {
  return __builtin_amdgcn_fdot2_f32_bf16(__builtin_bit_cast(bf16x2, k), __builtin_bit_cast(bf16x2, q), acc, false);
}
template <int CTRL> __device__ __forceinline__ float dpp(float x) {
  return __builtin_bit_cast(float, __builtin_amdgcn_mov_dpp(__builtin_bit_cast(int, x), CTRL, 0xf, 0xf, true));
}
constexpr int XOR1 = 0xB1, XOR2 = 0x4E, XOR7 = 0x141, XOR8 = 0x128, ROR4 = 0x124;
__device__ __forceinline__ float xrow16_max(float x) {
  auto s = __builtin_amdgcn_permlane16_swap(__float_as_uint(x), __float_as_uint(x), false, false);
  x = fmaxf(__uint_as_float(s[0]), __uint_as_float(s[1]));
  auto t = __builtin_amdgcn_permlane32_swap(__float_as_uint(x), __float_as_uint(x), false, false);
  return fmaxf(__uint_as_float(t[0]), __uint_as_float(t[1]));
}
__device__ __forceinline__ float xrow16_sum(float x) {
  auto s = __builtin_amdgcn_permlane16_swap(__float_as_uint(x), __float_as_uint(x), false, false);
  x = __uint_as_float(s[0]) + __uint_as_float(s[1]);
  auto t = __builtin_amdgcn_permlane32_swap(__float_as_uint(x), __float_as_uint(x), false, false);
  return __uint_as_float(t[0]) + __uint_as_float(t[1]);
}
__device__ __forceinline__ float wave_sum(float x) {
  x += dpp<XOR1>(x); x += dpp<XOR2>(x); x += dpp<XOR7>(x); x += dpp<XOR8>(x);
  return xrow16_sum(x);
}
__device__ __forceinline__ float par_sum(float x) {
  x += dpp<XOR2>(x); x += dpp<ROR4>(x); x += dpp<XOR8>(x);
  return xrow16_sum(x);
}
__device__ __forceinline__ void bar() { asm volatile("s_waitcnt lgkmcnt(0)\n\ts_barrier" ::: "memory"); }
__device__ __forceinline__ void put(u64* p, unsigned tag, unsigned v) {
  __hip_atomic_store(p, ((u64)tag << 32) | v, __ATOMIC_RELAXED, __HIP_MEMORY_SCOPE_AGENT);
}
__device__ __forceinline__ unsigned poll1(const u64* p, unsigned tag) {
  u64 x;
  for (unsigned n = 0;; ++n) {
    x = __hip_atomic_load(p, __ATOMIC_RELAXED, __HIP_MEMORY_SCOPE_AGENT);
    if ((unsigned)(x >> 32) == tag || n > SPIN_CAP) break;
    __builtin_amdgcn_s_sleep(1);
  }
  return (unsigned)x;
}
__device__ __forceinline__ void poll2(const u64* p0, const u64* p1, unsigned tag, unsigned& v0, unsigned& v1) {
  u64 x, y;
  for (unsigned n = 0;; ++n) {
    x = __hip_atomic_load(p0, __ATOMIC_RELAXED, __HIP_MEMORY_SCOPE_AGENT);
    y = __hip_atomic_load(p1, __ATOMIC_RELAXED, __HIP_MEMORY_SCOPE_AGENT);
    if (((unsigned)(x >> 32) == tag && (unsigned)(y >> 32) == tag) || n > SPIN_CAP) break;
    __builtin_amdgcn_s_sleep(1);
  }
  v0 = (unsigned)x; v1 = (unsigned)y;
}

#define STAMP(i) do { } while (0)

constexpr int L_A = 0;
constexpr int L_X = 1024;
constexpr int L_RED = 2048;
constexpr int L_RES = 2304;
constexpr int L_Q = 2368;
constexpr int L_P = 2624;
constexpr int L_PART = 3136;
constexpr int L_WST = 3776;
constexpr int L_BIG = L_WST + 10240;
constexpr int L_TOTAL = L_BIG + 8192;

__device__ __forceinline__ void ln_mod(float* lds, int tid, float x0, float x1, unsigned sh, unsigned sc) {
  const int lane = tid & 63;
  lds[L_X + tid] = x0; lds[L_X + 512 + tid] = x1;
  bar();
  float hv[2];
#pragma unroll
  for (int bb = 0; bb < 2; ++bb) {
    float v[8], s = 0.f;
#pragma unroll
    for (int j = 0; j < 8; ++j) { v[j] = lds[L_X + bb * 512 + lane + 64 * j]; s += v[j]; }
    const float mu = wave_sum(s) * (1.f / DM);
    float q = 0.f;
#pragma unroll
    for (int j = 0; j < 8; ++j) { const float d = v[j] - mu; q += d * d; }
    const float var = rbf(wave_sum(q) * (1.f / DM));
    const float sd = rbf(sqrtf(rbf(var + 1e-5f)));
    const float xx = bb ? x1 : x0;
    float t = rbf(rbf(xx - rbf(mu)) / sd);
    const float scv = bb ? bfhi(sc) : bflo(sc), shv = bb ? bfhi(sh) : bflo(sh);
    t = rbf(t * rbf(1.f + scv));
    hv[bb] = rbf(t + shv);
  }
  lds[L_A + tid] = hv[0]; lds[L_A + 512 + tid] = hv[1];
  bar();
}

__device__ __forceinline__ unsigned gemv16(float* lds, int tid, const u32x4* wv) {
  const int lane = tid & 63, wave = tid >> 6;
  float acc[16];
#pragma unroll
  for (int j = 0; j < 16; ++j) acc[j] = 0.f;
#pragma unroll
  for (int i = 0; i < 2; ++i) {
    const int row = (tid >> 1) + 256 * i;
    const float a0 = lds[L_A + row], a1 = lds[L_A + 512 + row];
#pragma unroll
    for (int j = 0; j < 4; ++j) {
      const float lo = bflo(wv[i][j]), hi = bfhi(wv[i][j]);
      acc[2 * j] = fmaf(a0, lo, acc[2 * j]); acc[2 * j + 1] = fmaf(a0, hi, acc[2 * j + 1]);
      acc[8 + 2 * j] = fmaf(a1, lo, acc[8 + 2 * j]); acc[8 + 2 * j + 1] = fmaf(a1, hi, acc[8 + 2 * j + 1]);
    }
  }
#pragma unroll
  for (int j = 0; j < 16; ++j) {
    float v = acc[j];
    acc[j] = par_sum(v);
  }
  if (lane < 2) {
#pragma unroll
    for (int j = 0; j < 16; ++j) lds[L_RED + wave * 32 + lane * 16 + j] = acc[j];
  }
  bar();
  unsigned r = 0;
  if (tid < 16) {
    const int half = tid >> 3, j = tid & 7;
    float s0 = 0.f, s1 = 0.f;
#pragma unroll
    for (int ww = 0; ww < 8; ++ww) { s0 += lds[L_RED + ww * 32 + half * 16 + j]; s1 += lds[L_RED + ww * 32 + half * 16 + 8 + j]; }
    r = f2bf(s0) | (f2bf(s1) << 16);
  }
  return r;
}

__global__ void __launch_bounds__(NT, 2) mega(const bf16* __restrict__ x, const bf16* __restrict__ kc, const bf16* __restrict__ vc,
                                              const bf16* __restrict__ wq, const bf16* __restrict__ wo, const bf16* __restrict__ wg,
                                              const bf16* __restrict__ wu, const bf16* __restrict__ wd, const bf16* __restrict__ c,
                                              const bf16* __restrict__ w_mod, bf16* __restrict__ out, char* __restrict__ ws) {
  extern __shared__ __attribute__((aligned(16))) float lds[];
  const int tid = threadIdx.x, lane = tid & 63, wave = __builtin_amdgcn_readfirstlane(tid >> 6);
  const int w = blockIdx.x;
  const unsigned tag = __hip_atomic_load((const unsigned*)(ws + WS_EPOCH), __ATOMIC_RELAXED, __HIP_MEMORY_SCOPE_AGENT) + 1u;
  u64* g1 = (u64*)(ws + WS_G1); u64* g2 = (u64*)(ws + WS_G2); u64* g3 = (u64*)(ws + WS_G3);
  u64* g4 = (u64*)(ws + WS_G4); u64* g5 = (u64*)(ws + WS_G5); u64* g6 = (u64*)(ws + WS_G6);
  u64* ts = (u64*)(ws + WS_TS); (void)ts;
  const int pair = w >> 4, b = pair >> 3, kvh = pair & 7, s = w & 15;
  const int r = lane >> 4, cc = lane & 15;
  STAMP(0);
  const unsigned short* cs = (const unsigned short*)c; const unsigned short* xs = (const unsigned short*)x;
  const float c0 = bf2f(cs[tid]), c1 = bf2f(cs[512 + tid]);
  const float x0 = bf2f(xs[tid]), x1 = bf2f(xs[512 + tid]);
  u32x4 wm[2], wqv[2];
  const bool has_mod = w < 192;
#pragma unroll
  for (int i = 0; i < 2; ++i) {
    const int row = (tid >> 1) + 256 * i;
    wm[i] = *reinterpret_cast<const u32x4*>(w_mod + (size_t)row * (6 * DM) + (has_mod ? w : 0) * 16 + (tid & 1) * 8);
    wqv[i] = *reinterpret_cast<const u32x4*>(wq + (size_t)row * NQ + w * 16 + (tid & 1) * 8);
  }
  const size_t base = ((size_t)(b * HKV + kvh) * SK + (size_t)s * PPS * PAGE) * DH + (size_t)r * DH + 8 * cc;
  const bf16* kb = kc + base; const bf16* vb = vc + base;
  u32x4 KA[4], VA[4], KB[4], VB[4];
#define ISSUE(KX, VX, i_) do { const size_t off_ = (size_t)(wave + NW * (i_)) * PAGE * DH; \
    _Pragma("unroll") for (int t_ = 0; t_ < 4; ++t_) { KX[t_] = __builtin_nontemporal_load(reinterpret_cast<const u32x4*>(kb + off_ + t_ * 4 * DH)); VX[t_] = __builtin_nontemporal_load(reinterpret_cast<const u32x4*>(vb + off_ + t_ * 4 * DH)); } } while (0)
  ISSUE(KA, VA, 0);
  ISSUE(KB, VB, 1);
  u32x4 wt[5];
  wt[0] = *reinterpret_cast<const u32x4*>(wo + (size_t)(16 * w + (tid >> 6)) * DM + 8 * lane);
  wt[1] = *reinterpret_cast<const u32x4*>(wo + (size_t)(16 * w + 8 + (tid >> 6)) * DM + 8 * lane);
  const bool has_gu = w < NGU;
  const int wgu = has_gu ? w : 0;
  wt[2] = *reinterpret_cast<const u32x4*>(wg + (size_t)tid * F + 8 * wgu);
  wt[3] = *reinterpret_cast<const u32x4*>(wu + (size_t)tid * F + 8 * wgu);
  wt[4] = *reinterpret_cast<const u32x4*>(wd + (size_t)(8 * wgu + (tid >> 6)) * DM + 8 * lane);
  lds[L_A + tid] = rbf(c0 / (1.f + __expf(-c0))); lds[L_A + 512 + tid] = rbf(c1 / (1.f + __expf(-c1)));
  bar();
  { const unsigned rr = gemv16(lds, tid, wm);
    if (has_mod && tid < 16) put(g1 + w * 16 + tid, tag, rr); }
  STAMP(1);
  { unsigned sh, sc; poll2(g1 + tid, g1 + 512 + tid, tag, sh, sc);
    STAMP(2);
    ln_mod(lds, tid, x0, x1, sh, sc); }
  { const unsigned rr = gemv16(lds, tid, wqv);
    if (tid < 16) put(g2 + w * 16 + tid, tag, rr); }
  STAMP(3);
#pragma unroll
  for (int i = 0; i < 5; ++i) *reinterpret_cast<u32x4*>(lds + L_WST + (i * 512 + tid) * 4) = wt[i];
  { const unsigned qq = poll1(g2 + kvh * 512 + tid, tag);
    ((unsigned short*)(lds + L_Q))[tid] = (unsigned short)(b ? (qq >> 16) : (qq & 0xffffu)); }
  bar();
  STAMP(4);
  u32x4 qpk[4];
#pragma unroll
  for (int g = 0; g < 4; ++g) qpk[g] = *reinterpret_cast<const u32x4*>((const unsigned short*)(lds + L_Q) + g * DH + 8 * cc);
  constexpr float C2 = 0.088388347648318440f * 1.4426950408889634f;
  float m = -INFINITY, l = 0.f, acc[32];
#pragma unroll
  for (int k = 0; k < 32; ++k) acc[k] = 0.f;
  float* prow = lds + L_P + (wave * 4 + r) * 16;
#define TR_STEP(HALF, CTRL, BIT) _Pragma("unroll") for (int h = 0; h < (HALF); ++h) { \
      const float lo_ = xx[h], hi_ = xx[h + (HALF)]; const float t1_ = lo_ + dpp<CTRL>(lo_), t2_ = hi_ + dpp<CTRL>(hi_); \
      xx[h] = (cc & (BIT)) ? t2_ : t1_; }
#define PROCESS(KX, VX) do { \
    float xx[16]; \
    _Pragma("unroll") for (int t = 0; t < 4; ++t) _Pragma("unroll") for (int g = 0; g < 4; ++g) { float d = 0.f; \
        _Pragma("unroll") for (int k = 0; k < 4; ++k) d = dot2(KX[t][k], qpk[g][k], d); xx[t * 4 + g] = d; } \
    TR_STEP(8, XOR8, 8) TR_STEP(4, XOR7, 4) TR_STEP(2, XOR2, 2) TR_STEP(1, XOR1, 1) \
    const float sc = xx[0] * C2; \
    float pm = sc; pm = fmaxf(pm, dpp<ROR4>(pm)); pm = fmaxf(pm, dpp<XOR8>(pm)); pm = xrow16_max(pm); \
    const float mn = fmaxf(m, pm); \
    const float alpha = __builtin_amdgcn_exp2f(m - mn), p = __builtin_amdgcn_exp2f(sc - mn); \
    l = fmaf(l, alpha, p); m = mn; \
    _Pragma("unroll") for (int q4 = 0; q4 < 4; ++q4) { \
      const float al = q4 == 0 ? dpp<0x00>(alpha) : q4 == 1 ? dpp<0x55>(alpha) : q4 == 2 ? dpp<0xAA>(alpha) : dpp<0xFF>(alpha); \
      _Pragma("unroll") for (int k = 0; k < 8; ++k) acc[q4 * 8 + k] *= al; } \
    prow[cc] = p; \
    asm volatile("s_waitcnt lgkmcnt(0)" ::: "memory"); \
    f32x4 pr[4]; \
    _Pragma("unroll") for (int u = 0; u < 4; ++u) pr[u] = *reinterpret_cast<const f32x4*>(prow + 4 * u); \
    _Pragma("unroll") for (int t = 0; t < 4; ++t) { \
      float vf[8]; \
      _Pragma("unroll") for (int k = 0; k < 4; ++k) { vf[2 * k] = bflo(VX[t][k]); vf[2 * k + 1] = bfhi(VX[t][k]); } \
      _Pragma("unroll") for (int g = 0; g < 4; ++g) { const float pg_ = pr[t][g]; \
        _Pragma("unroll") for (int k = 0; k < 8; ++k) acc[g * 8 + k] = fmaf(pg_, vf[k], acc[g * 8 + k]); } } \
  } while (0)
  for (int i = 0; i < PPW; i += 2) {
    PROCESS(KA, VA);
    if (i + 2 < PPW) ISSUE(KA, VA, i + 2);
    PROCESS(KB, VB);
    if (i + 3 < PPW) ISSUE(KB, VB, i + 3);
  }
#undef PROCESS
#undef TR_STEP
#undef ISSUE
  STAMP(5);
#pragma unroll
  for (int k = 0; k < 32; ++k) acc[k] = xrow16_sum(acc[k]);
  { float t = l; t += dpp<ROR4>(t); t += dpp<XOR8>(t); l = xrow16_sum(t); }
  float* mrg = lds + L_BIG;
  if (r == 0) {
#pragma unroll
    for (int g = 0; g < 4; ++g) {
      float* rec = mrg + (wave * 4 + g) * 132;
#pragma unroll
      for (int k = 0; k < 8; k += 4) *reinterpret_cast<f32x4*>(rec + 4 + 8 * cc + k) = f32x4{acc[g * 8 + k], acc[g * 8 + k + 1], acc[g * 8 + k + 2], acc[g * 8 + k + 3]};
      if (cc == g) { rec[0] = m; rec[1] = l; } } }
  bar();
  { const int g = tid >> 7, d = tid & 127;
    float M = -INFINITY;
#pragma unroll
    for (int ww = 0; ww < NW; ++ww) M = fmaxf(M, mrg[(ww * 4 + g) * 132]);
    float L = 0.f, o0 = 0.f;
#pragma unroll
    for (int ww = 0; ww < NW; ++ww) { const float* rec = mrg + (ww * 4 + g) * 132;
      const float wgt = __builtin_amdgcn_exp2f(rec[0] - M);
      L = fmaf(wgt, rec[1], L); o0 = fmaf(wgt, rec[4 + d], o0); }
    u64* dst = g3 + (size_t)((kvh * 4 + g) * 8 + (d >> 4)) * 576 + (b * 16 + s) * 18;
    put(dst + 2 + (d & 15), tag, __float_as_uint(o0));
    if ((d & 15) == 0) { put(dst, tag, __float_as_uint(M)); put(dst + 1, tag, __float_as_uint(L)); }
  }
  STAMP(6);
  { const unsigned v0 = poll1(g3 + (size_t)w * 576 + tid, tag);
    lds[L_PART + tid] = __uint_as_float(v0);
    if (tid < 64) lds[L_PART + 512 + tid] = __uint_as_float(poll1(g3 + (size_t)w * 576 + 512 + tid, tag)); }
  bar();
  STAMP(7);
  if (tid < 32) {
    const int bb = tid >> 4, d = tid & 15;
    const float* pp = lds + L_PART + bb * 16 * 18;
    float M = -INFINITY;
#pragma unroll
    for (int ss = 0; ss < 16; ++ss) M = fmaxf(M, pp[ss * 18]);
    float L = 0.f, o0 = 0.f;
#pragma unroll
    for (int ss = 0; ss < 16; ++ss) { const float wgt = __builtin_amdgcn_exp2f(pp[ss * 18] - M);
      L = fmaf(wgt, pp[ss * 18 + 1], L); o0 = fmaf(wgt, pp[ss * 18 + 2 + d], o0); }
    lds[L_RES + tid] = rbf(o0 / L);
  }
  bar();
  float* red2 = lds + L_BIG;
  { const u32x4 w0 = *reinterpret_cast<const u32x4*>(lds + L_WST + (0 * 512 + tid) * 4), w1 = *reinterpret_cast<const u32x4*>(lds + L_WST + (1 * 512 + tid) * 4);
#pragma unroll
    for (int bb = 0; bb < 2; ++bb) {
      const float a0 = lds[L_RES + bb * 16 + wave], a1 = lds[L_RES + bb * 16 + 8 + wave];
      float v[8];
#pragma unroll
      for (int j = 0; j < 4; ++j) { v[2 * j] = fmaf(a1, bflo(w1[j]), a0 * bflo(w0[j])); v[2 * j + 1] = fmaf(a1, bfhi(w1[j]), a0 * bfhi(w0[j])); }
      *reinterpret_cast<f32x4*>(red2 + (wave * 2 + bb) * 512 + 8 * lane) = f32x4{v[0], v[1], v[2], v[3]};
      *reinterpret_cast<f32x4*>(red2 + (wave * 2 + bb) * 512 + 8 * lane + 4) = f32x4{v[4], v[5], v[6], v[7]};
    } }
  bar();
  { float s0 = 0.f, s1 = 0.f;
#pragma unroll
    for (int ww = 0; ww < NW; ++ww) { s0 += red2[(ww * 2 + 0) * 512 + tid]; s1 += red2[(ww * 2 + 1) * 512 + tid]; }
    u64* dst = g4 + (size_t)(tid >> 1) * 1024 + w * 4 + (tid & 1) * 2;
    put(dst, tag, __float_as_uint(s0)); put(dst + 1, tag, __float_as_uint(s1)); }
  STAMP(8);
  float x1own0 = 0.f, x1own1 = 0.f;
  { unsigned v0, v1; poll2(g4 + (size_t)w * 1024 + 2 * tid, g4 + (size_t)w * 1024 + 2 * tid + 1, tag, v0, v1);
    float s0 = __uint_as_float(v0), s1 = __uint_as_float(v1);
    s0 = par_sum(s0); s1 = par_sum(s1);
    if (lane < 2) { lds[L_RED + wave * 4 + lane * 2] = s0; lds[L_RED + wave * 4 + lane * 2 + 1] = s1; }
    bar();
    STAMP(9);
    if (tid < 2) {
      float a0 = 0.f, a1 = 0.f;
#pragma unroll
      for (int ww = 0; ww < NW; ++ww) { a0 += lds[L_RED + ww * 4 + tid * 2]; a1 += lds[L_RED + ww * 4 + tid * 2 + 1]; }
      const int n = 2 * w + tid;
      const unsigned ga = poll1(g1 + 1024 + n, tag);
      x1own0 = rbf(bf2f(xs[n]) + rbf(bflo(ga) * rbf(a0)));
      x1own1 = rbf(bf2f(xs[512 + n]) + rbf(bfhi(ga) * rbf(a1)));
      put(g5 + n, tag, f2bf(x1own0) | (f2bf(x1own1) << 16));
    } }
  { unsigned sh, sc; poll2(g1 + 1536 + tid, g1 + 2048 + tid, tag, sh, sc);
    const unsigned xv = poll1(g5 + tid, tag);
    STAMP(10);
    bar();
    ln_mod(lds, tid, bflo(xv), bfhi(xv), sh, sc); }
  if (has_gu) {
    const u32x4 wgv = *reinterpret_cast<const u32x4*>(lds + L_WST + (2 * 512 + tid) * 4), wuv = *reinterpret_cast<const u32x4*>(lds + L_WST + (3 * 512 + tid) * 4);
    const float a0 = lds[L_A + tid], a1 = lds[L_A + 512 + tid];
    float v[32];
#pragma unroll
    for (int j = 0; j < 4; ++j) {
      v[2 * j] = a0 * bflo(wgv[j]); v[2 * j + 1] = a0 * bfhi(wgv[j]); v[8 + 2 * j] = a0 * bflo(wuv[j]); v[8 + 2 * j + 1] = a0 * bfhi(wuv[j]);
      v[16 + 2 * j] = a1 * bflo(wgv[j]); v[16 + 2 * j + 1] = a1 * bfhi(wgv[j]); v[24 + 2 * j] = a1 * bflo(wuv[j]); v[24 + 2 * j + 1] = a1 * bfhi(wuv[j]);
    }
#pragma unroll
    for (int h = 0; h < 16; ++h) { auto sw = __builtin_amdgcn_permlane32_swap(__float_as_uint(v[h]), __float_as_uint(v[h + 16]), false, false); v[h] = __uint_as_float(sw[0]) + __uint_as_float(sw[1]); }
#pragma unroll
    for (int h = 0; h < 8; ++h) { auto sw = __builtin_amdgcn_permlane16_swap(__float_as_uint(v[h]), __float_as_uint(v[h + 8]), false, false); v[h] = __uint_as_float(sw[0]) + __uint_as_float(sw[1]); }
#define TRS(HALF, CTRL, BIT) _Pragma("unroll") for (int h = 0; h < (HALF); ++h) { \
      const float lo_ = v[h], hi_ = v[h + (HALF)]; const float t1_ = lo_ + dpp<CTRL>(lo_), t2_ = hi_ + dpp<CTRL>(hi_); \
      v[h] = (lane & (BIT)) ? t2_ : t1_; }
    TRS(4, XOR8, 8) TRS(2, XOR7, 4) TRS(1, XOR2, 2)
#undef TRS
    v[0] += dpp<XOR1>(v[0]);
    if ((lane & 1) == 0) lds[L_RED + wave * 32 + (lane >> 1)] = v[0];
    bar();
    if (tid < 16) {
      const int bb = tid >> 3, j = tid & 7;
      float gs = 0.f, us = 0.f;
#pragma unroll
      for (int ww = 0; ww < NW; ++ww) { gs += lds[L_RED + ww * 32 + bb * 16 + j]; us += lds[L_RED + ww * 32 + bb * 16 + 8 + j]; }
      lds[L_RES + tid] = rbf(rbf(gs) * rbf(us));
    }
    bar();
    { const u32x4 w0 = *reinterpret_cast<const u32x4*>(lds + L_WST + (4 * 512 + tid) * 4);
#pragma unroll
      for (int bb = 0; bb < 2; ++bb) {
        const float a = lds[L_RES + bb * 8 + wave];
        *reinterpret_cast<f32x4*>(red2 + (wave * 2 + bb) * 512 + 8 * lane) = f32x4{a * bflo(w0[0]), a * bfhi(w0[0]), a * bflo(w0[1]), a * bfhi(w0[1])};
        *reinterpret_cast<f32x4*>(red2 + (wave * 2 + bb) * 512 + 8 * lane + 4) = f32x4{a * bflo(w0[2]), a * bfhi(w0[2]), a * bflo(w0[3]), a * bfhi(w0[3])};
      } }
    bar();
    { float s0 = 0.f, s1 = 0.f;
#pragma unroll
      for (int ww = 0; ww < NW; ++ww) { s0 += red2[(ww * 2 + 0) * 512 + tid]; s1 += red2[(ww * 2 + 1) * 512 + tid]; }
      u64* dst = g6 + (size_t)(tid >> 1) * 1024 + w * 4 + (tid & 1) * 2;
      put(dst, tag, __float_as_uint(s0)); put(dst + 1, tag, __float_as_uint(s1)); }
  }
  STAMP(11);
  { float s0 = 0.f, s1 = 0.f;
    if (tid < 2 * NGU) { unsigned v0, v1; poll2(g6 + (size_t)w * 1024 + 2 * tid, g6 + (size_t)w * 1024 + 2 * tid + 1, tag, v0, v1);
      s0 = __uint_as_float(v0); s1 = __uint_as_float(v1); }
    s0 = par_sum(s0); s1 = par_sum(s1);
    bar();
    if (lane < 2) { lds[L_RED + wave * 4 + lane * 2] = s0; lds[L_RED + wave * 4 + lane * 2 + 1] = s1; }
    bar();
    if (tid < 2) {
      float a0 = 0.f, a1 = 0.f;
#pragma unroll
      for (int ww = 0; ww < NW; ++ww) { a0 += lds[L_RED + ww * 4 + tid * 2]; a1 += lds[L_RED + ww * 4 + tid * 2 + 1]; }
      const int n = 2 * w + tid;
      const unsigned gb = poll1(g1 + 2560 + n, tag);
      out[n] = __float2bfloat16(x1own0 + rbf(bflo(gb) * rbf(a0)));
      out[512 + n] = __float2bfloat16(x1own1 + rbf(bfhi(gb) * rbf(a1)));
    }
    if (w == 0 && tid == 0) __hip_atomic_store((unsigned*)(ws + WS_EPOCH), tag, __ATOMIC_RELAXED, __HIP_MEMORY_SCOPE_AGENT);
  }
  STAMP(12);
}

extern "C" void kernel_launch(void* const* d_in, const int* in_sizes, int n_in, void* d_out, int out_size, void* d_ws, size_t ws_size, hipStream_t stream) {
  (void)hipFuncSetAttribute((const void*)mega, hipFuncAttributeMaxDynamicSharedMemorySize, L_TOTAL * 4);
  mega<<<NWG, NT, L_TOTAL * 4, stream>>>((const bf16*)d_in[0], (const bf16*)d_in[1], (const bf16*)d_in[2], (const bf16*)d_in[3], (const bf16*)d_in[4],
                                        (const bf16*)d_in[6], (const bf16*)d_in[7], (const bf16*)d_in[8], (const bf16*)d_in[9], (const bf16*)d_in[10],
                                        (bf16*)d_out, (char*)d_ws);
}
```

```cpp
#include <hip/hip_runtime.h>
#include <hip/hip_bf16.h>
#include <cstdint>

using bf16 = __hip_bfloat16;
typedef unsigned long long u64;
constexpr int B = 2, DM = 512, HQ = 32, HKV = 8, G = 4, SK = 32768, DH = 128, F = 1792;
constexpr int NQ = HQ * DH;
constexpr int NWG = 256, NT = 512, NW = 8;
constexpr int NSPLIT = 16, PAGE = 16;
constexpr int PPS = SK / PAGE / NSPLIT;
constexpr int NGU = F / 8;
constexpr unsigned SPIN_CAP = 1u << 22;

using u32x4 = __attribute__((ext_vector_type(4))) unsigned;
using f32x4 = __attribute__((ext_vector_type(4))) float;
using bf16x2 = __attribute__((ext_vector_type(2))) __bf16;

constexpr size_t WS_G1 = 0;
constexpr size_t WS_G2 = 32768;
constexpr size_t WS_G4 = 65536;
constexpr size_t WS_G5 = 98304;
constexpr size_t WS_G6 = 102400;
constexpr size_t WS_EPOCH = 131072;
constexpr size_t WS_G3 = 1u << 20;
constexpr size_t WS_TS = 8u << 20;

__device__ __forceinline__ float bf2f(unsigned h) { return __builtin_bit_cast(float, h << 16); }
__device__ __forceinline__ float bflo(unsigned w) { return __builtin_bit_cast(float, w << 16); }
__device__ __forceinline__ float bfhi(unsigned w) { return __builtin_bit_cast(float, w & 0xffff0000u); }
__device__ __forceinline__ unsigned f2bf(float f) {
  unsigned u = __builtin_bit_cast(unsigned, f);
  if ((u & 0x7fffffffu) > 0x7f800000u) return ((u >> 16) | 0x40) & 0xffffu;
  u += 0x7fffu + ((u >> 16) & 1u);
  return u >> 16;
}
__device__ __forceinline__ float rbf(float f) { return bf2f(f2bf(f)); }
__device__ __forceinline__ float dot2(unsigned k, unsigned q, float acc) {
  return __builtin_amdgcn_fdot2_f32_bf16(__builtin_bit_cast(bf16x2, k), __builtin_bit_cast(bf16x2, q), acc, false);
}
template <int CTRL> __device__ __forceinline__ float dpp(float x) {
  return __builtin_bit_cast(float, __builtin_amdgcn_mov_dpp(__builtin_bit_cast(int, x), CTRL, 0xf, 0xf, true));
}
constexpr int XOR1 = 0xB1, XOR2 = 0x4E, XOR7 = 0x141, XOR8 = 0x128, ROR4 = 0x124;
__device__ __forceinline__ float xrow16_max(float x) {
  auto s = __builtin_amdgcn_permlane16_swap(__float_as_uint(x), __float_as_uint(x), false, false);
  x = fmaxf(__uint_as_float(s[0]), __uint_as_float(s[1]));
  auto t = __builtin_amdgcn_permlane32_swap(__float_as_uint(x), __float_as_uint(x), false, false);
  return fmaxf(__uint_as_float(t[0]), __uint_as_float(t[1]));
}
__device__ __forceinline__ float xrow16_sum(float x) {
  auto s = __builtin_amdgcn_permlane16_swap(__float_as_uint(x), __float_as_uint(x), false, false);
  x = __uint_as_float(s[0]) + __uint_as_float(s[1]);
  auto t = __builtin_amdgcn_permlane32_swap(__float_as_uint(x), __float_as_uint(x), false, false);
  return __uint_as_float(t[0]) + __uint_as_float(t[1]);
}
__device__ __forceinline__ float wave_sum(float x) {
  x += dpp<XOR1>(x); x += dpp<XOR2>(x); x += dpp<XOR7>(x); x += dpp<XOR8>(x);
  return xrow16_sum(x);
}
__device__ __forceinline__ float par_sum(float x) {
  x += dpp<XOR2>(x); x += dpp<ROR4>(x); x += dpp<XOR8>(x);
  return xrow16_sum(x);
}
__device__ __forceinline__ void bar() { asm volatile("s_waitcnt lgkmcnt(0)\n\ts_barrier" ::: "memory"); }
__device__ __forceinline__ void put(u64* p, unsigned tag, unsigned v) {
  __hip_atomic_store(p, ((u64)tag << 32) | v, __ATOMIC_RELAXED, __HIP_MEMORY_SCOPE_AGENT);
}
template <int N>
__device__ __forceinline__ void sweep(const u64* p, int stride, unsigned tag, unsigned (&v)[N]) {
  for (unsigned n = 0;; ++n) {
    bool ok = true;
#pragma unroll
    for (int k = 0; k < N; ++k) { const u64 a = __hip_atomic_load(p + k * stride, __ATOMIC_RELAXED, __HIP_MEMORY_SCOPE_AGENT); v[k] = (unsigned)a; ok &= (unsigned)(a >> 32) == tag; }
    if (__all(ok) || n > SPIN_CAP) break;
    __builtin_amdgcn_s_sleep(1);
  }
}

#define STAMP(i) do { } while (0)

constexpr int L_A = 0;
constexpr int L_X = 1024;
constexpr int L_RED = 2048;
constexpr int L_RES = 2304;
constexpr int L_Q = 2368;
constexpr int L_P = 2624;
constexpr int L_TICK = 3136;
constexpr int L_MODS = 3140;
constexpr int L_PART = L_MODS + 3072;
constexpr int L_WST = L_PART + 640;
constexpr int L_MRG = L_WST + 20 * 512;
constexpr int L_TOTAL = L_MRG + 4224 + 6144;

__device__ __forceinline__ void ln_norm(const float* lds, int lane, float x0, float x1, float& n0, float& n1) {
#pragma unroll
  for (int bb = 0; bb < 2; ++bb) {
    float v[8], s = 0.f;
#pragma unroll
    for (int j = 0; j < 8; ++j) { v[j] = lds[L_X + bb * 512 + lane + 64 * j]; s += v[j]; }
    const float mu = wave_sum(s) * (1.f / DM);
    float q = 0.f;
#pragma unroll
    for (int j = 0; j < 8; ++j) { const float d = v[j] - mu; q += d * d; }
    const float var = rbf(wave_sum(q) * (1.f / DM));
    const float sd = rbf(sqrtf(rbf(var + 1e-5f)));
    const float t = rbf(rbf((bb ? x1 : x0) - rbf(mu)) / sd);
    if (bb) n1 = t; else n0 = t;
  }
}
__device__ __forceinline__ void gemv16_part(float* lds, int tid, const u32x4* wv) {
  const int lane = tid & 63, wave = tid >> 6;
  float acc[16];
#pragma unroll
  for (int j = 0; j < 16; ++j) acc[j] = 0.f;
#pragma unroll
  for (int i = 0; i < 2; ++i) {
    const int row = (tid >> 1) + 256 * i;
    const float a0 = lds[L_A + row], a1 = lds[L_A + 512 + row];
#pragma unroll
    for (int j = 0; j < 4; ++j) {
      const float lo = bflo(wv[i][j]), hi = bfhi(wv[i][j]);
      acc[2 * j] = fmaf(a0, lo, acc[2 * j]); acc[2 * j + 1] = fmaf(a0, hi, acc[2 * j + 1]);
      acc[8 + 2 * j] = fmaf(a1, lo, acc[8 + 2 * j]); acc[8 + 2 * j + 1] = fmaf(a1, hi, acc[8 + 2 * j + 1]);
    }
  }
#pragma unroll
  for (int j = 0; j < 16; ++j) acc[j] = par_sum(acc[j]);
  if (lane < 2) {
#pragma unroll
    for (int j = 0; j < 16; j += 4) *reinterpret_cast<f32x4*>(lds + L_RED + wave * 32 + lane * 16 + j) = f32x4{acc[j], acc[j + 1], acc[j + 2], acc[j + 3]};
  }
}
__device__ __forceinline__ unsigned gemv16_fin(const float* lds, int t) {
  const int half = t >> 3, j = t & 7;
  float s0 = 0.f, s1 = 0.f;
#pragma unroll
  for (int ww = 0; ww < 8; ++ww) { s0 += lds[L_RED + ww * 32 + half * 16 + j]; s1 += lds[L_RED + ww * 32 + half * 16 + 8 + j]; }
  return f2bf(s0) | (f2bf(s1) << 16);
}

__global__ void __launch_bounds__(NT, 2) mega(const bf16* __restrict__ x, const bf16* __restrict__ kc, const bf16* __restrict__ vc,
                                              const bf16* __restrict__ wq, const bf16* __restrict__ wo, const bf16* __restrict__ wg,
                                              const bf16* __restrict__ wu, const bf16* __restrict__ wd, const bf16* __restrict__ c,
                                              const bf16* __restrict__ w_mod, bf16* __restrict__ out, char* __restrict__ ws) {
  extern __shared__ __attribute__((aligned(16))) float lds[];
  const int tid = threadIdx.x, lane = tid & 63, wave = __builtin_amdgcn_readfirstlane(tid >> 6);
  const int w = blockIdx.x;
  const unsigned tag = __hip_atomic_load((const unsigned*)(ws + WS_EPOCH), __ATOMIC_RELAXED, __HIP_MEMORY_SCOPE_AGENT) + 1u;
  u64* g1 = (u64*)(ws + WS_G1); u64* g2 = (u64*)(ws + WS_G2); u64* g3 = (u64*)(ws + WS_G3);
  u64* g4 = (u64*)(ws + WS_G4); u64* g5 = (u64*)(ws + WS_G5); u64* g6 = (u64*)(ws + WS_G6);
  u64* ts = (u64*)(ws + WS_TS); (void)ts;
  const int pair = w >> 4, b = pair >> 3, kvh = pair & 7, s = w & 15;
  const int r = lane >> 4, cc = lane & 15;
  const int col0 = 2 * ((w & 7) * 32 + (w >> 3));
  STAMP(0);
  const unsigned short* cs = (const unsigned short*)c; const unsigned short* xs = (const unsigned short*)x;
  const bool has_mod = w < 192;
  const bool has_gu = w < NGU;
  const int wgu = has_gu ? w : 0;
  int* tick = (int*)(lds + L_TICK);
  const float c0 = bf2f(cs[tid]), c1 = bf2f(cs[512 + tid]);
  const float x0 = bf2f(xs[tid]), x1 = bf2f(xs[512 + tid]);
  u32x4 wm[2], wqv[2];
#pragma unroll
  for (int i = 0; i < 2; ++i) {
    const int row = (tid >> 1) + 256 * i;
    wm[i] = *reinterpret_cast<const u32x4*>(w_mod + (size_t)row * (6 * DM) + (has_mod ? w : 0) * 16 + (tid & 1) * 8);
    wqv[i] = *reinterpret_cast<const u32x4*>(wq + (size_t)row * NQ + w * 16 + (tid & 1) * 8);
  }
  lds[L_A + tid] = rbf(c0 / (1.f + __expf(-c0))); lds[L_A + 512 + tid] = rbf(c1 / (1.f + __expf(-c1)));
  lds[L_X + tid] = x0; lds[L_X + 512 + tid] = x1;
  if (tid == 0) *tick = 14;
  bar();
  gemv16_part(lds, tid, wm);
  float n0, n1;
  ln_norm(lds, lane, x0, x1, n0, n1);
  bar();
  if (has_mod && tid < 16) put(g1 + w * 16 + tid, tag, gemv16_fin(lds, tid));
  STAMP(1);
  const size_t base = ((size_t)(b * HKV + kvh) * SK + (size_t)s * PPS * PAGE) * DH + (size_t)r * DH + 8 * cc;
  const bf16* kb = kc + base; const bf16* vb = vc + base;
  u32x4 KA[4], VA[4], KB[4], VB[4];
#define ISSUE(KX, VX, pg_) do { const size_t off_ = (size_t)(pg_) * PAGE * DH; \
    _Pragma("unroll") for (int t_ = 0; t_ < 4; ++t_) { KX[t_] = __builtin_nontemporal_load(reinterpret_cast<const u32x4*>(kb + off_ + t_ * 4 * DH)); VX[t_] = __builtin_nontemporal_load(reinterpret_cast<const u32x4*>(vb + off_ + t_ * 4 * DH)); } } while (0)
  unsigned st[20];
#define STASH_LOAD() do { \
    _Pragma("unroll") for (int i = 0; i < 8; ++i) st[i] = *reinterpret_cast<const unsigned*>(wo + (size_t)(tid + 512 * i) * DM + col0); \
    { const u32x4 a_ = *reinterpret_cast<const u32x4*>(wg + (size_t)tid * F + 8 * wgu); st[8] = a_[0]; st[9] = a_[1]; st[10] = a_[2]; st[11] = a_[3]; } \
    { const u32x4 a_ = *reinterpret_cast<const u32x4*>(wu + (size_t)tid * F + 8 * wgu); st[12] = a_[0]; st[13] = a_[1]; st[14] = a_[2]; st[15] = a_[3]; } \
    _Pragma("unroll") for (int i = 0; i < 4; ++i) { const int k_ = tid + 512 * i; st[16 + i] = *reinterpret_cast<const unsigned*>(wd + (size_t)(k_ < F ? k_ : 0) * DM + col0); } } while (0)
#define STASH_STORE() do { _Pragma("unroll") for (int i = 0; i < 20; i += 4) *reinterpret_cast<u32x4*>(lds + L_WST + (i * 128 + tid) * 4 - 0 + 0 * i) = u32x4{st[i], st[i + 1], st[i + 2], st[i + 3]}; } while (0)
  int pA = wave - 1, pB = wave + 6;
  if (wave != 0) {
    ISSUE(KA, VA, pA);
    ISSUE(KB, VB, pB);
    STASH_LOAD();
  } else {
    unsigned v[16];
    sweep<16>(g1 + lane, 64, tag, v);
#pragma unroll
    for (int j = 0; j < 16; ++j) ((unsigned*)(lds + L_MODS))[lane + 64 * j] = v[j];
  }
  bar();
  STAMP(2);
  { const unsigned sh = ((const unsigned*)(lds + L_MODS))[tid], sc = ((const unsigned*)(lds + L_MODS))[512 + tid];
    lds[L_A + tid] = rbf(rbf(n0 * rbf(1.f + bflo(sc))) + bflo(sh));
    lds[L_A + 512 + tid] = rbf(rbf(n1 * rbf(1.f + bfhi(sc))) + bfhi(sh)); }
  bar();
  gemv16_part(lds, tid, wqv);
  bar();
  if (wave == 0) {
    if (tid < 16) put(g2 + w * 16 + tid, tag, gemv16_fin(lds, tid));
    STAMP(3);
    { unsigned v[16];
      sweep<16>(g1 + 1536 + lane, 64, tag, v);
#pragma unroll
      for (int j = 0; j < 16; ++j) ((unsigned*)(lds + L_MODS))[1536 + lane + 64 * j] = v[j]; }
    { unsigned v[1];
      sweep<1>(g1 + (lane < 2 ? 1024 + col0 + lane : 2560 + col0 + (lane & 1)), 0, tag, v);
      if (lane < 4) ((unsigned*)(lds + L_MODS))[1024 + lane] = v[0]; }
    unsigned qq[8];
    sweep<8>(g2 + kvh * 512 + lane, 64, tag, qq);
#pragma unroll
    for (int j = 0; j < 8; ++j) ((unsigned short*)(lds + L_Q))[lane + 64 * j] = (unsigned short)(b ? (qq[j] >> 16) : (qq[j] & 0xffffu));
    pA = __builtin_amdgcn_readfirstlane(lane == 0 ? atomicAdd(tick, 1) : 0);
    pB = __builtin_amdgcn_readfirstlane(lane == 0 ? atomicAdd(tick, 1) : 0);
    ISSUE(KA, VA, pA);
    ISSUE(KB, VB, pB);
    STASH_LOAD();
  }
  bar();
  STAMP(4);
  u32x4 qpk[4];
#pragma unroll
  for (int g = 0; g < 4; ++g) qpk[g] = *reinterpret_cast<const u32x4*>((const unsigned short*)(lds + L_Q) + g * DH + 8 * cc);
  STASH_STORE();
  constexpr float C2 = 0.088388347648318440f * 1.4426950408889634f;
  float m = -INFINITY, l = 0.f, acc[32];
#pragma unroll
  for (int k = 0; k < 32; ++k) acc[k] = 0.f;
  float* prow = lds + L_P + (wave * 4 + r) * 16;
#define TR_STEP(HALF, CTRL, BIT) _Pragma("unroll") for (int h = 0; h < (HALF); ++h) { \
      const float lo_ = xx[h], hi_ = xx[h + (HALF)]; const float t1_ = lo_ + dpp<CTRL>(lo_), t2_ = hi_ + dpp<CTRL>(hi_); \
      xx[h] = (cc & (BIT)) ? t2_ : t1_; }
#define PROCESS(KX, VX) do { \
    float xx[16]; \
    _Pragma("unroll") for (int t = 0; t < 4; ++t) _Pragma("unroll") for (int g = 0; g < 4; ++g) { float d = 0.f; \
        _Pragma("unroll") for (int k = 0; k < 4; ++k) d = dot2(KX[t][k], qpk[g][k], d); xx[t * 4 + g] = d; } \
    TR_STEP(8, XOR8, 8) TR_STEP(4, XOR7, 4) TR_STEP(2, XOR2, 2) TR_STEP(1, XOR1, 1) \
    const float sc = xx[0] * C2; \
    float pm = sc; pm = fmaxf(pm, dpp<ROR4>(pm)); pm = fmaxf(pm, dpp<XOR8>(pm)); pm = xrow16_max(pm); \
    const float mn = fmaxf(m, pm); \
    const float alpha = __builtin_amdgcn_exp2f(m - mn), p = __builtin_amdgcn_exp2f(sc - mn); \
    l = fmaf(l, alpha, p); m = mn; \
    _Pragma("unroll") for (int q4 = 0; q4 < 4; ++q4) { \
      const float al = q4 == 0 ? dpp<0x00>(alpha) : q4 == 1 ? dpp<0x55>(alpha) : q4 == 2 ? dpp<0xAA>(alpha) : dpp<0xFF>(alpha); \
      _Pragma("unroll") for (int k = 0; k < 8; ++k) acc[q4 * 8 + k] *= al; } \
    prow[cc] = p; \
    asm volatile("s_waitcnt lgkmcnt(0)" ::: "memory"); \
    f32x4 pr[4]; \
    _Pragma("unroll") for (int u = 0; u < 4; ++u) pr[u] = *reinterpret_cast<const f32x4*>(prow + 4 * u); \
    _Pragma("unroll") for (int t = 0; t < 4; ++t) { \
      float vf[8]; \
      _Pragma("unroll") for (int k = 0; k < 4; ++k) { vf[2 * k] = bflo(VX[t][k]); vf[2 * k + 1] = bfhi(VX[t][k]); } \
      _Pragma("unroll") for (int g = 0; g < 4; ++g) { const float pg_ = pr[t][g]; \
        _Pragma("unroll") for (int k = 0; k < 8; ++k) acc[g * 8 + k] = fmaf(pg_, vf[k], acc[g * 8 + k]); } } \
  } while (0)
  for (;;) {
    PROCESS(KA, VA);
    pA = __builtin_amdgcn_readfirstlane(lane == 0 ? atomicAdd(tick, 1) : 0);
    if (pA < PPS) ISSUE(KA, VA, pA);
    if (pB >= PPS) break;
    PROCESS(KB, VB);
    pB = __builtin_amdgcn_readfirstlane(lane == 0 ? atomicAdd(tick, 1) : 0);
    if (pB < PPS) ISSUE(KB, VB, pB);
    if (pA >= PPS) break;
  }
#undef PROCESS
#undef TR_STEP
#undef ISSUE
  STAMP(5);
#pragma unroll
  for (int k = 0; k < 32; ++k) acc[k] = xrow16_sum(acc[k]);
  { float t = l; t += dpp<ROR4>(t); t += dpp<XOR8>(t); l = xrow16_sum(t); }
  float* mrg = lds + L_MRG;
  if (r == 0) {
#pragma unroll
    for (int g = 0; g < 4; ++g) {
      float* rec = mrg + (wave * 4 + g) * 132;
#pragma unroll
      for (int k = 0; k < 8; k += 4) *reinterpret_cast<f32x4*>(rec + 4 + 8 * cc + k) = f32x4{acc[g * 8 + k], acc[g * 8 + k + 1], acc[g * 8 + k + 2], acc[g * 8 + k + 3]};
      if (cc == g) { rec[0] = m; rec[1] = l; } } }
  bar();
  { const int g = tid >> 7, d = tid & 127;
    float M = -INFINITY;
#pragma unroll
    for (int ww = 0; ww < NW; ++ww) M = fmaxf(M, mrg[(ww * 4 + g) * 132]);
    float L = 0.f, o0 = 0.f;
#pragma unroll
    for (int ww = 0; ww < NW; ++ww) { const float* rec = mrg + (ww * 4 + g) * 132;
      const float wgt = __builtin_amdgcn_exp2f(rec[0] - M);
      L = fmaf(wgt, rec[1], L); o0 = fmaf(wgt, rec[4 + d], o0); }
    u64* dst = g3 + (size_t)((kvh * 4 + g) * 8 + (d >> 4)) * 576 + (b * 16 + s) * 18;
    put(dst + 2 + (d & 15), tag, __float_as_uint(o0));
    if ((d & 15) == 0) { put(dst, tag, __float_as_uint(M)); put(dst + 1, tag, __float_as_uint(L)); }
  }
  STAMP(6);
  { unsigned v[1]; sweep<1>(g3 + (size_t)w * 576 + tid, 0, tag, v);
    lds[L_PART + tid] = __uint_as_float(v[0]);
    if (wave == 0) { sweep<1>(g3 + (size_t)w * 576 + 512 + tid, 0, tag, v); lds[L_PART + 512 + tid] = __uint_as_float(v[0]); } }
  bar();
  STAMP(7);
  if (tid < 32) {
    const int bb = tid >> 4, d = tid & 15;
    const float* pp = lds + L_PART + bb * 16 * 18;
    float M = -INFINITY;
#pragma unroll
    for (int ss = 0; ss < 16; ++ss) M = fmaxf(M, pp[ss * 18]);
    float L = 0.f, o0 = 0.f;
#pragma unroll
    for (int ss = 0; ss < 16; ++ss) { const float wgt = __builtin_amdgcn_exp2f(pp[ss * 18] - M);
      L = fmaf(wgt, pp[ss * 18 + 1], L); o0 = fmaf(wgt, pp[ss * 18 + 2 + d], o0); }
    const unsigned ob = f2bf(o0 / L);
    const unsigned other = __shfl_down(ob, 16);
    if (tid < 16) put(g4 + w * 16 + tid, tag, ob | (other << 16));
  }
  STAMP(8);
  float x1own0 = 0.f, x1own1 = 0.f;
  { unsigned ov[8]; sweep<8>(g4 + tid, 512, tag, ov);
    STAMP(9);
    float a00 = 0.f, a01 = 0.f, a10 = 0.f, a11 = 0.f;
#pragma unroll
    for (int i = 0; i < 8; ++i) {
      const unsigned wv = ((const unsigned*)(lds + L_WST))[((i >> 2) * 512 + tid) * 4 + (i & 3)];
      const float w0 = bflo(wv), w1 = bfhi(wv), o0 = bflo(ov[i]), o1 = bfhi(ov[i]);
      a00 = fmaf(o0, w0, a00); a01 = fmaf(o0, w1, a01); a10 = fmaf(o1, w0, a10); a11 = fmaf(o1, w1, a11);
    }
    a00 = wave_sum(a00); a01 = wave_sum(a01); a10 = wave_sum(a10); a11 = wave_sum(a11);
    if (lane == 0) *reinterpret_cast<f32x4*>(lds + L_RED + wave * 4) = f32x4{a00, a01, a10, a11};
    bar();
    if (tid < 2) {
      float a0 = 0.f, a1 = 0.f;
#pragma unroll
      for (int ww = 0; ww < NW; ++ww) { a0 += lds[L_RED + ww * 4 + tid]; a1 += lds[L_RED + ww * 4 + 2 + tid]; }
      const int n = col0 + tid;
      const unsigned ga = ((const unsigned*)(lds + L_MODS))[1024 + tid];
      x1own0 = rbf(bf2f(xs[n]) + rbf(bflo(ga) * rbf(a0)));
      x1own1 = rbf(bf2f(xs[512 + n]) + rbf(bfhi(ga) * rbf(a1)));
      put(g5 + n, tag, f2bf(x1own0) | (f2bf(x1own1) << 16));
    } }
  STAMP(10);
  { unsigned v[1]; sweep<1>(g5 + tid, 0, tag, v);
    STAMP(11);
    const float y0 = bflo(v[0]), y1 = bfhi(v[0]);
    lds[L_X + tid] = y0; lds[L_X + 512 + tid] = y1;
    bar();
    if (has_gu) {
      float m0, m1; ln_norm(lds, lane, y0, y1, m0, m1);
      const unsigned sh = ((const unsigned*)(lds + L_MODS))[1536 + tid], sc = ((const unsigned*)(lds + L_MODS))[2048 + tid];
      const float a0 = rbf(rbf(m0 * rbf(1.f + bflo(sc))) + bflo(sh)), a1 = rbf(rbf(m1 * rbf(1.f + bfhi(sc))) + bfhi(sh));
      const u32x4 wgv = *reinterpret_cast<const u32x4*>(lds + L_WST + (2 * 512 + tid) * 4), wuv = *reinterpret_cast<const u32x4*>(lds + L_WST + (3 * 512 + tid) * 4);
      float v2[32];
#pragma unroll
      for (int j = 0; j < 4; ++j) {
        v2[2 * j] = a0 * bflo(wgv[j]); v2[2 * j + 1] = a0 * bfhi(wgv[j]); v2[8 + 2 * j] = a0 * bflo(wuv[j]); v2[8 + 2 * j + 1] = a0 * bfhi(wuv[j]);
        v2[16 + 2 * j] = a1 * bflo(wgv[j]); v2[16 + 2 * j + 1] = a1 * bfhi(wgv[j]); v2[24 + 2 * j] = a1 * bflo(wuv[j]); v2[24 + 2 * j + 1] = a1 * bfhi(wuv[j]);
      }
#pragma unroll
      for (int h = 0; h < 16; ++h) { auto sw = __builtin_amdgcn_permlane32_swap(__float_as_uint(v2[h]), __float_as_uint(v2[h + 16]), false, false); v2[h] = __uint_as_float(sw[0]) + __uint_as_float(sw[1]); }
#pragma unroll
      for (int h = 0; h < 8; ++h) { auto sw = __builtin_amdgcn_permlane16_swap(__float_as_uint(v2[h]), __float_as_uint(v2[h + 8]), false, false); v2[h] = __uint_as_float(sw[0]) + __uint_as_float(sw[1]); }
#define TRS(HALF, CTRL, BIT) _Pragma("unroll") for (int h = 0; h < (HALF); ++h) { \
      const float lo_ = v2[h], hi_ = v2[h + (HALF)]; const float t1_ = lo_ + dpp<CTRL>(lo_), t2_ = hi_ + dpp<CTRL>(hi_); \
      v2[h] = (lane & (BIT)) ? t2_ : t1_; }
      TRS(4, XOR8, 8) TRS(2, XOR7, 4) TRS(1, XOR2, 2)
#undef TRS
      v2[0] += dpp<XOR1>(v2[0]);
      if ((lane & 1) == 0) lds[L_RED + wave * 32 + (lane >> 1)] = v2[0];
    }
    bar();
    if (has_gu && tid < 8) {
      float g0 = 0.f, u0 = 0.f, g1s = 0.f, u1 = 0.f;
#pragma unroll
      for (int ww = 0; ww < NW; ++ww) { g0 += lds[L_RED + ww * 32 + tid]; u0 += lds[L_RED + ww * 32 + 8 + tid]; g1s += lds[L_RED + ww * 32 + 16 + tid]; u1 += lds[L_RED + ww * 32 + 24 + tid]; }
      put(g6 + 8 * w + tid, tag, f2bf(rbf(g0) * rbf(u0)) | (f2bf(rbf(g1s) * rbf(u1)) << 16));
    } }
  STAMP(12);
  { unsigned hv[4];
    for (unsigned n = 0;; ++n) {
      bool ok = true;
#pragma unroll
      for (int i = 0; i < 4; ++i) { const int k = tid + 512 * i; const u64 a = __hip_atomic_load(g6 + (k < F ? k : 0), __ATOMIC_RELAXED, __HIP_MEMORY_SCOPE_AGENT); hv[i] = k < F ? (unsigned)a : 0u; ok &= (unsigned)(a >> 32) == tag; }
      if (__all(ok) || n > SPIN_CAP) break;
      __builtin_amdgcn_s_sleep(1);
    }
    STAMP(13);
    float a00 = 0.f, a01 = 0.f, a10 = 0.f, a11 = 0.f;
    const u32x4 wdv = *reinterpret_cast<const u32x4*>(lds + L_WST + (4 * 512 + tid) * 4);
#pragma unroll
    for (int i = 0; i < 4; ++i) {
      const float w0 = bflo(wdv[i]), w1 = bfhi(wdv[i]), o0 = bflo(hv[i]), o1 = bfhi(hv[i]);
      a00 = fmaf(o0, w0, a00); a01 = fmaf(o0, w1, a01); a10 = fmaf(o1, w0, a10); a11 = fmaf(o1, w1, a11);
    }
    a00 = wave_sum(a00); a01 = wave_sum(a01); a10 = wave_sum(a10); a11 = wave_sum(a11);
    if (lane == 0) *reinterpret_cast<f32x4*>(lds + L_RED + wave * 4) = f32x4{a00, a01, a10, a11};
    bar();
    if (tid < 2) {
      float a0 = 0.f, a1 = 0.f;
#pragma unroll
      for (int ww = 0; ww < NW; ++ww) { a0 += lds[L_RED + ww * 4 + tid]; a1 += lds[L_RED + ww * 4 + 2 + tid]; }
      const int n = col0 + tid;
      const unsigned gb = ((const unsigned*)(lds + L_MODS))[1026 + tid];
      out[n] = __float2bfloat16(x1own0 + rbf(bflo(gb) * rbf(a0)));
      out[512 + n] = __float2bfloat16(x1own1 + rbf(bfhi(gb) * rbf(a1)));
    }
    if (w == 0 && tid == 0) __hip_atomic_store((unsigned*)(ws + WS_EPOCH), tag, __ATOMIC_RELAXED, __HIP_MEMORY_SCOPE_AGENT);
  }
  STAMP(14);
}

extern "C" void kernel_launch(void* const* d_in, const int* in_sizes, int n_in, void* d_out, int out_size, void* d_ws, size_t ws_size, hipStream_t stream) {
  (void)hipFuncSetAttribute((const void*)mega, hipFuncAttributeMaxDynamicSharedMemorySize, L_TOTAL * 4);
  mega<<<NWG, NT, L_TOTAL * 4, stream>>>((const bf16*)d_in[0], (const bf16*)d_in[1], (const bf16*)d_in[2], (const bf16*)d_in[3], (const bf16*)d_in[4],
                                        (const bf16*)d_in[6], (const bf16*)d_in[7], (const bf16*)d_in[8], (const bf16*)d_in[9], (const bf16*)d_in[10],
                                        (bf16*)d_out, (char*)d_ws);
}
```

```cpp
#include <hip/hip_runtime.h>
#include <hip/hip_bf16.h>
#include <cstdint>

using bf16 = __hip_bfloat16;
typedef unsigned long long u64;
constexpr int B = 2, DM = 512, HQ = 32, HKV = 8, G = 4, SK = 32768, DH = 128, F = 1792;
constexpr int NQ = HQ * DH;
constexpr int NWG = 256, NT = 512, NW = 8;
constexpr int NSPLIT = 16, PAGE = 16;
constexpr int NPG = SK / PAGE;
constexpr int NSTATIC = 224;
constexpr int NGU = F / 8;
constexpr unsigned SPIN_CAP = 1u << 22;

using u32x4 = __attribute__((ext_vector_type(4))) unsigned;
using f32x4 = __attribute__((ext_vector_type(4))) float;
using bf16x2 = __attribute__((ext_vector_type(2))) __bf16;

constexpr size_t WS_G1 = 0;
constexpr size_t WS_G2 = 32768;
constexpr size_t WS_G4 = 65536;
constexpr size_t WS_G5 = 98304;
constexpr size_t WS_G6 = 102400;
constexpr size_t WS_EPOCH = 131072;
constexpr size_t WS_CNT = 135168;
constexpr size_t WS_G3 = 1u << 20;
constexpr size_t WS_TS = 8u << 20;

__device__ __forceinline__ float bf2f(unsigned h) { return __builtin_bit_cast(float, h << 16); }
__device__ __forceinline__ float bflo(unsigned w) { return __builtin_bit_cast(float, w << 16); }
__device__ __forceinline__ float bfhi(unsigned w) { return __builtin_bit_cast(float, w & 0xffff0000u); }
__device__ __forceinline__ unsigned cvtpk(float lo, float hi) {
  unsigned r; asm("v_cvt_pk_bf16_f32 %0, %1, %2" : "=v"(r) : "v"(lo), "v"(hi)); return r;
}
__device__ __forceinline__ float rbf(float f) { unsigned u = __builtin_bit_cast(unsigned, f); u += 0x7fffu + ((u >> 16) & 1u); return __builtin_bit_cast(float, u & 0xffff0000u); }
__device__ __forceinline__ float frcp(float x) { return __builtin_amdgcn_rcpf(x); }
__device__ __forceinline__ float dot2(unsigned k, unsigned q, float acc) {
  return __builtin_amdgcn_fdot2_f32_bf16(__builtin_bit_cast(bf16x2, k), __builtin_bit_cast(bf16x2, q), acc, false);
}
template <int CTRL> __device__ __forceinline__ float dpp(float x) {
  return __builtin_bit_cast(float, __builtin_amdgcn_mov_dpp(__builtin_bit_cast(int, x), CTRL, 0xf, 0xf, true));
}
constexpr int XOR1 = 0xB1, XOR2 = 0x4E, XOR7 = 0x141, XOR8 = 0x128, ROR4 = 0x124;
__device__ __forceinline__ float xrow16_max(float x) {
  auto s = __builtin_amdgcn_permlane16_swap(__float_as_uint(x), __float_as_uint(x), false, false);
  x = fmaxf(__uint_as_float(s[0]), __uint_as_float(s[1]));
  auto t = __builtin_amdgcn_permlane32_swap(__float_as_uint(x), __float_as_uint(x), false, false);
  return fmaxf(__uint_as_float(t[0]), __uint_as_float(t[1]));
}
__device__ __forceinline__ float xrow16_sum(float x) {
  auto s = __builtin_amdgcn_permlane16_swap(__float_as_uint(x), __float_as_uint(x), false, false);
  x = __uint_as_float(s[0]) + __uint_as_float(s[1]);
  auto t = __builtin_amdgcn_permlane32_swap(__float_as_uint(x), __float_as_uint(x), false, false);
  return __uint_as_float(t[0]) + __uint_as_float(t[1]);
}
__device__ __forceinline__ float wave_sum(float x) {
  x += dpp<XOR1>(x); x += dpp<XOR2>(x); x += dpp<XOR7>(x); x += dpp<XOR8>(x);
  return xrow16_sum(x);
}
__device__ __forceinline__ float par_sum(float x) {
  x += dpp<XOR2>(x); x += dpp<ROR4>(x); x += dpp<XOR8>(x);
  return xrow16_sum(x);
}
__device__ __forceinline__ void bar() { asm volatile("s_waitcnt lgkmcnt(0)\n\ts_barrier" ::: "memory"); }
__device__ __forceinline__ void put(u64* p, unsigned tag, unsigned v) {
  __hip_atomic_store(p, ((u64)tag << 32) | v, __ATOMIC_RELAXED, __HIP_MEMORY_SCOPE_AGENT);
}
template <int N>
__device__ __forceinline__ void sweep(const u64* p, int stride, unsigned tag, unsigned (&v)[N]) {
  for (unsigned n = 0;; ++n) {
    bool ok = true;
#pragma unroll
    for (int k = 0; k < N; ++k) { const u64 a = __hip_atomic_load(p + k * stride, __ATOMIC_RELAXED, __HIP_MEMORY_SCOPE_AGENT); v[k] = (unsigned)a; ok &= (unsigned)(a >> 32) == tag; }
    if (__all(ok) || n > SPIN_CAP) break;
    __builtin_amdgcn_s_sleep(1);
  }
}

#define STAMP(i) do { } while (0)

constexpr int L_A = 0;
constexpr int L_X = 1024;
constexpr int L_RED = 2048;
constexpr int L_RES = 2304;
constexpr int L_Q = 2368;
constexpr int L_P = 2624;
constexpr int L_TICK = 3136;
constexpr int L_MODS = 3140;
constexpr int L_PART = L_MODS + 3072;
constexpr int L_WST = L_PART + 640;
constexpr int L_MRG = L_WST + 20 * 512;
constexpr int L_TOTAL = L_MRG + 4224 + 6144;

__device__ __forceinline__ void ln_norm(const float* lds, int lane, float x0, float x1, float& n0, float& n1) {
#pragma unroll
  for (int bb = 0; bb < 2; ++bb) {
    float v[8], s = 0.f;
#pragma unroll
    for (int j = 0; j < 8; ++j) { v[j] = lds[L_X + bb * 512 + lane + 64 * j]; s += v[j]; }
    const float mu = wave_sum(s) * (1.f / DM);
    float q = 0.f;
#pragma unroll
    for (int j = 0; j < 8; ++j) { const float d = v[j] - mu; q += d * d; }
    const float var = rbf(wave_sum(q) * (1.f / DM));
    const float sd = rbf(__builtin_amdgcn_sqrtf(rbf(var + 1e-5f)));
    const float t = rbf(rbf((bb ? x1 : x0) - rbf(mu)) * frcp(sd));
    if (bb) n1 = t; else n0 = t;
  }
}
__device__ __forceinline__ void gemv16_part(float* lds, int tid, const u32x4* wv) {
  const int lane = tid & 63, wave = tid >> 6;
  float acc[16];
#pragma unroll
  for (int j = 0; j < 16; ++j) acc[j] = 0.f;
#pragma unroll
  for (int i = 0; i < 2; ++i) {
    const int row = (tid >> 1) + 256 * i;
    const float a0 = lds[L_A + row], a1 = lds[L_A + 512 + row];
#pragma unroll
    for (int j = 0; j < 4; ++j) {
      const float lo = bflo(wv[i][j]), hi = bfhi(wv[i][j]);
      acc[2 * j] = fmaf(a0, lo, acc[2 * j]); acc[2 * j + 1] = fmaf(a0, hi, acc[2 * j + 1]);
      acc[8 + 2 * j] = fmaf(a1, lo, acc[8 + 2 * j]); acc[8 + 2 * j + 1] = fmaf(a1, hi, acc[8 + 2 * j + 1]);
    }
  }
#pragma unroll
  for (int h = 0; h < 8; ++h) { auto sw = __builtin_amdgcn_permlane32_swap(__float_as_uint(acc[h]), __float_as_uint(acc[h + 8]), false, false); acc[h] = __uint_as_float(sw[0]) + __uint_as_float(sw[1]); }
#pragma unroll
  for (int h = 0; h < 4; ++h) { auto sw = __builtin_amdgcn_permlane16_swap(__float_as_uint(acc[h]), __float_as_uint(acc[h + 4]), false, false); acc[h] = __uint_as_float(sw[0]) + __uint_as_float(sw[1]); }
#pragma unroll
  for (int h = 0; h < 4; ++h) { float v = acc[h]; v += dpp<XOR2>(v); v += dpp<ROR4>(v); v += dpp<XOR8>(v); acc[h] = v; }
  if ((lane & 14) == 0) *reinterpret_cast<f32x4*>(lds + L_RED + wave * 32 + (lane & 1) * 16 + (lane >> 4) * 4) = f32x4{acc[0], acc[1], acc[2], acc[3]};
}
__device__ __forceinline__ unsigned gemv16_fin(const float* lds, int t) {
  const int half = t >> 3, j = t & 7;
  float s0 = 0.f, s1 = 0.f;
#pragma unroll
  for (int ww = 0; ww < 8; ++ww) { s0 += lds[L_RED + ww * 32 + half * 16 + j]; s1 += lds[L_RED + ww * 32 + half * 16 + 8 + j]; }
  return cvtpk(s0, s1);
}

__global__ void __launch_bounds__(NT, 2) mega(const bf16* __restrict__ x, const bf16* __restrict__ kc, const bf16* __restrict__ vc,
                                              const bf16* __restrict__ wq, const bf16* __restrict__ wo, const bf16* __restrict__ wg,
                                              const bf16* __restrict__ wu, const bf16* __restrict__ wd, const bf16* __restrict__ c,
                                              const bf16* __restrict__ w_mod, bf16* __restrict__ out, char* __restrict__ ws) {
  extern __shared__ __attribute__((aligned(16))) float lds[];
  const int tid = threadIdx.x, lane = tid & 63, wave = __builtin_amdgcn_readfirstlane(tid >> 6);
  const int w = blockIdx.x;
  const unsigned tag = __hip_atomic_load((const unsigned*)(ws + WS_EPOCH), __ATOMIC_RELAXED, __HIP_MEMORY_SCOPE_AGENT) + 1u;
  u64* g1 = (u64*)(ws + WS_G1); u64* g2 = (u64*)(ws + WS_G2); u64* g3 = (u64*)(ws + WS_G3);
  u64* g4 = (u64*)(ws + WS_G4); u64* g5 = (u64*)(ws + WS_G5); u64* g6 = (u64*)(ws + WS_G6);
  u64* ts = (u64*)(ws + WS_TS); (void)ts;
  const int pair = w >> 4, b = pair >> 3, kvh = pair & 7, s = w & 15;
  const int r = lane >> 4, cc = lane & 15;
  const int col0 = 2 * ((w & 7) * 32 + (w >> 3));
  STAMP(0);
  const unsigned short* cs = (const unsigned short*)c; const unsigned short* xs = (const unsigned short*)x;
  const int wx = w & 7, wi = w >> 3;
  const bool has_mod = wi < 24;
  const int mslice = wi < 8 ? wx * 8 + wi : (wi < 24 ? 64 + wx * 16 + (wi - 8) : 0);
  const int qslice = wx * 32 + wi;
  const bool has_gu = wi < 28;
  const int wgu = has_gu ? wx * 28 + wi : 0;
  unsigned* cnt = (unsigned*)(ws + WS_CNT + (size_t)pair * 128);
  if (w == 0 && tid < 16) __hip_atomic_store((unsigned*)(ws + WS_CNT + (size_t)tid * 128), (unsigned)NSTATIC, __ATOMIC_RELAXED, __HIP_MEMORY_SCOPE_AGENT);
  const float c0 = bf2f(cs[tid]), c1 = bf2f(cs[512 + tid]);
  const float x0 = bf2f(xs[tid]), x1 = bf2f(xs[512 + tid]);
  u32x4 wm[2], wqv[2];
#pragma unroll
  for (int i = 0; i < 2; ++i) {
    const int row = (tid >> 1) + 256 * i;
    wm[i] = *reinterpret_cast<const u32x4*>(w_mod + (size_t)row * (6 * DM) + mslice * 16 + (tid & 1) * 8);
    wqv[i] = *reinterpret_cast<const u32x4*>(wq + (size_t)row * NQ + qslice * 16 + (tid & 1) * 8);
  }
  lds[L_A + tid] = rbf(c0 * frcp(1.f + __expf(-c0))); lds[L_A + 512 + tid] = rbf(c1 * frcp(1.f + __expf(-c1)));
  lds[L_X + tid] = x0; lds[L_X + 512 + tid] = x1;
  bar();
  gemv16_part(lds, tid, wm);
  float n0, n1;
  ln_norm(lds, lane, x0, x1, n0, n1);
  bar();
  if (has_mod && tid < 16) { const unsigned rr = gemv16_fin(lds, tid); asm volatile("s_waitcnt vmcnt(0)" ::: "memory"); put(g1 + mslice * 16 + tid, tag, rr); }
  STAMP(1);
  const size_t base = ((size_t)(b * HKV + kvh) * SK) * DH + (size_t)r * DH + 8 * cc;
  const bf16* kb = kc + base; const bf16* vb = vc + base;
  u32x4 KA[4], VA[4], KB[4], VB[4];
#define ISSUE(KX, VX, pg_) do { const size_t off_ = (size_t)(pg_) * PAGE * DH; \
    _Pragma("unroll") for (int t_ = 0; t_ < 4; ++t_) { KX[t_] = __builtin_nontemporal_load(reinterpret_cast<const u32x4*>(kb + off_ + t_ * 4 * DH)); VX[t_] = __builtin_nontemporal_load(reinterpret_cast<const u32x4*>(vb + off_ + t_ * 4 * DH)); } } while (0)
  unsigned st[20];
#define STASH_LOAD() do { \
    _Pragma("unroll") for (int i = 0; i < 8; ++i) st[i] = *reinterpret_cast<const unsigned*>(wo + (size_t)(tid + 512 * i) * DM + col0); \
    { const u32x4 a_ = *reinterpret_cast<const u32x4*>(wg + (size_t)tid * F + 8 * wgu); st[8] = a_[0]; st[9] = a_[1]; st[10] = a_[2]; st[11] = a_[3]; } \
    { const u32x4 a_ = *reinterpret_cast<const u32x4*>(wu + (size_t)tid * F + 8 * wgu); st[12] = a_[0]; st[13] = a_[1]; st[14] = a_[2]; st[15] = a_[3]; } \
    _Pragma("unroll") for (int i = 0; i < 4; ++i) { const int k_ = tid + 512 * i; st[16 + i] = *reinterpret_cast<const unsigned*>(wd + (size_t)(k_ < F ? k_ : 0) * DM + col0); } } while (0)
#define STASH_STORE() do { _Pragma("unroll") for (int i = 0; i < 20; i += 4) *reinterpret_cast<u32x4*>(lds + L_WST + (i * 128 + tid) * 4 - 0 + 0 * i) = u32x4{st[i], st[i + 1], st[i + 2], st[i + 3]}; } while (0)
  int pA = s * 7 + wave - 1, pB = 112 + s * 7 + wave - 1;
#define TICKET() ((int)__builtin_amdgcn_readfirstlane(lane == 0 ? __hip_atomic_fetch_add(cnt, 1u, __ATOMIC_RELAXED, __HIP_MEMORY_SCOPE_AGENT) : 0u))
  if (wave != 0) {
    ISSUE(KA, VA, pA);
  } else {
    unsigned v[16];
    sweep<16>(g1 + lane, 64, tag, v);
#pragma unroll
    for (int j = 0; j < 16; ++j) ((unsigned*)(lds + L_MODS))[lane + 64 * j] = v[j];
  }
  bar();
  STAMP(2);
  { const unsigned sh = ((const unsigned*)(lds + L_MODS))[tid], sc = ((const unsigned*)(lds + L_MODS))[512 + tid];
    lds[L_A + tid] = rbf(rbf(n0 * rbf(1.f + bflo(sc))) + bflo(sh));
    lds[L_A + 512 + tid] = rbf(rbf(n1 * rbf(1.f + bfhi(sc))) + bfhi(sh)); }
  bar();
  gemv16_part(lds, tid, wqv);
  bar();
  if (wave != 0) {
    ISSUE(KB, VB, pB);
  } else {
    if (tid < 16) put(g2 + qslice * 16 + tid, tag, gemv16_fin(lds, tid));
    STAMP(3);
    { unsigned v[16];
      sweep<16>(g1 + 1536 + lane, 64, tag, v);
#pragma unroll
      for (int j = 0; j < 16; ++j) ((unsigned*)(lds + L_MODS))[1536 + lane + 64 * j] = v[j]; }
    { unsigned v[1];
      sweep<1>(g1 + (lane < 2 ? 1024 + col0 + lane : 2560 + col0 + (lane & 1)), 0, tag, v);
      if (lane < 4) ((unsigned*)(lds + L_MODS))[1024 + lane] = v[0]; }
    unsigned qq[8];
    sweep<8>(g2 + kvh * 512 + lane, 64, tag, qq);
#pragma unroll
    for (int j = 0; j < 8; ++j) ((unsigned short*)(lds + L_Q))[lane + 64 * j] = (unsigned short)(b ? (qq[j] >> 16) : (qq[j] & 0xffffu));
    pA = TICKET();
    pB = TICKET();
    if (pA < NPG) ISSUE(KA, VA, pA);
    if (pB < NPG) ISSUE(KB, VB, pB);
  }
  bar();
  STAMP(4);
  u32x4 qpk[4];
#pragma unroll
  for (int g = 0; g < 4; ++g) qpk[g] = *reinterpret_cast<const u32x4*>((const unsigned short*)(lds + L_Q) + g * DH + 8 * cc);
  constexpr float C2 = 0.088388347648318440f * 1.4426950408889634f;
  float m = -INFINITY, l = 0.f, acc[32];
#pragma unroll
  for (int k = 0; k < 32; ++k) acc[k] = 0.f;
  float* prow = lds + L_P + (wave * 4 + r) * 16;
#define TR_STEP(HALF, CTRL, BIT) _Pragma("unroll") for (int h = 0; h < (HALF); ++h) { \
      const float lo_ = xx[h], hi_ = xx[h + (HALF)]; const float t1_ = lo_ + dpp<CTRL>(lo_), t2_ = hi_ + dpp<CTRL>(hi_); \
      xx[h] = (cc & (BIT)) ? t2_ : t1_; }
#define PROCESS(KX, VX) do { \
    float xx[16]; \
    _Pragma("unroll") for (int t = 0; t < 4; ++t) _Pragma("unroll") for (int g = 0; g < 4; ++g) { float d = 0.f; \
        _Pragma("unroll") for (int k = 0; k < 4; ++k) d = dot2(KX[t][k], qpk[g][k], d); xx[t * 4 + g] = d; } \
    TR_STEP(8, XOR8, 8) TR_STEP(4, XOR7, 4) TR_STEP(2, XOR2, 2) TR_STEP(1, XOR1, 1) \
    const float sc = xx[0] * C2; \
    float pm = sc; pm = fmaxf(pm, dpp<ROR4>(pm)); pm = fmaxf(pm, dpp<XOR8>(pm)); pm = xrow16_max(pm); \
    const float mn = fmaxf(m, pm); \
    const float alpha = __builtin_amdgcn_exp2f(m - mn), p = __builtin_amdgcn_exp2f(sc - mn); \
    l = fmaf(l, alpha, p); m = mn; \
    _Pragma("unroll") for (int q4 = 0; q4 < 4; ++q4) { \
      const float al = q4 == 0 ? dpp<0x00>(alpha) : q4 == 1 ? dpp<0x55>(alpha) : q4 == 2 ? dpp<0xAA>(alpha) : dpp<0xFF>(alpha); \
      _Pragma("unroll") for (int k = 0; k < 8; ++k) acc[q4 * 8 + k] *= al; } \
    prow[cc] = p; \
    asm volatile("s_waitcnt lgkmcnt(0)" ::: "memory"); \
    f32x4 pr[4]; \
    _Pragma("unroll") for (int u = 0; u < 4; ++u) pr[u] = *reinterpret_cast<const f32x4*>(prow + 4 * u); \
    _Pragma("unroll") for (int t = 0; t < 4; ++t) { \
      float vf[8]; \
      _Pragma("unroll") for (int k = 0; k < 4; ++k) { vf[2 * k] = bflo(VX[t][k]); vf[2 * k + 1] = bfhi(VX[t][k]); } \
      _Pragma("unroll") for (int g = 0; g < 4; ++g) { const float pg_ = pr[t][g]; \
        _Pragma("unroll") for (int k = 0; k < 8; ++k) acc[g * 8 + k] = fmaf(pg_, vf[k], acc[g * 8 + k]); } } \
  } while (0)
#define STASH(it_) do { \
    if ((it_) == 1) { unsigned st[8]; \
      _Pragma("unroll") for (int i = 0; i < 8; ++i) st[i] = *reinterpret_cast<const unsigned*>(wo + (size_t)(tid + 512 * i) * DM + col0); \
      *reinterpret_cast<u32x4*>(lds + L_WST + (0 * 512 + tid) * 4) = u32x4{st[0], st[1], st[2], st[3]}; \
      *reinterpret_cast<u32x4*>(lds + L_WST + (1 * 512 + tid) * 4) = u32x4{st[4], st[5], st[6], st[7]}; \
    } else if ((it_) == 2) { \
      const u32x4 a_ = *reinterpret_cast<const u32x4*>(wg + (size_t)tid * F + 8 * wgu), b_ = *reinterpret_cast<const u32x4*>(wu + (size_t)tid * F + 8 * wgu); \
      *reinterpret_cast<u32x4*>(lds + L_WST + (2 * 512 + tid) * 4) = a_; \
      *reinterpret_cast<u32x4*>(lds + L_WST + (3 * 512 + tid) * 4) = b_; \
    } else if ((it_) == 3) { unsigned st[4]; \
      _Pragma("unroll") for (int i = 0; i < 4; ++i) { const int k_ = tid + 512 * i; st[i] = *reinterpret_cast<const unsigned*>(wd + (size_t)(k_ < F ? k_ : 0) * DM + col0); } \
      *reinterpret_cast<u32x4*>(lds + L_WST + (4 * 512 + tid) * 4) = u32x4{st[0], st[1], st[2], st[3]}; \
    } } while (0)
  int nxt = TICKET();
  int it = 0;
  for (;; ++it) {
    if (pA >= NPG) break;
    PROCESS(KA, VA);
    pA = nxt;
    if (pA < NPG) { nxt = TICKET(); ISSUE(KA, VA, pA); }
    STASH(it);
    if (pB >= NPG) break;
    PROCESS(KB, VB);
    pB = nxt;
    if (pB < NPG) { nxt = TICKET(); ISSUE(KB, VB, pB); }
  }
  for (int j = it; j <= 3; ++j) STASH(j);
#undef PROCESS
#undef TR_STEP
#undef ISSUE
  STAMP(5);
#pragma unroll
  for (int k = 0; k < 32; ++k) acc[k] = xrow16_sum(acc[k]);
  { float t = l; t += dpp<ROR4>(t); t += dpp<XOR8>(t); l = xrow16_sum(t); }
  float* mrg = lds + L_MRG;
  if (r == 0) {
#pragma unroll
    for (int g = 0; g < 4; ++g) {
      float* rec = mrg + (wave * 4 + g) * 132;
#pragma unroll
      for (int k = 0; k < 8; k += 4) *reinterpret_cast<f32x4*>(rec + 4 + 8 * cc + k) = f32x4{acc[g * 8 + k], acc[g * 8 + k + 1], acc[g * 8 + k + 2], acc[g * 8 + k + 3]};
      if (cc == g) { rec[0] = m; rec[1] = l; } } }
  bar();
  { const int g = tid >> 7, d = tid & 127;
    float M = -INFINITY;
#pragma unroll
    for (int ww = 0; ww < NW; ++ww) M = fmaxf(M, mrg[(ww * 4 + g) * 132]);
    float L = 0.f, o0 = 0.f;
#pragma unroll
    for (int ww = 0; ww < NW; ++ww) { const float* rec = mrg + (ww * 4 + g) * 132;
      const float wgt = __builtin_amdgcn_exp2f(rec[0] - M);
      L = fmaf(wgt, rec[1], L); o0 = fmaf(wgt, rec[4 + d], o0); }
    u64* dst = g3 + (size_t)((kvh * 4 + g) * 8 + (d >> 4)) * 576 + (b * 16 + s) * 18;
    put(dst + 2 + (d & 15), tag, __float_as_uint(o0));
    if ((d & 15) == 0) { put(dst, tag, __float_as_uint(M)); put(dst + 1, tag, __float_as_uint(L)); }
  }
  STAMP(6);
  { unsigned v[1]; sweep<1>(g3 + (size_t)w * 576 + tid, 0, tag, v);
    lds[L_PART + tid] = __uint_as_float(v[0]);
    if (wave == 0) { sweep<1>(g3 + (size_t)w * 576 + 512 + tid, 0, tag, v); lds[L_PART + 512 + tid] = __uint_as_float(v[0]); } }
  bar();
  STAMP(7);
  if (tid < 32) {
    const int bb = tid >> 4, d = tid & 15;
    const float* pp = lds + L_PART + bb * 16 * 18;
    float M = -INFINITY;
#pragma unroll
    for (int ss = 0; ss < 16; ++ss) M = fmaxf(M, pp[ss * 18]);
    float L = 0.f, o0 = 0.f;
#pragma unroll
    for (int ss = 0; ss < 16; ++ss) { const float wgt = __builtin_amdgcn_exp2f(pp[ss * 18] - M);
      L = fmaf(wgt, pp[ss * 18 + 1], L); o0 = fmaf(wgt, pp[ss * 18 + 2 + d], o0); }
    const unsigned ob = cvtpk(o0 * frcp(L), 0.f) & 0xffffu;
    const unsigned other = __shfl_down(ob, 16);
    if (tid < 16) put(g4 + w * 16 + tid, tag, ob | (other << 16));
  }
  STAMP(8);
  float x1own0 = 0.f, x1own1 = 0.f;
  { unsigned ov[8]; sweep<8>(g4 + tid, 512, tag, ov);
    STAMP(9);
    float a00 = 0.f, a01 = 0.f, a10 = 0.f, a11 = 0.f;
#pragma unroll
    for (int i = 0; i < 8; ++i) {
      const unsigned wv = ((const unsigned*)(lds + L_WST))[((i >> 2) * 512 + tid) * 4 + (i & 3)];
      const float w0 = bflo(wv), w1 = bfhi(wv), o0 = bflo(ov[i]), o1 = bfhi(ov[i]);
      a00 = fmaf(o0, w0, a00); a01 = fmaf(o0, w1, a01); a10 = fmaf(o1, w0, a10); a11 = fmaf(o1, w1, a11);
    }
    a00 = wave_sum(a00); a01 = wave_sum(a01); a10 = wave_sum(a10); a11 = wave_sum(a11);
    if (lane == 0) *reinterpret_cast<f32x4*>(lds + L_RED + wave * 4) = f32x4{a00, a01, a10, a11};
    bar();
    if (tid < 2) {
      float a0 = 0.f, a1 = 0.f;
#pragma unroll
      for (int ww = 0; ww < NW; ++ww) { a0 += lds[L_RED + ww * 4 + tid]; a1 += lds[L_RED + ww * 4 + 2 + tid]; }
      const int n = col0 + tid;
      const unsigned ga = ((const unsigned*)(lds + L_MODS))[1024 + tid];
      x1own0 = rbf(bf2f(xs[n]) + rbf(bflo(ga) * rbf(a0)));
      x1own1 = rbf(bf2f(xs[512 + n]) + rbf(bfhi(ga) * rbf(a1)));
      put(g5 + n, tag, cvtpk(x1own0, x1own1));
    } }
  STAMP(10);
  { unsigned v[1]; sweep<1>(g5 + tid, 0, tag, v);
    STAMP(11);
    const float y0 = bflo(v[0]), y1 = bfhi(v[0]);
    lds[L_X + tid] = y0; lds[L_X + 512 + tid] = y1;
    bar();
    if (has_gu) {
      float m0, m1; ln_norm(lds, lane, y0, y1, m0, m1);
      const unsigned sh = ((const unsigned*)(lds + L_MODS))[1536 + tid], sc = ((const unsigned*)(lds + L_MODS))[2048 + tid];
      const float a0 = rbf(rbf(m0 * rbf(1.f + bflo(sc))) + bflo(sh)), a1 = rbf(rbf(m1 * rbf(1.f + bfhi(sc))) + bfhi(sh));
      const u32x4 wgv = *reinterpret_cast<const u32x4*>(lds + L_WST + (2 * 512 + tid) * 4), wuv = *reinterpret_cast<const u32x4*>(lds + L_WST + (3 * 512 + tid) * 4);
      float v2[32];
#pragma unroll
      for (int j = 0; j < 4; ++j) {
        v2[2 * j] = a0 * bflo(wgv[j]); v2[2 * j + 1] = a0 * bfhi(wgv[j]); v2[8 + 2 * j] = a0 * bflo(wuv[j]); v2[8 + 2 * j + 1] = a0 * bfhi(wuv[j]);
        v2[16 + 2 * j] = a1 * bflo(wgv[j]); v2[16 + 2 * j + 1] = a1 * bfhi(wgv[j]); v2[24 + 2 * j] = a1 * bflo(wuv[j]); v2[24 + 2 * j + 1] = a1 * bfhi(wuv[j]);
      }
#pragma unroll
      for (int h = 0; h < 16; ++h) { auto sw = __builtin_amdgcn_permlane32_swap(__float_as_uint(v2[h]), __float_as_uint(v2[h + 16]), false, false); v2[h] = __uint_as_float(sw[0]) + __uint_as_float(sw[1]); }
#pragma unroll
      for (int h = 0; h < 8; ++h) { auto sw = __builtin_amdgcn_permlane16_swap(__float_as_uint(v2[h]), __float_as_uint(v2[h + 8]), false, false); v2[h] = __uint_as_float(sw[0]) + __uint_as_float(sw[1]); }
#define TRS(HALF, CTRL, BIT) _Pragma("unroll") for (int h = 0; h < (HALF); ++h) { \
      const float lo_ = v2[h], hi_ = v2[h + (HALF)]; const float t1_ = lo_ + dpp<CTRL>(lo_), t2_ = hi_ + dpp<CTRL>(hi_); \
      v2[h] = (lane & (BIT)) ? t2_ : t1_; }
      TRS(4, XOR8, 8) TRS(2, XOR7, 4) TRS(1, XOR2, 2)
#undef TRS
      v2[0] += dpp<XOR1>(v2[0]);
      if ((lane & 1) == 0) lds[L_RED + wave * 32 + (lane >> 1)] = v2[0];
    }
    bar();
    if (has_gu && tid < 8) {
      float g0 = 0.f, u0 = 0.f, g1s = 0.f, u1 = 0.f;
#pragma unroll
      for (int ww = 0; ww < NW; ++ww) { g0 += lds[L_RED + ww * 32 + tid]; u0 += lds[L_RED + ww * 32 + 8 + tid]; g1s += lds[L_RED + ww * 32 + 16 + tid]; u1 += lds[L_RED + ww * 32 + 24 + tid]; }
      put(g6 + 8 * wgu + tid, tag, cvtpk(rbf(g0) * rbf(u0), rbf(g1s) * rbf(u1)));
    } }
  STAMP(12);
  { unsigned hv[4];
    for (unsigned n = 0;; ++n) {
      bool ok = true;
#pragma unroll
      for (int i = 0; i < 4; ++i) { const int k = tid + 512 * i; const u64 a = __hip_atomic_load(g6 + (k < F ? k : 0), __ATOMIC_RELAXED, __HIP_MEMORY_SCOPE_AGENT); hv[i] = k < F ? (unsigned)a : 0u; ok &= (unsigned)(a >> 32) == tag; }
      if (__all(ok) || n > SPIN_CAP) break;
      __builtin_amdgcn_s_sleep(1);
    }
    STAMP(13);
    float a00 = 0.f, a01 = 0.f, a10 = 0.f, a11 = 0.f;
    const u32x4 wdv = *reinterpret_cast<const u32x4*>(lds + L_WST + (4 * 512 + tid) * 4);
#pragma unroll
    for (int i = 0; i < 4; ++i) {
      const float w0 = bflo(wdv[i]), w1 = bfhi(wdv[i]), o0 = bflo(hv[i]), o1 = bfhi(hv[i]);
      a00 = fmaf(o0, w0, a00); a01 = fmaf(o0, w1, a01); a10 = fmaf(o1, w0, a10); a11 = fmaf(o1, w1, a11);
    }
    a00 = wave_sum(a00); a01 = wave_sum(a01); a10 = wave_sum(a10); a11 = wave_sum(a11);
    if (lane == 0) *reinterpret_cast<f32x4*>(lds + L_RED + wave * 4) = f32x4{a00, a01, a10, a11};
    bar();
    if (tid < 2) {
      float a0 = 0.f, a1 = 0.f;
#pragma unroll
      for (int ww = 0; ww < NW; ++ww) { a0 += lds[L_RED + ww * 4 + tid]; a1 += lds[L_RED + ww * 4 + 2 + tid]; }
      const int n = col0 + tid;
      const unsigned gb = ((const unsigned*)(lds + L_MODS))[1026 + tid];
      out[n] = __float2bfloat16(x1own0 + rbf(bflo(gb) * rbf(a0)));
      out[512 + n] = __float2bfloat16(x1own1 + rbf(bfhi(gb) * rbf(a1)));
    }
    if (w == 0 && tid == 0) __hip_atomic_store((unsigned*)(ws + WS_EPOCH), tag, __ATOMIC_RELAXED, __HIP_MEMORY_SCOPE_AGENT);
  }
  STAMP(14);
}

extern "C" void kernel_launch(void* const* d_in, const int* in_sizes, int n_in, void* d_out, int out_size, void* d_ws, size_t ws_size, hipStream_t stream) {
  (void)hipFuncSetAttribute((const void*)mega, hipFuncAttributeMaxDynamicSharedMemorySize, L_TOTAL * 4);
  mega<<<NWG, NT, L_TOTAL * 4, stream>>>((const bf16*)d_in[0], (const bf16*)d_in[1], (const bf16*)d_in[2], (const bf16*)d_in[3], (const bf16*)d_in[4],
                                        (const bf16*)d_in[6], (const bf16*)d_in[7], (const bf16*)d_in[8], (const bf16*)d_in[9], (const bf16*)d_in[10],
                                        (bf16*)d_out, (char*)d_ws);
}
```

```cpp
#include <hip/hip_runtime.h>
#include <hip/hip_bf16.h>
#include <cstdint>

using bf16 = __hip_bfloat16;
typedef unsigned long long u64;
constexpr int B = 2, DM = 512, HQ = 32, HKV = 8, G = 4, SK = 32768, DH = 128, F = 1792;
constexpr int NQ = HQ * DH;
constexpr int NWG = 256, NT = 512, NW = 8;
constexpr int NSPLIT = 16, PAGE = 16;
constexpr int NPG = SK / PAGE;
constexpr int CH = 4;
constexpr int NSTATIC = 112;
constexpr int NGU = F / 8;
constexpr unsigned SPIN_CAP = 1u << 22;

using u32x4 = __attribute__((ext_vector_type(4))) unsigned;
using f32x4 = __attribute__((ext_vector_type(4))) float;
using bf16x2 = __attribute__((ext_vector_type(2))) __bf16;

constexpr size_t WS_G1 = 0;
constexpr size_t WS_G2 = 32768;
constexpr size_t WS_G4 = 65536;
constexpr size_t WS_G5 = 98304;
constexpr size_t WS_G6 = 102400;
constexpr size_t WS_EPOCH = 131072;
constexpr size_t WS_CNT = 135168;
constexpr size_t WS_G3 = 1u << 20;
constexpr size_t WS_TS = 8u << 20;

__device__ __forceinline__ float bf2f(unsigned h) { return __builtin_bit_cast(float, h << 16); }
__device__ __forceinline__ float bflo(unsigned w) { return __builtin_bit_cast(float, w << 16); }
__device__ __forceinline__ float bfhi(unsigned w) { return __builtin_bit_cast(float, w & 0xffff0000u); }
typedef __attribute__((ext_vector_type(2))) float f32x2;
__device__ __forceinline__ unsigned cvtpk(float lo, float hi) {
  return __builtin_bit_cast(unsigned, __builtin_convertvector(f32x2{lo, hi}, bf16x2));
}
__device__ __forceinline__ float rbf(float f) { return __builtin_bit_cast(float, cvtpk(f, f) << 16); }
__device__ __forceinline__ float frcp(float x) { return __builtin_amdgcn_rcpf(x); }
__device__ __forceinline__ float dot2(unsigned k, unsigned q, float acc) {
  return __builtin_amdgcn_fdot2_f32_bf16(__builtin_bit_cast(bf16x2, k), __builtin_bit_cast(bf16x2, q), acc, false);
}
template <int CTRL> __device__ __forceinline__ float dpp(float x) {
  return __builtin_bit_cast(float, __builtin_amdgcn_mov_dpp(__builtin_bit_cast(int, x), CTRL, 0xf, 0xf, true));
}
constexpr int XOR1 = 0xB1, XOR2 = 0x4E, XOR7 = 0x141, XOR8 = 0x128, ROR4 = 0x124;
__device__ __forceinline__ float xrow16_max(float x) {
  auto s = __builtin_amdgcn_permlane16_swap(__float_as_uint(x), __float_as_uint(x), false, false);
  x = fmaxf(__uint_as_float(s[0]), __uint_as_float(s[1]));
  auto t = __builtin_amdgcn_permlane32_swap(__float_as_uint(x), __float_as_uint(x), false, false);
  return fmaxf(__uint_as_float(t[0]), __uint_as_float(t[1]));
}
__device__ __forceinline__ float xrow16_sum(float x) {
  auto s = __builtin_amdgcn_permlane16_swap(__float_as_uint(x), __float_as_uint(x), false, false);
  x = __uint_as_float(s[0]) + __uint_as_float(s[1]);
  auto t = __builtin_amdgcn_permlane32_swap(__float_as_uint(x), __float_as_uint(x), false, false);
  return __uint_as_float(t[0]) + __uint_as_float(t[1]);
}
__device__ __forceinline__ float wave_sum(float x) {
  x += dpp<XOR1>(x); x += dpp<XOR2>(x); x += dpp<XOR7>(x); x += dpp<XOR8>(x);
  return xrow16_sum(x);
}
__device__ __forceinline__ float par_sum(float x) {
  x += dpp<XOR2>(x); x += dpp<ROR4>(x); x += dpp<XOR8>(x);
  return xrow16_sum(x);
}
__device__ __forceinline__ void bar() { asm volatile("s_waitcnt lgkmcnt(0)\n\ts_barrier" ::: "memory"); }
__device__ __forceinline__ void put(u64* p, unsigned tag, unsigned v) {
  __hip_atomic_store(p, ((u64)tag << 32) | v, __ATOMIC_RELAXED, __HIP_MEMORY_SCOPE_AGENT);
}
template <int N>
__device__ __forceinline__ void sweep(const u64* p, int stride, unsigned tag, unsigned (&v)[N]) {
  for (unsigned n = 0;; ++n) {
    bool ok = true;
#pragma unroll
    for (int k = 0; k < N; ++k) { const u64 a = __hip_atomic_load(p + k * stride, __ATOMIC_RELAXED, __HIP_MEMORY_SCOPE_AGENT); v[k] = (unsigned)a; ok &= (unsigned)(a >> 32) == tag; }
    if (__all(ok) || n > SPIN_CAP) break;
    __builtin_amdgcn_s_sleep(1);
  }
}

#define STAMP(i) do { } while (0)

constexpr int L_A = 0;
constexpr int L_X = 1024;
constexpr int L_RED = 2048;
constexpr int L_RES = 2304;
constexpr int L_Q = 2368;
constexpr int L_P = 2624;
constexpr int L_TICK = 3136;
constexpr int L_MODS = 3140;
constexpr int L_PART = L_MODS + 3072;
constexpr int L_WST = L_PART + 640;
constexpr int L_MRG = L_WST + 20 * 512;
constexpr int L_TOTAL = L_MRG + 4224 + 6144;

__device__ __forceinline__ void ln_norm(const float* lds, int lane, float x0, float x1, float& n0, float& n1) {
#pragma unroll
  for (int bb = 0; bb < 2; ++bb) {
    float v[8], s = 0.f;
#pragma unroll
    for (int j = 0; j < 8; ++j) { v[j] = lds[L_X + bb * 512 + lane + 64 * j]; s += v[j]; }
    const float mu = wave_sum(s) * (1.f / DM);
    float q = 0.f;
#pragma unroll
    for (int j = 0; j < 8; ++j) { const float d = v[j] - mu; q += d * d; }
    const float var = rbf(wave_sum(q) * (1.f / DM));
    const float sd = rbf(__builtin_amdgcn_sqrtf(rbf(var + 1e-5f)));
    const float t = rbf(rbf((bb ? x1 : x0) - rbf(mu)) * frcp(sd));
    if (bb) n1 = t; else n0 = t;
  }
}
__device__ __forceinline__ void gemv16_part(float* lds, int tid, const u32x4* wv) {
  const int lane = tid & 63, wave = tid >> 6;
  float acc[16];
#pragma unroll
  for (int j = 0; j < 16; ++j) acc[j] = 0.f;
#pragma unroll
  for (int i = 0; i < 2; ++i) {
    const int row = (tid >> 1) + 256 * i;
    const float a0 = lds[L_A + row], a1 = lds[L_A + 512 + row];
#pragma unroll
    for (int j = 0; j < 4; ++j) {
      const float lo = bflo(wv[i][j]), hi = bfhi(wv[i][j]);
      acc[2 * j] = fmaf(a0, lo, acc[2 * j]); acc[2 * j + 1] = fmaf(a0, hi, acc[2 * j + 1]);
      acc[8 + 2 * j] = fmaf(a1, lo, acc[8 + 2 * j]); acc[8 + 2 * j + 1] = fmaf(a1, hi, acc[8 + 2 * j + 1]);
    }
  }
#pragma unroll
  for (int h = 0; h < 8; ++h) { auto sw = __builtin_amdgcn_permlane32_swap(__float_as_uint(acc[h]), __float_as_uint(acc[h + 8]), false, false); acc[h] = __uint_as_float(sw[0]) + __uint_as_float(sw[1]); }
#pragma unroll
  for (int h = 0; h < 4; ++h) { auto sw = __builtin_amdgcn_permlane16_swap(__float_as_uint(acc[h]), __float_as_uint(acc[h + 4]), false, false); acc[h] = __uint_as_float(sw[0]) + __uint_as_float(sw[1]); }
#pragma unroll
  for (int h = 0; h < 4; ++h) { float v = acc[h]; v += dpp<XOR2>(v); v += dpp<ROR4>(v); v += dpp<XOR8>(v); acc[h] = v; }
  if ((lane & 14) == 0) *reinterpret_cast<f32x4*>(lds + L_RED + wave * 32 + (lane & 1) * 16 + (lane >> 4) * 4) = f32x4{acc[0], acc[1], acc[2], acc[3]};
}
__device__ __forceinline__ unsigned gemv16_fin(const float* lds, int t) {
  const int half = t >> 3, j = t & 7;
  float s0 = 0.f, s1 = 0.f;
#pragma unroll
  for (int ww = 0; ww < 8; ++ww) { s0 += lds[L_RED + ww * 32 + half * 16 + j]; s1 += lds[L_RED + ww * 32 + half * 16 + 8 + j]; }
  return cvtpk(s0, s1);
}

__global__ void __launch_bounds__(NT, 2) mega(const bf16* __restrict__ x, const bf16* __restrict__ kc, const bf16* __restrict__ vc,
                                              const bf16* __restrict__ wq, const bf16* __restrict__ wo, const bf16* __restrict__ wg,
                                              const bf16* __restrict__ wu, const bf16* __restrict__ wd, const bf16* __restrict__ c,
                                              const bf16* __restrict__ w_mod, bf16* __restrict__ out, char* __restrict__ ws) {
  extern __shared__ __attribute__((aligned(16))) float lds[];
  const int tid = threadIdx.x, lane = tid & 63, wave = __builtin_amdgcn_readfirstlane(tid >> 6);
  const int w = blockIdx.x;
  const unsigned tag = __hip_atomic_load((const unsigned*)(ws + WS_EPOCH), __ATOMIC_RELAXED, __HIP_MEMORY_SCOPE_AGENT) + 1u;
  u64* g1 = (u64*)(ws + WS_G1); u64* g2 = (u64*)(ws + WS_G2); u64* g3 = (u64*)(ws + WS_G3);
  u64* g4 = (u64*)(ws + WS_G4); u64* g5 = (u64*)(ws + WS_G5); u64* g6 = (u64*)(ws + WS_G6);
  u64* ts = (u64*)(ws + WS_TS); (void)ts;
  const int pair = w >> 4, b = pair >> 3, kvh = pair & 7, s = w & 15;
  const int r = lane >> 4, cc = lane & 15;
  const int col0 = 2 * ((w & 7) * 32 + (w >> 3));
  STAMP(0);
  const unsigned short* cs = (const unsigned short*)c; const unsigned short* xs = (const unsigned short*)x;
  const int wx = w & 7, wi = w >> 3;
  const bool has_mod = wi < 24;
  const int mslice = wi < 8 ? wx * 8 + wi : (wi < 24 ? 64 + wx * 16 + (wi - 8) : 0);
  const int qslice = wx * 32 + wi;
  const bool has_gu = wi < 28;
  const int wgu = has_gu ? wx * 28 + wi : 0;
  unsigned* cnt = (unsigned*)(ws + WS_CNT + (size_t)pair * 128);
  if (w == 0 && tid < 16) __hip_atomic_store((unsigned*)(ws + WS_CNT + (size_t)tid * 128), (unsigned)NSTATIC, __ATOMIC_RELAXED, __HIP_MEMORY_SCOPE_AGENT);
  const float c0 = bf2f(cs[tid]), c1 = bf2f(cs[512 + tid]);
  const float x0 = bf2f(xs[tid]), x1 = bf2f(xs[512 + tid]);
  u32x4 wm[2], wqv[2];
#pragma unroll
  for (int i = 0; i < 2; ++i) {
    const int row = (tid >> 1) + 256 * i;
    wm[i] = *reinterpret_cast<const u32x4*>(w_mod + (size_t)row * (6 * DM) + mslice * 16 + (tid & 1) * 8);
    wqv[i] = *reinterpret_cast<const u32x4*>(wq + (size_t)row * NQ + qslice * 16 + (tid & 1) * 8);
  }
  lds[L_A + tid] = rbf(c0 * frcp(1.f + __expf(-c0))); lds[L_A + 512 + tid] = rbf(c1 * frcp(1.f + __expf(-c1)));
  lds[L_X + tid] = x0; lds[L_X + 512 + tid] = x1;
  bar();
  gemv16_part(lds, tid, wm);
  float n0, n1;
  ln_norm(lds, lane, x0, x1, n0, n1);
  bar();
  if (has_mod && tid < 16) { const unsigned rr = gemv16_fin(lds, tid); asm volatile("s_waitcnt vmcnt(0)" ::: "memory"); put(g1 + mslice * 16 + tid, tag, rr); }
  STAMP(1);
  const size_t base = ((size_t)(b * HKV + kvh) * SK) * DH + (size_t)r * DH + 8 * cc;
  const bf16* kb = kc + base; const bf16* vb = vc + base;
  u32x4 KA[4], VA[4], KB[4], VB[4];
#define ISSUE(KX, VX, pg_) do { const size_t off_ = (size_t)(pg_) * PAGE * DH; \
    _Pragma("unroll") for (int t_ = 0; t_ < 4; ++t_) { KX[t_] = __builtin_nontemporal_load(reinterpret_cast<const u32x4*>(kb + off_ + t_ * 4 * DH)); VX[t_] = __builtin_nontemporal_load(reinterpret_cast<const u32x4*>(vb + off_ + t_ * 4 * DH)); } } while (0)
  unsigned st[20];
#define STASH_LOAD() do { \
    _Pragma("unroll") for (int i = 0; i < 8; ++i) st[i] = *reinterpret_cast<const unsigned*>(wo + (size_t)(tid + 512 * i) * DM + col0); \
    { const u32x4 a_ = *reinterpret_cast<const u32x4*>(wg + (size_t)tid * F + 8 * wgu); st[8] = a_[0]; st[9] = a_[1]; st[10] = a_[2]; st[11] = a_[3]; } \
    { const u32x4 a_ = *reinterpret_cast<const u32x4*>(wu + (size_t)tid * F + 8 * wgu); st[12] = a_[0]; st[13] = a_[1]; st[14] = a_[2]; st[15] = a_[3]; } \
    _Pragma("unroll") for (int i = 0; i < 4; ++i) { const int k_ = tid + 512 * i; st[16 + i] = *reinterpret_cast<const unsigned*>(wd + (size_t)(k_ < F ? k_ : 0) * DM + col0); } } while (0)
#define STASH_STORE() do { _Pragma("unroll") for (int i = 0; i < 20; i += 4) *reinterpret_cast<u32x4*>(lds + L_WST + (i * 128 + tid) * 4 - 0 + 0 * i) = u32x4{st[i], st[i + 1], st[i + 2], st[i + 3]}; } while (0)
  int pA = (s * 7 + wave - 1) * CH, pB = pA + 1;
#define TICKET() ((int)__builtin_amdgcn_readfirstlane(lane == 0 ? __hip_atomic_fetch_add(cnt, 1u, __ATOMIC_RELAXED, __HIP_MEMORY_SCOPE_AGENT) : 0u))
  if (wave != 0) {
    ISSUE(KA, VA, pA);
  } else {
    unsigned v[16];
    sweep<16>(g1 + lane, 64, tag, v);
#pragma unroll
    for (int j = 0; j < 16; ++j) ((unsigned*)(lds + L_MODS))[lane + 64 * j] = v[j];
  }
  bar();
  STAMP(2);
  { const unsigned sh = ((const unsigned*)(lds + L_MODS))[tid], sc = ((const unsigned*)(lds + L_MODS))[512 + tid];
    lds[L_A + tid] = rbf(rbf(n0 * rbf(1.f + bflo(sc))) + bflo(sh));
    lds[L_A + 512 + tid] = rbf(rbf(n1 * rbf(1.f + bfhi(sc))) + bfhi(sh)); }
  bar();
  gemv16_part(lds, tid, wqv);
  bar();
  if (wave != 0) {
    ISSUE(KB, VB, pB);
  } else {
    if (tid < 16) put(g2 + qslice * 16 + tid, tag, gemv16_fin(lds, tid));
    STAMP(3);
    { unsigned v[16];
      sweep<16>(g1 + 1536 + lane, 64, tag, v);
#pragma unroll
      for (int j = 0; j < 16; ++j) ((unsigned*)(lds + L_MODS))[1536 + lane + 64 * j] = v[j]; }
    { unsigned v[1];
      sweep<1>(g1 + (lane < 2 ? 1024 + col0 + lane : 2560 + col0 + (lane & 1)), 0, tag, v);
      if (lane < 4) ((unsigned*)(lds + L_MODS))[1024 + lane] = v[0]; }
    unsigned qq[8];
    sweep<8>(g2 + kvh * 512 + lane, 64, tag, qq);
#pragma unroll
    for (int j = 0; j < 8; ++j) ((unsigned short*)(lds + L_Q))[lane + 64 * j] = (unsigned short)(b ? (qq[j] >> 16) : (qq[j] & 0xffffu));
    pA = TICKET() * CH;
    pB = pA + 1;
    if (pA < NPG) ISSUE(KA, VA, pA);
    if (pB < NPG) ISSUE(KB, VB, pB);
  }
  bar();
  STAMP(4);
  u32x4 qpk[4];
#pragma unroll
  for (int g = 0; g < 4; ++g) qpk[g] = *reinterpret_cast<const u32x4*>((const unsigned short*)(lds + L_Q) + g * DH + 8 * cc);
  constexpr float C2 = 0.088388347648318440f * 1.4426950408889634f;
  float m = -INFINITY, l = 0.f, acc[32];
#pragma unroll
  for (int k = 0; k < 32; ++k) acc[k] = 0.f;
  float* prow = lds + L_P + (wave * 4 + r) * 16;
#define TR_STEP(HALF, CTRL, BIT) _Pragma("unroll") for (int h = 0; h < (HALF); ++h) { \
      const float lo_ = xx[h], hi_ = xx[h + (HALF)]; const float t1_ = lo_ + dpp<CTRL>(lo_), t2_ = hi_ + dpp<CTRL>(hi_); \
      xx[h] = (cc & (BIT)) ? t2_ : t1_; }
#define PROCESS(KX, VX) do { \
    float xx[16]; \
    _Pragma("unroll") for (int t = 0; t < 4; ++t) _Pragma("unroll") for (int g = 0; g < 4; ++g) { float d = 0.f; \
        _Pragma("unroll") for (int k = 0; k < 4; ++k) d = dot2(KX[t][k], qpk[g][k], d); xx[t * 4 + g] = d; } \
    TR_STEP(8, XOR8, 8) TR_STEP(4, XOR7, 4) TR_STEP(2, XOR2, 2) TR_STEP(1, XOR1, 1) \
    const float sc = xx[0] * C2; \
    float pm = sc; pm = fmaxf(pm, dpp<ROR4>(pm)); pm = fmaxf(pm, dpp<XOR8>(pm)); pm = xrow16_max(pm); \
    const float mn = fmaxf(m, pm); \
    const float alpha = __builtin_amdgcn_exp2f(m - mn), p = __builtin_amdgcn_exp2f(sc - mn); \
    l = fmaf(l, alpha, p); m = mn; \
    _Pragma("unroll") for (int q4 = 0; q4 < 4; ++q4) { \
      const float al = q4 == 0 ? dpp<0x00>(alpha) : q4 == 1 ? dpp<0x55>(alpha) : q4 == 2 ? dpp<0xAA>(alpha) : dpp<0xFF>(alpha); \
      _Pragma("unroll") for (int k = 0; k < 8; ++k) acc[q4 * 8 + k] *= al; } \
    prow[cc] = p; \
    asm volatile("s_waitcnt lgkmcnt(0)" ::: "memory"); \
    f32x4 pr[4]; \
    _Pragma("unroll") for (int u = 0; u < 4; ++u) pr[u] = *reinterpret_cast<const f32x4*>(prow + 4 * u); \
    _Pragma("unroll") for (int t = 0; t < 4; ++t) { \
      float vf[8]; \
      _Pragma("unroll") for (int k = 0; k < 4; ++k) { vf[2 * k] = bflo(VX[t][k]); vf[2 * k + 1] = bfhi(VX[t][k]); } \
      _Pragma("unroll") for (int g = 0; g < 4; ++g) { const float pg_ = pr[t][g]; \
        _Pragma("unroll") for (int k = 0; k < 8; ++k) acc[g * 8 + k] = fmaf(pg_, vf[k], acc[g * 8 + k]); } } \
  } while (0)
#define STASH(it_) do { \
    if ((it_) == 1) { unsigned st[8]; \
      _Pragma("unroll") for (int i = 0; i < 8; ++i) st[i] = *reinterpret_cast<const unsigned*>(wo + (size_t)(tid + 512 * i) * DM + col0); \
      *reinterpret_cast<u32x4*>(lds + L_WST + (0 * 512 + tid) * 4) = u32x4{st[0], st[1], st[2], st[3]}; \
      *reinterpret_cast<u32x4*>(lds + L_WST + (1 * 512 + tid) * 4) = u32x4{st[4], st[5], st[6], st[7]}; \
    } else if ((it_) == 2) { \
      const u32x4 a_ = *reinterpret_cast<const u32x4*>(wg + (size_t)tid * F + 8 * wgu), b_ = *reinterpret_cast<const u32x4*>(wu + (size_t)tid * F + 8 * wgu); \
      *reinterpret_cast<u32x4*>(lds + L_WST + (2 * 512 + tid) * 4) = a_; \
      *reinterpret_cast<u32x4*>(lds + L_WST + (3 * 512 + tid) * 4) = b_; \
    } else if ((it_) == 3) { unsigned st[4]; \
      _Pragma("unroll") for (int i = 0; i < 4; ++i) { const int k_ = tid + 512 * i; st[i] = *reinterpret_cast<const unsigned*>(wd + (size_t)(k_ < F ? k_ : 0) * DM + col0); } \
      *reinterpret_cast<u32x4*>(lds + L_WST + (4 * 512 + tid) * 4) = u32x4{st[0], st[1], st[2], st[3]}; \
    } } while (0)
  int nxt = TICKET();
  int it = 0;
  for (;; ++it) {
    if (pA >= NPG) break;
    PROCESS(KA, VA);
    if ((pB & (CH - 1)) != CH - 1) pA = pB + 1; else { pA = nxt * CH; if (pA < NPG) nxt = TICKET(); }
    if (pA < NPG) ISSUE(KA, VA, pA);
    STASH(it);
    if (pB >= NPG) break;
    PROCESS(KB, VB);
    pB = pA + 1;
    if (pB < NPG) ISSUE(KB, VB, pB);
  }
  for (int j = it; j <= 3; ++j) STASH(j);
#undef PROCESS
#undef TR_STEP
#undef ISSUE
  STAMP(5);
#pragma unroll
  for (int k = 0; k < 32; ++k) acc[k] = xrow16_sum(acc[k]);
  { float t = l; t += dpp<ROR4>(t); t += dpp<XOR8>(t); l = xrow16_sum(t); }
  float* mrg = lds + L_MRG;
  if (r == 0) {
#pragma unroll
    for (int g = 0; g < 4; ++g) {
      float* rec = mrg + (wave * 4 + g) * 132;
#pragma unroll
      for (int k = 0; k < 8; k += 4) *reinterpret_cast<f32x4*>(rec + 4 + 8 * cc + k) = f32x4{acc[g * 8 + k], acc[g * 8 + k + 1], acc[g * 8 + k + 2], acc[g * 8 + k + 3]};
      if (cc == g) { rec[0] = m; rec[1] = l; } } }
  bar();
  { const int g = tid >> 7, d = tid & 127;
    float M = -INFINITY;
#pragma unroll
    for (int ww = 0; ww < NW; ++ww) M = fmaxf(M, mrg[(ww * 4 + g) * 132]);
    float L = 0.f, o0 = 0.f;
#pragma unroll
    for (int ww = 0; ww < NW; ++ww) { const float* rec = mrg + (ww * 4 + g) * 132;
      const float wgt = __builtin_amdgcn_exp2f(rec[0] - M);
      L = fmaf(wgt, rec[1], L); o0 = fmaf(wgt, rec[4 + d], o0); }
    u64* dst = g3 + (size_t)((kvh * 4 + g) * 8 + (d >> 4)) * 576 + (b * 16 + s) * 18;
    put(dst + 2 + (d & 15), tag, __float_as_uint(o0));
    if ((d & 15) == 0) { put(dst, tag, __float_as_uint(M)); put(dst + 1, tag, __float_as_uint(L)); }
  }
  STAMP(6);
  { unsigned v[1]; sweep<1>(g3 + (size_t)w * 576 + tid, 0, tag, v);
    lds[L_PART + tid] = __uint_as_float(v[0]);
    if (wave == 0) { sweep<1>(g3 + (size_t)w * 576 + 512 + tid, 0, tag, v); lds[L_PART + 512 + tid] = __uint_as_float(v[0]); } }
  bar();
  STAMP(7);
  if (tid < 32) {
    const int bb = tid >> 4, d = tid & 15;
    const float* pp = lds + L_PART + bb * 16 * 18;
    float M = -INFINITY;
#pragma unroll
    for (int ss = 0; ss < 16; ++ss) M = fmaxf(M, pp[ss * 18]);
    float L = 0.f, o0 = 0.f;
#pragma unroll
    for (int ss = 0; ss < 16; ++ss) { const float wgt = __builtin_amdgcn_exp2f(pp[ss * 18] - M);
      L = fmaf(wgt, pp[ss * 18 + 1], L); o0 = fmaf(wgt, pp[ss * 18 + 2 + d], o0); }
    const unsigned ob = cvtpk(o0 * frcp(L), 0.f) & 0xffffu;
    const unsigned other = __shfl_down(ob, 16);
    if (tid < 16) put(g4 + w * 16 + tid, tag, ob | (other << 16));
  }
  STAMP(8);
  float x1own0 = 0.f, x1own1 = 0.f;
  { unsigned ov[8]; sweep<8>(g4 + tid, 512, tag, ov);
    STAMP(9);
    float a00 = 0.f, a01 = 0.f, a10 = 0.f, a11 = 0.f;
#pragma unroll
    for (int i = 0; i < 8; ++i) {
      const unsigned wv = ((const unsigned*)(lds + L_WST))[((i >> 2) * 512 + tid) * 4 + (i & 3)];
      const float w0 = bflo(wv), w1 = bfhi(wv), o0 = bflo(ov[i]), o1 = bfhi(ov[i]);
      a00 = fmaf(o0, w0, a00); a01 = fmaf(o0, w1, a01); a10 = fmaf(o1, w0, a10); a11 = fmaf(o1, w1, a11);
    }
    a00 = wave_sum(a00); a01 = wave_sum(a01); a10 = wave_sum(a10); a11 = wave_sum(a11);
    if (lane == 0) *reinterpret_cast<f32x4*>(lds + L_RED + wave * 4) = f32x4{a00, a01, a10, a11};
    bar();
    if (tid < 2) {
      float a0 = 0.f, a1 = 0.f;
#pragma unroll
      for (int ww = 0; ww < NW; ++ww) { a0 += lds[L_RED + ww * 4 + tid]; a1 += lds[L_RED + ww * 4 + 2 + tid]; }
      const int n = col0 + tid;
      const unsigned ga = ((const unsigned*)(lds + L_MODS))[1024 + tid];
      x1own0 = rbf(bf2f(xs[n]) + rbf(bflo(ga) * rbf(a0)));
      x1own1 = rbf(bf2f(xs[512 + n]) + rbf(bfhi(ga) * rbf(a1)));
      put(g5 + n, tag, cvtpk(x1own0, x1own1));
    } }
  STAMP(10);
  { unsigned v[1]; sweep<1>(g5 + tid, 0, tag, v);
    STAMP(11);
    const float y0 = bflo(v[0]), y1 = bfhi(v[0]);
    lds[L_X + tid] = y0; lds[L_X + 512 + tid] = y1;
    bar();
    if (has_gu) {
      float m0, m1; ln_norm(lds, lane, y0, y1, m0, m1);
      const unsigned sh = ((const unsigned*)(lds + L_MODS))[1536 + tid], sc = ((const unsigned*)(lds + L_MODS))[2048 + tid];
      const float a0 = rbf(rbf(m0 * rbf(1.f + bflo(sc))) + bflo(sh)), a1 = rbf(rbf(m1 * rbf(1.f + bfhi(sc))) + bfhi(sh));
      const u32x4 wgv = *reinterpret_cast<const u32x4*>(lds + L_WST + (2 * 512 + tid) * 4), wuv = *reinterpret_cast<const u32x4*>(lds + L_WST + (3 * 512 + tid) * 4);
      float v2[32];
#pragma unroll
      for (int j = 0; j < 4; ++j) {
        v2[2 * j] = a0 * bflo(wgv[j]); v2[2 * j + 1] = a0 * bfhi(wgv[j]); v2[8 + 2 * j] = a0 * bflo(wuv[j]); v2[8 + 2 * j + 1] = a0 * bfhi(wuv[j]);
        v2[16 + 2 * j] = a1 * bflo(wgv[j]); v2[16 + 2 * j + 1] = a1 * bfhi(wgv[j]); v2[24 + 2 * j] = a1 * bflo(wuv[j]); v2[24 + 2 * j + 1] = a1 * bfhi(wuv[j]);
      }
#pragma unroll
      for (int h = 0; h < 16; ++h) { auto sw = __builtin_amdgcn_permlane32_swap(__float_as_uint(v2[h]), __float_as_uint(v2[h + 16]), false, false); v2[h] = __uint_as_float(sw[0]) + __uint_as_float(sw[1]); }
#pragma unroll
      for (int h = 0; h < 8; ++h) { auto sw = __builtin_amdgcn_permlane16_swap(__float_as_uint(v2[h]), __float_as_uint(v2[h + 8]), false, false); v2[h] = __uint_as_float(sw[0]) + __uint_as_float(sw[1]); }
#define TRS(HALF, CTRL, BIT) _Pragma("unroll") for (int h = 0; h < (HALF); ++h) { \
      const float lo_ = v2[h], hi_ = v2[h + (HALF)]; const float t1_ = lo_ + dpp<CTRL>(lo_), t2_ = hi_ + dpp<CTRL>(hi_); \
      v2[h] = (lane & (BIT)) ? t2_ : t1_; }
      TRS(4, XOR8, 8) TRS(2, XOR7, 4) TRS(1, XOR2, 2)
#undef TRS
      v2[0] += dpp<XOR1>(v2[0]);
      if ((lane & 1) == 0) lds[L_RED + wave * 32 + (lane >> 1)] = v2[0];
    }
    bar();
    if (has_gu && tid < 8) {
      float g0 = 0.f, u0 = 0.f, g1s = 0.f, u1 = 0.f;
#pragma unroll
      for (int ww = 0; ww < NW; ++ww) { g0 += lds[L_RED + ww * 32 + tid]; u0 += lds[L_RED + ww * 32 + 8 + tid]; g1s += lds[L_RED + ww * 32 + 16 + tid]; u1 += lds[L_RED + ww * 32 + 24 + tid]; }
      put(g6 + 8 * wgu + tid, tag, cvtpk(rbf(g0) * rbf(u0), rbf(g1s) * rbf(u1)));
    } }
  STAMP(12);
  { unsigned hv[4];
    for (unsigned n = 0;; ++n) {
      bool ok = true;
#pragma unroll
      for (int i = 0; i < 4; ++i) { const int k = tid + 512 * i; const u64 a = __hip_atomic_load(g6 + (k < F ? k : 0), __ATOMIC_RELAXED, __HIP_MEMORY_SCOPE_AGENT); hv[i] = k < F ? (unsigned)a : 0u; ok &= (unsigned)(a >> 32) == tag; }
      if (__all(ok) || n > SPIN_CAP) break;
      __builtin_amdgcn_s_sleep(1);
    }
    STAMP(13);
    float a00 = 0.f, a01 = 0.f, a10 = 0.f, a11 = 0.f;
    const u32x4 wdv = *reinterpret_cast<const u32x4*>(lds + L_WST + (4 * 512 + tid) * 4);
#pragma unroll
    for (int i = 0; i < 4; ++i) {
      const float w0 = bflo(wdv[i]), w1 = bfhi(wdv[i]), o0 = bflo(hv[i]), o1 = bfhi(hv[i]);
      a00 = fmaf(o0, w0, a00); a01 = fmaf(o0, w1, a01); a10 = fmaf(o1, w0, a10); a11 = fmaf(o1, w1, a11);
    }
    a00 = wave_sum(a00); a01 = wave_sum(a01); a10 = wave_sum(a10); a11 = wave_sum(a11);
    if (lane == 0) *reinterpret_cast<f32x4*>(lds + L_RED + wave * 4) = f32x4{a00, a01, a10, a11};
    bar();
    if (tid < 2) {
      float a0 = 0.f, a1 = 0.f;
#pragma unroll
      for (int ww = 0; ww < NW; ++ww) { a0 += lds[L_RED + ww * 4 + tid]; a1 += lds[L_RED + ww * 4 + 2 + tid]; }
      const int n = col0 + tid;
      const unsigned gb = ((const unsigned*)(lds + L_MODS))[1026 + tid];
      out[n] = __float2bfloat16(x1own0 + rbf(bflo(gb) * rbf(a0)));
      out[512 + n] = __float2bfloat16(x1own1 + rbf(bfhi(gb) * rbf(a1)));
    }
    if (w == 0 && tid == 0) __hip_atomic_store((unsigned*)(ws + WS_EPOCH), tag, __ATOMIC_RELAXED, __HIP_MEMORY_SCOPE_AGENT);
  }
  STAMP(14);
}

extern "C" void kernel_launch(void* const* d_in, const int* in_sizes, int n_in, void* d_out, int out_size, void* d_ws, size_t ws_size, hipStream_t stream) {
  (void)hipFuncSetAttribute((const void*)mega, hipFuncAttributeMaxDynamicSharedMemorySize, L_TOTAL * 4);
  mega<<<NWG, NT, L_TOTAL * 4, stream>>>((const bf16*)d_in[0], (const bf16*)d_in[1], (const bf16*)d_in[2], (const bf16*)d_in[3], (const bf16*)d_in[4],
                                        (const bf16*)d_in[6], (const bf16*)d_in[7], (const bf16*)d_in[8], (const bf16*)d_in[9], (const bf16*)d_in[10],
                                        (bf16*)d_out, (char*)d_ws);
}
```

```cpp
#include <hip/hip_runtime.h>
#include <hip/hip_bf16.h>
#include <cstdint>

using bf16 = __hip_bfloat16;
typedef unsigned long long u64;
constexpr int B = 2, DM = 512, HQ = 32, HKV = 8, G = 4, SK = 32768, DH = 128, F = 1792;
constexpr int NQ = HQ * DH;
constexpr int NWG = 256, NT = 512, NW = 8;
constexpr int NSPLIT = 16, PAGE = 16;
constexpr int NPG = SK / PAGE;
constexpr int SPG = 112;
constexpr int FCH = 8;
constexpr int NFL = (NPG - 16 * SPG) / FCH;
constexpr int NREC = 16 + NFL;
constexpr int RECW = 2 * NREC * 18;
constexpr int NGU = F / 8;
constexpr unsigned SPIN_CAP = 1u << 22;

using u32x4 = __attribute__((ext_vector_type(4))) unsigned;
using f32x4 = __attribute__((ext_vector_type(4))) float;
using bf16x2 = __attribute__((ext_vector_type(2))) __bf16;

constexpr size_t WS_G1 = 0;
constexpr size_t WS_G2 = 32768;
constexpr size_t WS_G4 = 65536;
constexpr size_t WS_G5 = 98304;
constexpr size_t WS_G6 = 102400;
constexpr size_t WS_EPOCH = 131072;
constexpr size_t WS_CNT = 135168;
constexpr size_t WS_G3 = 1u << 20;
constexpr size_t WS_TS = 8u << 20;

__device__ __forceinline__ float bf2f(unsigned h) { return __builtin_bit_cast(float, h << 16); }
__device__ __forceinline__ float bflo(unsigned w) { return __builtin_bit_cast(float, w << 16); }
__device__ __forceinline__ float bfhi(unsigned w) { return __builtin_bit_cast(float, w & 0xffff0000u); }
typedef __attribute__((ext_vector_type(2))) float f32x2;
__device__ __forceinline__ unsigned cvtpk(float lo, float hi) {
  return __builtin_bit_cast(unsigned, __builtin_convertvector(f32x2{lo, hi}, bf16x2));
}
__device__ __forceinline__ float rbf(float f) { return __builtin_bit_cast(float, cvtpk(f, f) << 16); }
__device__ __forceinline__ float frcp(float x) { return __builtin_amdgcn_rcpf(x); }
__device__ __forceinline__ float dot2(unsigned k, unsigned q, float acc) {
  return __builtin_amdgcn_fdot2_f32_bf16(__builtin_bit_cast(bf16x2, k), __builtin_bit_cast(bf16x2, q), acc, false);
}
template <int CTRL> __device__ __forceinline__ float dpp(float x) {
  return __builtin_bit_cast(float, __builtin_amdgcn_mov_dpp(__builtin_bit_cast(int, x), CTRL, 0xf, 0xf, true));
}
constexpr int XOR1 = 0xB1, XOR2 = 0x4E, XOR7 = 0x141, XOR8 = 0x128, ROR4 = 0x124;
__device__ __forceinline__ float xrow16_max(float x) {
  auto s = __builtin_amdgcn_permlane16_swap(__float_as_uint(x), __float_as_uint(x), false, false);
  x = fmaxf(__uint_as_float(s[0]), __uint_as_float(s[1]));
  auto t = __builtin_amdgcn_permlane32_swap(__float_as_uint(x), __float_as_uint(x), false, false);
  return fmaxf(__uint_as_float(t[0]), __uint_as_float(t[1]));
}
__device__ __forceinline__ float xrow16_sum(float x) {
  auto s = __builtin_amdgcn_permlane16_swap(__float_as_uint(x), __float_as_uint(x), false, false);
  x = __uint_as_float(s[0]) + __uint_as_float(s[1]);
  auto t = __builtin_amdgcn_permlane32_swap(__float_as_uint(x), __float_as_uint(x), false, false);
  return __uint_as_float(t[0]) + __uint_as_float(t[1]);
}
__device__ __forceinline__ float wave_sum(float x) {
  x += dpp<XOR1>(x); x += dpp<XOR2>(x); x += dpp<XOR7>(x); x += dpp<XOR8>(x);
  return xrow16_sum(x);
}
__device__ __forceinline__ float par_sum(float x) {
  x += dpp<XOR2>(x); x += dpp<ROR4>(x); x += dpp<XOR8>(x);
  return xrow16_sum(x);
}
__device__ __forceinline__ void bar() { asm volatile("s_waitcnt lgkmcnt(0)\n\ts_barrier" ::: "memory"); }
__device__ __forceinline__ void put(u64* p, unsigned tag, unsigned v) {
  __hip_atomic_store(p, ((u64)tag << 32) | v, __ATOMIC_RELAXED, __HIP_MEMORY_SCOPE_AGENT);
}
template <int N>
__device__ __forceinline__ void sweep(const u64* p, int stride, unsigned tag, unsigned (&v)[N]) {
  for (unsigned n = 0;; ++n) {
    bool ok = true;
#pragma unroll
    for (int k = 0; k < N; ++k) { const u64 a = __hip_atomic_load(p + k * stride, __ATOMIC_RELAXED, __HIP_MEMORY_SCOPE_AGENT); v[k] = (unsigned)a; ok &= (unsigned)(a >> 32) == tag; }
    if (__all(ok) || n > SPIN_CAP) break;
    __builtin_amdgcn_s_sleep(1);
  }
}

#define STAMP(i) do { } while (0)

constexpr int L_A = 0;
constexpr int L_X = 1024;
constexpr int L_RED = 2048;
constexpr int L_RES = 2304;
constexpr int L_Q = 2368;
constexpr int L_P = 2624;
constexpr int L_TICK = 3136;
constexpr int L_MODS = 3140;
constexpr int L_PART = L_MODS + 3072;
constexpr int L_WST = L_PART + 1728 + 64;
constexpr int L_MRG = L_WST + 20 * 512;
constexpr int L_TOTAL = L_MRG + 4224 + 6144;

__device__ __forceinline__ void ln_norm(const float* lds, int lane, float x0, float x1, float& n0, float& n1) {
#pragma unroll
  for (int bb = 0; bb < 2; ++bb) {
    float v[8], s = 0.f;
#pragma unroll
    for (int j = 0; j < 8; ++j) { v[j] = lds[L_X + bb * 512 + lane + 64 * j]; s += v[j]; }
    const float mu = wave_sum(s) * (1.f / DM);
    float q = 0.f;
#pragma unroll
    for (int j = 0; j < 8; ++j) { const float d = v[j] - mu; q += d * d; }
    const float var = rbf(wave_sum(q) * (1.f / DM));
    const float sd = rbf(__builtin_amdgcn_sqrtf(rbf(var + 1e-5f)));
    const float t = rbf(rbf((bb ? x1 : x0) - rbf(mu)) * frcp(sd));
    if (bb) n1 = t; else n0 = t;
  }
}
__device__ __forceinline__ void gemv16_part(float* lds, int tid, const u32x4* wv) {
  const int lane = tid & 63, wave = tid >> 6;
  float acc[16];
#pragma unroll
  for (int j = 0; j < 16; ++j) acc[j] = 0.f;
#pragma unroll
  for (int i = 0; i < 2; ++i) {
    const int row = (tid >> 1) + 256 * i;
    const float a0 = lds[L_A + row], a1 = lds[L_A + 512 + row];
#pragma unroll
    for (int j = 0; j < 4; ++j) {
      const float lo = bflo(wv[i][j]), hi = bfhi(wv[i][j]);
      acc[2 * j] = fmaf(a0, lo, acc[2 * j]); acc[2 * j + 1] = fmaf(a0, hi, acc[2 * j + 1]);
      acc[8 + 2 * j] = fmaf(a1, lo, acc[8 + 2 * j]); acc[8 + 2 * j + 1] = fmaf(a1, hi, acc[8 + 2 * j + 1]);
    }
  }
#pragma unroll
  for (int h = 0; h < 8; ++h) { auto sw = __builtin_amdgcn_permlane32_swap(__float_as_uint(acc[h]), __float_as_uint(acc[h + 8]), false, false); acc[h] = __uint_as_float(sw[0]) + __uint_as_float(sw[1]); }
#pragma unroll
  for (int h = 0; h < 4; ++h) { auto sw = __builtin_amdgcn_permlane16_swap(__float_as_uint(acc[h]), __float_as_uint(acc[h + 4]), false, false); acc[h] = __uint_as_float(sw[0]) + __uint_as_float(sw[1]); }
#pragma unroll
  for (int h = 0; h < 4; ++h) { float v = acc[h]; v += dpp<XOR2>(v); v += dpp<ROR4>(v); v += dpp<XOR8>(v); acc[h] = v; }
  if ((lane & 14) == 0) *reinterpret_cast<f32x4*>(lds + L_RED + wave * 32 + (lane & 1) * 16 + (lane >> 4) * 4) = f32x4{acc[0], acc[1], acc[2], acc[3]};
}
__device__ __forceinline__ unsigned gemv16_fin(const float* lds, int t) {
  const int half = t >> 3, j = t & 7;
  float s0 = 0.f, s1 = 0.f;
#pragma unroll
  for (int ww = 0; ww < 8; ++ww) { s0 += lds[L_RED + ww * 32 + half * 16 + j]; s1 += lds[L_RED + ww * 32 + half * 16 + 8 + j]; }
  return cvtpk(s0, s1);
}

__global__ void __launch_bounds__(NT, 2) mega(const bf16* __restrict__ x, const bf16* __restrict__ kc, const bf16* __restrict__ vc,
                                              const bf16* __restrict__ wq, const bf16* __restrict__ wo, const bf16* __restrict__ wg,
                                              const bf16* __restrict__ wu, const bf16* __restrict__ wd, const bf16* __restrict__ c,
                                              const bf16* __restrict__ w_mod, bf16* __restrict__ out, char* __restrict__ ws) {
  extern __shared__ __attribute__((aligned(16))) float lds[];
  const int tid = threadIdx.x, lane = tid & 63, wave = __builtin_amdgcn_readfirstlane(tid >> 6);
  const int w = blockIdx.x;
  const unsigned tag = __hip_atomic_load((const unsigned*)(ws + WS_EPOCH), __ATOMIC_RELAXED, __HIP_MEMORY_SCOPE_AGENT) + 1u;
  u64* g1 = (u64*)(ws + WS_G1); u64* g2 = (u64*)(ws + WS_G2); u64* g3 = (u64*)(ws + WS_G3);
  u64* g4 = (u64*)(ws + WS_G4); u64* g5 = (u64*)(ws + WS_G5); u64* g6 = (u64*)(ws + WS_G6);
  u64* ts = (u64*)(ws + WS_TS); (void)ts;
  const int pair = w >> 4, b = pair >> 3, kvh = pair & 7, s = w & 15;
  const int r = lane >> 4, cc = lane & 15;
  const int col0 = 2 * ((w & 7) * 32 + (w >> 3));
  STAMP(0);
  const unsigned short* cs = (const unsigned short*)c; const unsigned short* xs = (const unsigned short*)x;
  const int wx = w & 7, wi = w >> 3;
  const bool has_mod = wi < 24;
  const int mslice = wi < 8 ? wx * 8 + wi : (wi < 24 ? 64 + wx * 16 + (wi - 8) : 0);
  const int qslice = wx * 32 + wi;
  const bool has_gu = wi < 28;
  const int wgu = has_gu ? wx * 28 + wi : 0;
  unsigned* cnt = (unsigned*)(ws + WS_CNT + (size_t)pair * 128);
  if (w == 0 && tid < 16) __hip_atomic_store((unsigned*)(ws + WS_CNT + (size_t)tid * 128), 0u, __ATOMIC_RELAXED, __HIP_MEMORY_SCOPE_AGENT);
  const float c0 = bf2f(cs[tid]), c1 = bf2f(cs[512 + tid]);
  const float x0 = bf2f(xs[tid]), x1 = bf2f(xs[512 + tid]);
  u32x4 wm[2], wqv[2];
#pragma unroll
  for (int i = 0; i < 2; ++i) {
    const int row = (tid >> 1) + 256 * i;
    wm[i] = *reinterpret_cast<const u32x4*>(w_mod + (size_t)row * (6 * DM) + mslice * 16 + (tid & 1) * 8);
    wqv[i] = *reinterpret_cast<const u32x4*>(wq + (size_t)row * NQ + qslice * 16 + (tid & 1) * 8);
  }
  lds[L_A + tid] = rbf(c0 * frcp(1.f + __expf(-c0))); lds[L_A + 512 + tid] = rbf(c1 * frcp(1.f + __expf(-c1)));
  lds[L_X + tid] = x0; lds[L_X + 512 + tid] = x1;
  bar();
  gemv16_part(lds, tid, wm);
  float n0, n1;
  ln_norm(lds, lane, x0, x1, n0, n1);
  bar();
  if (has_mod && tid < 16) { const unsigned rr = gemv16_fin(lds, tid); asm volatile("s_waitcnt vmcnt(0)" ::: "memory"); put(g1 + mslice * 16 + tid, tag, rr); }
  STAMP(1);
  const size_t base = ((size_t)(b * HKV + kvh) * SK) * DH + (size_t)r * DH + 8 * cc;
  const bf16* kb = kc + base; const bf16* vb = vc + base;
  u32x4 KA[4], VA[4], KB[4], VB[4];
#define ISSUE(KX, VX, pg_) do { const size_t off_ = (size_t)(pg_) * PAGE * DH; \
    _Pragma("unroll") for (int t_ = 0; t_ < 4; ++t_) { KX[t_] = __builtin_nontemporal_load(reinterpret_cast<const u32x4*>(kb + off_ + t_ * 4 * DH)); VX[t_] = __builtin_nontemporal_load(reinterpret_cast<const u32x4*>(vb + off_ + t_ * 4 * DH)); } } while (0)
  unsigned st[20];
#define STASH_LOAD() do { \
    _Pragma("unroll") for (int i = 0; i < 8; ++i) st[i] = *reinterpret_cast<const unsigned*>(wo + (size_t)(tid + 512 * i) * DM + col0); \
    { const u32x4 a_ = *reinterpret_cast<const u32x4*>(wg + (size_t)tid * F + 8 * wgu); st[8] = a_[0]; st[9] = a_[1]; st[10] = a_[2]; st[11] = a_[3]; } \
    { const u32x4 a_ = *reinterpret_cast<const u32x4*>(wu + (size_t)tid * F + 8 * wgu); st[12] = a_[0]; st[13] = a_[1]; st[14] = a_[2]; st[15] = a_[3]; } \
    _Pragma("unroll") for (int i = 0; i < 4; ++i) { const int k_ = tid + 512 * i; st[16 + i] = *reinterpret_cast<const unsigned*>(wd + (size_t)(k_ < F ? k_ : 0) * DM + col0); } } while (0)
#define STASH_STORE() do { _Pragma("unroll") for (int i = 0; i < 20; i += 4) *reinterpret_cast<u32x4*>(lds + L_WST + (i * 128 + tid) * 4 - 0 + 0 * i) = u32x4{st[i], st[i + 1], st[i + 2], st[i + 3]}; } while (0)
  const int sbase = s * SPG;
  const int nstat = wave ? 15 : 7, pstep = wave ? 7 : 1;
  const int pfirst = sbase + (wave ? wave - 1 : 105);
#define TICKET_RAW() (lane == 0 ? __hip_atomic_fetch_add(cnt, 1u, __ATOMIC_RELAXED, __HIP_MEMORY_SCOPE_AGENT) : 0u)
  if (wave != 0) {
    ISSUE(KA, VA, pfirst);
  } else {
    unsigned v[16];
    sweep<16>(g1 + lane, 64, tag, v);
#pragma unroll
    for (int j = 0; j < 16; ++j) ((unsigned*)(lds + L_MODS))[lane + 64 * j] = v[j];
  }
  bar();
  STAMP(2);
  { const unsigned sh = ((const unsigned*)(lds + L_MODS))[tid], sc = ((const unsigned*)(lds + L_MODS))[512 + tid];
    lds[L_A + tid] = rbf(rbf(n0 * rbf(1.f + bflo(sc))) + bflo(sh));
    lds[L_A + 512 + tid] = rbf(rbf(n1 * rbf(1.f + bfhi(sc))) + bfhi(sh)); }
  bar();
  gemv16_part(lds, tid, wqv);
  bar();
  if (wave != 0) {
    ISSUE(KB, VB, pfirst + pstep);
  } else {
    if (tid < 16) put(g2 + qslice * 16 + tid, tag, gemv16_fin(lds, tid));
    STAMP(3);
    { unsigned v[16];
      sweep<16>(g1 + 1536 + lane, 64, tag, v);
#pragma unroll
      for (int j = 0; j < 16; ++j) ((unsigned*)(lds + L_MODS))[1536 + lane + 64 * j] = v[j]; }
    { unsigned v[1];
      sweep<1>(g1 + (lane < 2 ? 1024 + col0 + lane : 2560 + col0 + (lane & 1)), 0, tag, v);
      if (lane < 4) ((unsigned*)(lds + L_MODS))[1024 + lane] = v[0]; }
    unsigned qq[8];
    sweep<8>(g2 + kvh * 512 + lane, 64, tag, qq);
#pragma unroll
    for (int j = 0; j < 8; ++j) ((unsigned short*)(lds + L_Q))[lane + 64 * j] = (unsigned short)(b ? (qq[j] >> 16) : (qq[j] & 0xffffu));
    ISSUE(KA, VA, pfirst);
    ISSUE(KB, VB, pfirst + pstep);
  }
  bar();
  STAMP(4);
  u32x4 qpk[4];
#pragma unroll
  for (int g = 0; g < 4; ++g) qpk[g] = *reinterpret_cast<const u32x4*>((const unsigned short*)(lds + L_Q) + g * DH + 8 * cc);
  constexpr float C2 = 0.088388347648318440f * 1.4426950408889634f;
  float m = -INFINITY, l = 0.f, acc[32];
#pragma unroll
  for (int k = 0; k < 32; ++k) acc[k] = 0.f;
  float* prow = lds + L_P + (wave * 4 + r) * 16;
#define TR_STEP(HALF, CTRL, BIT) _Pragma("unroll") for (int h = 0; h < (HALF); ++h) { \
      const float lo_ = xx[h], hi_ = xx[h + (HALF)]; const float t1_ = lo_ + dpp<CTRL>(lo_), t2_ = hi_ + dpp<CTRL>(hi_); \
      xx[h] = (cc & (BIT)) ? t2_ : t1_; }
#define PROCESS(KX, VX) do { \
    float xx[16]; \
    _Pragma("unroll") for (int t = 0; t < 4; ++t) _Pragma("unroll") for (int g = 0; g < 4; ++g) { float d = 0.f; \
        _Pragma("unroll") for (int k = 0; k < 4; ++k) d = dot2(KX[t][k], qpk[g][k], d); xx[t * 4 + g] = d; } \
    TR_STEP(8, XOR8, 8) TR_STEP(4, XOR7, 4) TR_STEP(2, XOR2, 2) TR_STEP(1, XOR1, 1) \
    const float sc = xx[0] * C2; \
    float pm = sc; pm = fmaxf(pm, dpp<ROR4>(pm)); pm = fmaxf(pm, dpp<XOR8>(pm)); pm = xrow16_max(pm); \
    const float mn = fmaxf(m, pm); \
    const float alpha = __builtin_amdgcn_exp2f(m - mn), p = __builtin_amdgcn_exp2f(sc - mn); \
    l = fmaf(l, alpha, p); m = mn; \
    _Pragma("unroll") for (int q4 = 0; q4 < 4; ++q4) { \
      const float al = q4 == 0 ? dpp<0x00>(alpha) : q4 == 1 ? dpp<0x55>(alpha) : q4 == 2 ? dpp<0xAA>(alpha) : dpp<0xFF>(alpha); \
      _Pragma("unroll") for (int k = 0; k < 8; ++k) acc[q4 * 8 + k] *= al; } \
    prow[cc] = p; \
    asm volatile("s_waitcnt lgkmcnt(0)" ::: "memory"); \
    f32x4 pr[4]; \
    _Pragma("unroll") for (int u = 0; u < 4; ++u) pr[u] = *reinterpret_cast<const f32x4*>(prow + 4 * u); \
    _Pragma("unroll") for (int t = 0; t < 4; ++t) { \
      float vf[8]; \
      _Pragma("unroll") for (int k = 0; k < 4; ++k) { vf[2 * k] = bflo(VX[t][k]); vf[2 * k + 1] = bfhi(VX[t][k]); } \
      _Pragma("unroll") for (int g = 0; g < 4; ++g) { const float pg_ = pr[t][g]; \
        _Pragma("unroll") for (int k = 0; k < 8; ++k) acc[g * 8 + k] = fmaf(pg_, vf[k], acc[g * 8 + k]); } } \
  } while (0)
#define STASH(it_) do { \
    if ((it_) == 1) { unsigned st[8]; \
      _Pragma("unroll") for (int i = 0; i < 8; ++i) st[i] = *reinterpret_cast<const unsigned*>(wo + (size_t)(tid + 512 * i) * DM + col0); \
      *reinterpret_cast<u32x4*>(lds + L_WST + (0 * 512 + tid) * 4) = u32x4{st[0], st[1], st[2], st[3]}; \
      *reinterpret_cast<u32x4*>(lds + L_WST + (1 * 512 + tid) * 4) = u32x4{st[4], st[5], st[6], st[7]}; \
    } else if ((it_) == 2) { \
      const u32x4 a_ = *reinterpret_cast<const u32x4*>(wg + (size_t)tid * F + 8 * wgu), b_ = *reinterpret_cast<const u32x4*>(wu + (size_t)tid * F + 8 * wgu); \
      *reinterpret_cast<u32x4*>(lds + L_WST + (2 * 512 + tid) * 4) = a_; \
      *reinterpret_cast<u32x4*>(lds + L_WST + (3 * 512 + tid) * 4) = b_; \
    } else if ((it_) == 3) { unsigned st[4]; \
      _Pragma("unroll") for (int i = 0; i < 4; ++i) { const int k_ = tid + 512 * i; st[i] = *reinterpret_cast<const unsigned*>(wd + (size_t)(k_ < F ? k_ : 0) * DM + col0); } \
      *reinterpret_cast<u32x4*>(lds + L_WST + (4 * 512 + tid) * 4) = u32x4{st[0], st[1], st[2], st[3]}; \
    } } while (0)
  unsigned tkraw = 0;
  for (int it = 0; it < nstat; it += 2) {
    PROCESS(KA, VA);
    if (it + 2 < nstat) ISSUE(KA, VA, pfirst + (it + 2) * pstep);
    STASH(it >> 1);
    if (it + 4 >= nstat && it + 2 < nstat) tkraw = TICKET_RAW();
    if (it + 1 < nstat) {
      PROCESS(KB, VB);
      if (it + 3 < nstat) ISSUE(KB, VB, pfirst + (it + 3) * pstep);
    }
  }
#define WAVE_REDUCE() do { _Pragma("unroll") for (int k = 0; k < 32; ++k) acc[k] = xrow16_sum(acc[k]); \
    { float t = l; t += dpp<ROR4>(t); t += dpp<XOR8>(t); l = xrow16_sum(t); } } while (0)
  WAVE_REDUCE();
  float* mrg = lds + L_MRG;
  if (r == 0) {
#pragma unroll
    for (int g = 0; g < 4; ++g) {
      float* rec = mrg + (wave * 4 + g) * 132;
#pragma unroll
      for (int k = 0; k < 8; k += 4) *reinterpret_cast<f32x4*>(rec + 4 + 8 * cc + k) = f32x4{acc[g * 8 + k], acc[g * 8 + k + 1], acc[g * 8 + k + 2], acc[g * 8 + k + 3]};
      if (cc == g) { rec[0] = m; rec[1] = l; } } }
  int fc = (int)__builtin_amdgcn_readfirstlane(tkraw);
  while (fc < NFL) {
    const int p0 = 16 * SPG + fc * FCH;
    ISSUE(KA, VA, p0);
    ISSUE(KB, VB, p0 + 1);
    tkraw = TICKET_RAW();
    m = -INFINITY; l = 0.f;
#pragma unroll
    for (int k = 0; k < 32; ++k) acc[k] = 0.f;
    for (int it = 0; it < FCH; it += 2) {
      PROCESS(KA, VA);
      if (it + 2 < FCH) ISSUE(KA, VA, p0 + it + 2);
      PROCESS(KB, VB);
      if (it + 3 < FCH) ISSUE(KB, VB, p0 + it + 3);
    }
    WAVE_REDUCE();
    {
      const float mg = __shfl(m, r), lg = __shfl(l, r);
      u64* dst = g3 + (size_t)((kvh * 4 + r) * 8 + (cc >> 1)) * RECW + (b * NREC + 16 + fc) * 18;
      float av[8];
#pragma unroll
      for (int k = 0; k < 8; ++k) av[k] = r == 0 ? acc[k] : r == 1 ? acc[8 + k] : r == 2 ? acc[16 + k] : acc[24 + k];
#pragma unroll
      for (int k = 0; k < 8; ++k) put(dst + 2 + (cc & 1) * 8 + k, tag, __float_as_uint(av[k]));
      if ((cc & 1) == 0) { put(dst, tag, __float_as_uint(mg)); put(dst + 1, tag, __float_as_uint(lg)); }
    }
    fc = (int)__builtin_amdgcn_readfirstlane(tkraw);
  }
#undef WAVE_REDUCE
#undef PROCESS
#undef TR_STEP
#undef ISSUE
  STAMP(5);
  bar();
  { const int g = tid >> 7, d = tid & 127;
    float M = -INFINITY;
#pragma unroll
    for (int ww = 0; ww < NW; ++ww) M = fmaxf(M, mrg[(ww * 4 + g) * 132]);
    float L = 0.f, o0 = 0.f;
#pragma unroll
    for (int ww = 0; ww < NW; ++ww) { const float* rec = mrg + (ww * 4 + g) * 132;
      const float wgt = __builtin_amdgcn_exp2f(rec[0] - M);
      L = fmaf(wgt, rec[1], L); o0 = fmaf(wgt, rec[4 + d], o0); }
    u64* dst = g3 + (size_t)((kvh * 4 + g) * 8 + (d >> 4)) * RECW + (b * NREC + s) * 18;
    put(dst + 2 + (d & 15), tag, __float_as_uint(o0));
    if ((d & 15) == 0) { put(dst, tag, __float_as_uint(M)); put(dst + 1, tag, __float_as_uint(L)); }
  }
  STAMP(6);
  { unsigned v[3]; sweep<3>(g3 + (size_t)w * RECW + tid, 512, tag, v);
#pragma unroll
    for (int k = 0; k < 3; ++k) lds[L_PART + 512 * k + tid] = __uint_as_float(v[k]);
    if (wave < 3) { unsigned v1[1]; sweep<1>(g3 + (size_t)w * RECW + 1536 + tid, 0, tag, v1); lds[L_PART + 1536 + tid] = __uint_as_float(v1[0]); } }
  bar();
  STAMP(7);
  if (tid < 32) {
    const int bb = tid >> 4, d = tid & 15;
    const float* pp = lds + L_PART + bb * NREC * 18;
    float M = -INFINITY;
#pragma unroll
    for (int ss = 0; ss < NREC; ++ss) M = fmaxf(M, pp[ss * 18]);
    float L = 0.f, o0 = 0.f;
#pragma unroll
    for (int ss = 0; ss < NREC; ++ss) { const float wgt = __builtin_amdgcn_exp2f(pp[ss * 18] - M);
      L = fmaf(wgt, pp[ss * 18 + 1], L); o0 = fmaf(wgt, pp[ss * 18 + 2 + d], o0); }
    const unsigned ob = cvtpk(o0 * frcp(L), 0.f) & 0xffffu;
    const unsigned other = __shfl_down(ob, 16);
    if (tid < 16) put(g4 + w * 16 + tid, tag, ob | (other << 16));
  }
  STAMP(8);
  float x1own0 = 0.f, x1own1 = 0.f;
  { unsigned ov[8]; sweep<8>(g4 + tid, 512, tag, ov);
    STAMP(9);
    float a00 = 0.f, a01 = 0.f, a10 = 0.f, a11 = 0.f;
#pragma unroll
    for (int i = 0; i < 8; ++i) {
      const unsigned wv = ((const unsigned*)(lds + L_WST))[((i >> 2) * 512 + tid) * 4 + (i & 3)];
      const float w0 = bflo(wv), w1 = bfhi(wv), o0 = bflo(ov[i]), o1 = bfhi(ov[i]);
      a00 = fmaf(o0, w0, a00); a01 = fmaf(o0, w1, a01); a10 = fmaf(o1, w0, a10); a11 = fmaf(o1, w1, a11);
    }
    a00 = wave_sum(a00); a01 = wave_sum(a01); a10 = wave_sum(a10); a11 = wave_sum(a11);
    if (lane == 0) *reinterpret_cast<f32x4*>(lds + L_RED + wave * 4) = f32x4{a00, a01, a10, a11};
    bar();
    if (tid < 2) {
      float a0 = 0.f, a1 = 0.f;
#pragma unroll
      for (int ww = 0; ww < NW; ++ww) { a0 += lds[L_RED + ww * 4 + tid]; a1 += lds[L_RED + ww * 4 + 2 + tid]; }
      const int n = col0 + tid;
      const unsigned ga = ((const unsigned*)(lds + L_MODS))[1024 + tid];
      x1own0 = rbf(bf2f(xs[n]) + rbf(bflo(ga) * rbf(a0)));
      x1own1 = rbf(bf2f(xs[512 + n]) + rbf(bfhi(ga) * rbf(a1)));
      put(g5 + n, tag, cvtpk(x1own0, x1own1));
    } }
  STAMP(10);
  { unsigned v[1]; sweep<1>(g5 + tid, 0, tag, v);
    STAMP(11);
    const float y0 = bflo(v[0]), y1 = bfhi(v[0]);
    lds[L_X + tid] = y0; lds[L_X + 512 + tid] = y1;
    bar();
    if (has_gu) {
      float m0, m1; ln_norm(lds, lane, y0, y1, m0, m1);
      const unsigned sh = ((const unsigned*)(lds + L_MODS))[1536 + tid], sc = ((const unsigned*)(lds + L_MODS))[2048 + tid];
      const float a0 = rbf(rbf(m0 * rbf(1.f + bflo(sc))) + bflo(sh)), a1 = rbf(rbf(m1 * rbf(1.f + bfhi(sc))) + bfhi(sh));
      const u32x4 wgv = *reinterpret_cast<const u32x4*>(lds + L_WST + (2 * 512 + tid) * 4), wuv = *reinterpret_cast<const u32x4*>(lds + L_WST + (3 * 512 + tid) * 4);
      float v2[32];
#pragma unroll
      for (int j = 0; j < 4; ++j) {
        v2[2 * j] = a0 * bflo(wgv[j]); v2[2 * j + 1] = a0 * bfhi(wgv[j]); v2[8 + 2 * j] = a0 * bflo(wuv[j]); v2[8 + 2 * j + 1] = a0 * bfhi(wuv[j]);
        v2[16 + 2 * j] = a1 * bflo(wgv[j]); v2[16 + 2 * j + 1] = a1 * bfhi(wgv[j]); v2[24 + 2 * j] = a1 * bflo(wuv[j]); v2[24 + 2 * j + 1] = a1 * bfhi(wuv[j]);
      }
#pragma unroll
      for (int h = 0; h < 16; ++h) { auto sw = __builtin_amdgcn_permlane32_swap(__float_as_uint(v2[h]), __float_as_uint(v2[h + 16]), false, false); v2[h] = __uint_as_float(sw[0]) + __uint_as_float(sw[1]); }
#pragma unroll
      for (int h = 0; h < 8; ++h) { auto sw = __builtin_amdgcn_permlane16_swap(__float_as_uint(v2[h]), __float_as_uint(v2[h + 8]), false, false); v2[h] = __uint_as_float(sw[0]) + __uint_as_float(sw[1]); }
#define TRS(HALF, CTRL, BIT) _Pragma("unroll") for (int h = 0; h < (HALF); ++h) { \
      const float lo_ = v2[h], hi_ = v2[h + (HALF)]; const float t1_ = lo_ + dpp<CTRL>(lo_), t2_ = hi_ + dpp<CTRL>(hi_); \
      v2[h] = (lane & (BIT)) ? t2_ : t1_; }
      TRS(4, XOR8, 8) TRS(2, XOR7, 4) TRS(1, XOR2, 2)
#undef TRS
      v2[0] += dpp<XOR1>(v2[0]);
      if ((lane & 1) == 0) lds[L_RED + wave * 32 + (lane >> 1)] = v2[0];
    }
    bar();
    if (has_gu && tid < 8) {
      float g0 = 0.f, u0 = 0.f, g1s = 0.f, u1 = 0.f;
#pragma unroll
      for (int ww = 0; ww < NW; ++ww) { g0 += lds[L_RED + ww * 32 + tid]; u0 += lds[L_RED + ww * 32 + 8 + tid]; g1s += lds[L_RED + ww * 32 + 16 + tid]; u1 += lds[L_RED + ww * 32 + 24 + tid]; }
      put(g6 + 8 * wgu + tid, tag, cvtpk(rbf(g0) * rbf(u0), rbf(g1s) * rbf(u1)));
    } }
  STAMP(12);
  { unsigned hv[4];
    for (unsigned n = 0;; ++n) {
      bool ok = true;
#pragma unroll
      for (int i = 0; i < 4; ++i) { const int k = tid + 512 * i; const u64 a = __hip_atomic_load(g6 + (k < F ? k : 0), __ATOMIC_RELAXED, __HIP_MEMORY_SCOPE_AGENT); hv[i] = k < F ? (unsigned)a : 0u; ok &= (unsigned)(a >> 32) == tag; }
      if (__all(ok) || n > SPIN_CAP) break;
      __builtin_amdgcn_s_sleep(1);
    }
    STAMP(13);
    float a00 = 0.f, a01 = 0.f, a10 = 0.f, a11 = 0.f;
    const u32x4 wdv = *reinterpret_cast<const u32x4*>(lds + L_WST + (4 * 512 + tid) * 4);
#pragma unroll
    for (int i = 0; i < 4; ++i) {
      const float w0 = bflo(wdv[i]), w1 = bfhi(wdv[i]), o0 = bflo(hv[i]), o1 = bfhi(hv[i]);
      a00 = fmaf(o0, w0, a00); a01 = fmaf(o0, w1, a01); a10 = fmaf(o1, w0, a10); a11 = fmaf(o1, w1, a11);
    }
    a00 = wave_sum(a00); a01 = wave_sum(a01); a10 = wave_sum(a10); a11 = wave_sum(a11);
    if (lane == 0) *reinterpret_cast<f32x4*>(lds + L_RED + wave * 4) = f32x4{a00, a01, a10, a11};
    bar();
    if (tid < 2) {
      float a0 = 0.f, a1 = 0.f;
#pragma unroll
      for (int ww = 0; ww < NW; ++ww) { a0 += lds[L_RED + ww * 4 + tid]; a1 += lds[L_RED + ww * 4 + 2 + tid]; }
      const int n = col0 + tid;
      const unsigned gb = ((const unsigned*)(lds + L_MODS))[1026 + tid];
      out[n] = __float2bfloat16(x1own0 + rbf(bflo(gb) * rbf(a0)));
      out[512 + n] = __float2bfloat16(x1own1 + rbf(bfhi(gb) * rbf(a1)));
    }
    if (w == 0 && tid == 0) __hip_atomic_store((unsigned*)(ws + WS_EPOCH), tag, __ATOMIC_RELAXED, __HIP_MEMORY_SCOPE_AGENT);
  }
  STAMP(14);
}

extern "C" void kernel_launch(void* const* d_in, const int* in_sizes, int n_in, void* d_out, int out_size, void* d_ws, size_t ws_size, hipStream_t stream) {
  (void)hipFuncSetAttribute((const void*)mega, hipFuncAttributeMaxDynamicSharedMemorySize, L_TOTAL * 4);
  mega<<<NWG, NT, L_TOTAL * 4, stream>>>((const bf16*)d_in[0], (const bf16*)d_in[1], (const bf16*)d_in[2], (const bf16*)d_in[3], (const bf16*)d_in[4],
                                        (const bf16*)d_in[6], (const bf16*)d_in[7], (const bf16*)d_in[8], (const bf16*)d_in[9], (const bf16*)d_in[10],
                                        (bf16*)d_out, (char*)d_ws);
}
```

```cpp
#include <hip/hip_runtime.h>
#include <hip/hip_bf16.h>
#include <cstdint>

using bf16 = __hip_bfloat16;
typedef unsigned long long u64;
constexpr int B = 2, DM = 512, HQ = 32, HKV = 8, G = 4, SK = 32768, DH = 128, F = 1792;
constexpr int NQ = HQ * DH;
constexpr int NWG = 256, NT = 512, NW = 8;
constexpr int NSPLIT = 16, PAGE = 16;
constexpr int NPG = SK / PAGE;
constexpr int SPG = 112;
constexpr int FCH = 8;
constexpr int NFL = (NPG - 16 * SPG) / FCH;
constexpr int NREC = 16 + NFL;
constexpr int RECW = 2 * NREC * 18;
constexpr int NGU = F / 8;
constexpr unsigned SPIN_CAP = 1u << 22;

using u32x4 = __attribute__((ext_vector_type(4))) unsigned;
using f32x4 = __attribute__((ext_vector_type(4))) float;
using bf16x2 = __attribute__((ext_vector_type(2))) __bf16;

constexpr size_t WS_G1 = 0;
constexpr size_t WS_G2 = 32768;
constexpr size_t WS_G4 = 65536;
constexpr size_t WS_G5 = 98304;
constexpr size_t WS_G6 = 102400;
constexpr size_t WS_EPOCH = 131072;
constexpr size_t WS_CNT = 135168;
constexpr size_t WS_G3 = 1u << 20;
constexpr size_t WS_TS = 8u << 20;

__device__ __forceinline__ float bf2f(unsigned h) { return __builtin_bit_cast(float, h << 16); }
__device__ __forceinline__ float bflo(unsigned w) { return __builtin_bit_cast(float, w << 16); }
__device__ __forceinline__ float bfhi(unsigned w) { return __builtin_bit_cast(float, w & 0xffff0000u); }
typedef __attribute__((ext_vector_type(2))) float f32x2;
__device__ __forceinline__ unsigned cvtpk(float lo, float hi) {
  return __builtin_bit_cast(unsigned, __builtin_convertvector(f32x2{lo, hi}, bf16x2));
}
__device__ __forceinline__ float rbf(float f) { return __builtin_bit_cast(float, cvtpk(f, f) << 16); }
__device__ __forceinline__ float frcp(float x) { return __builtin_amdgcn_rcpf(x); }
__device__ __forceinline__ float dot2(unsigned k, unsigned q, float acc) {
  return __builtin_amdgcn_fdot2_f32_bf16(__builtin_bit_cast(bf16x2, k), __builtin_bit_cast(bf16x2, q), acc, false);
}
template <int CTRL> __device__ __forceinline__ float dpp(float x) {
  return __builtin_bit_cast(float, __builtin_amdgcn_mov_dpp(__builtin_bit_cast(int, x), CTRL, 0xf, 0xf, true));
}
constexpr int XOR1 = 0xB1, XOR2 = 0x4E, XOR7 = 0x141, XOR8 = 0x128, ROR4 = 0x124;
__device__ __forceinline__ float xrow16_max(float x) {
  auto s = __builtin_amdgcn_permlane16_swap(__float_as_uint(x), __float_as_uint(x), false, false);
  x = fmaxf(__uint_as_float(s[0]), __uint_as_float(s[1]));
  auto t = __builtin_amdgcn_permlane32_swap(__float_as_uint(x), __float_as_uint(x), false, false);
  return fmaxf(__uint_as_float(t[0]), __uint_as_float(t[1]));
}
__device__ __forceinline__ float xrow16_sum(float x) {
  auto s = __builtin_amdgcn_permlane16_swap(__float_as_uint(x), __float_as_uint(x), false, false);
  x = __uint_as_float(s[0]) + __uint_as_float(s[1]);
  auto t = __builtin_amdgcn_permlane32_swap(__float_as_uint(x), __float_as_uint(x), false, false);
  return __uint_as_float(t[0]) + __uint_as_float(t[1]);
}
__device__ __forceinline__ float wave_sum(float x) {
  x += dpp<XOR1>(x); x += dpp<XOR2>(x); x += dpp<XOR7>(x); x += dpp<XOR8>(x);
  return xrow16_sum(x);
}
__device__ __forceinline__ float par_sum(float x) {
  x += dpp<XOR2>(x); x += dpp<ROR4>(x); x += dpp<XOR8>(x);
  return xrow16_sum(x);
}
__device__ __forceinline__ void bar() { asm volatile("s_waitcnt lgkmcnt(0)\n\ts_barrier" ::: "memory"); }
__device__ __forceinline__ void put(u64* p, unsigned tag, unsigned v) {
  __hip_atomic_store(p, ((u64)tag << 32) | v, __ATOMIC_RELAXED, __HIP_MEMORY_SCOPE_AGENT);
}
template <int N>
__device__ __forceinline__ void sweep(const u64* p, int stride, unsigned tag, unsigned (&v)[N]) {
  for (unsigned n = 0;; ++n) {
    bool ok = true;
#pragma unroll
    for (int k = 0; k < N; ++k) { const u64 a = __hip_atomic_load(p + k * stride, __ATOMIC_RELAXED, __HIP_MEMORY_SCOPE_AGENT); v[k] = (unsigned)a; ok &= (unsigned)(a >> 32) == tag; }
    if (__all(ok) || n > SPIN_CAP) break;
    __builtin_amdgcn_s_sleep(1);
  }
}

#define STAMP(i) do { } while (0)

constexpr int L_A = 0;
constexpr int L_X = 1024;
constexpr int L_RED = 2048;
constexpr int L_RES = 2304;
constexpr int L_Q = 2368;
constexpr int L_P = 2624;
constexpr int L_TICK = 3136;
constexpr int L_MODS = 3140;
constexpr int L_PART = L_MODS + 3072;
constexpr int L_WST = L_PART + 1728 + 64;
constexpr int L_MRG = L_WST + 20 * 512;
constexpr int L_TOTAL = L_MRG + 4224 + 6144;

__device__ __forceinline__ void ln_norm(const float* lds, int lane, float x0, float x1, float& n0, float& n1) {
#pragma unroll
  for (int bb = 0; bb < 2; ++bb) {
    float v[8], s = 0.f;
#pragma unroll
    for (int j = 0; j < 8; ++j) { v[j] = lds[L_X + bb * 512 + lane + 64 * j]; s += v[j]; }
    const float mu = wave_sum(s) * (1.f / DM);
    float q = 0.f;
#pragma unroll
    for (int j = 0; j < 8; ++j) { const float d = v[j] - mu; q += d * d; }
    const float var = rbf(wave_sum(q) * (1.f / DM));
    const float sd = rbf(__builtin_amdgcn_sqrtf(rbf(var + 1e-5f)));
    const float t = rbf(rbf((bb ? x1 : x0) - rbf(mu)) * frcp(sd));
    if (bb) n1 = t; else n0 = t;
  }
}
__device__ __forceinline__ void gemv16_part(float* lds, int tid, const u32x4* wv) {
  const int lane = tid & 63, wave = tid >> 6;
  float acc[16];
#pragma unroll
  for (int j = 0; j < 16; ++j) acc[j] = 0.f;
#pragma unroll
  for (int i = 0; i < 2; ++i) {
    const int row = (tid >> 1) + 256 * i;
    const float a0 = lds[L_A + row], a1 = lds[L_A + 512 + row];
#pragma unroll
    for (int j = 0; j < 4; ++j) {
      const float lo = bflo(wv[i][j]), hi = bfhi(wv[i][j]);
      acc[2 * j] = fmaf(a0, lo, acc[2 * j]); acc[2 * j + 1] = fmaf(a0, hi, acc[2 * j + 1]);
      acc[8 + 2 * j] = fmaf(a1, lo, acc[8 + 2 * j]); acc[8 + 2 * j + 1] = fmaf(a1, hi, acc[8 + 2 * j + 1]);
    }
  }
#pragma unroll
  for (int h = 0; h < 8; ++h) { auto sw = __builtin_amdgcn_permlane32_swap(__float_as_uint(acc[h]), __float_as_uint(acc[h + 8]), false, false); acc[h] = __uint_as_float(sw[0]) + __uint_as_float(sw[1]); }
#pragma unroll
  for (int h = 0; h < 4; ++h) { auto sw = __builtin_amdgcn_permlane16_swap(__float_as_uint(acc[h]), __float_as_uint(acc[h + 4]), false, false); acc[h] = __uint_as_float(sw[0]) + __uint_as_float(sw[1]); }
#pragma unroll
  for (int h = 0; h < 4; ++h) { float v = acc[h]; v += dpp<XOR2>(v); v += dpp<ROR4>(v); v += dpp<XOR8>(v); acc[h] = v; }
  if ((lane & 14) == 0) *reinterpret_cast<f32x4*>(lds + L_RED + wave * 32 + (lane & 1) * 16 + (lane >> 4) * 4) = f32x4{acc[0], acc[1], acc[2], acc[3]};
}
__device__ __forceinline__ unsigned gemv16_fin(const float* lds, int t) {
  const int half = t >> 3, j = t & 7;
  float s0 = 0.f, s1 = 0.f;
#pragma unroll
  for (int ww = 0; ww < 8; ++ww) { s0 += lds[L_RED + ww * 32 + half * 16 + j]; s1 += lds[L_RED + ww * 32 + half * 16 + 8 + j]; }
  return cvtpk(s0, s1);
}

__global__ void __launch_bounds__(NT, 2) mega(const bf16* __restrict__ x, const bf16* __restrict__ kc, const bf16* __restrict__ vc,
                                              const bf16* __restrict__ wq, const bf16* __restrict__ wo, const bf16* __restrict__ wg,
                                              const bf16* __restrict__ wu, const bf16* __restrict__ wd, const bf16* __restrict__ c,
                                              const bf16* __restrict__ w_mod, bf16* __restrict__ out, char* __restrict__ ws) {
  extern __shared__ __attribute__((aligned(16))) float lds[];
  const int tid = threadIdx.x, lane = tid & 63, wave = __builtin_amdgcn_readfirstlane(tid >> 6);
  const int w = blockIdx.x;
  const unsigned tag = __hip_atomic_load((const unsigned*)(ws + WS_EPOCH), __ATOMIC_RELAXED, __HIP_MEMORY_SCOPE_AGENT) + 1u;
  u64* g1 = (u64*)(ws + WS_G1); u64* g2 = (u64*)(ws + WS_G2); u64* g3 = (u64*)(ws + WS_G3);
  u64* g4 = (u64*)(ws + WS_G4); u64* g5 = (u64*)(ws + WS_G5); u64* g6 = (u64*)(ws + WS_G6);
  u64* ts = (u64*)(ws + WS_TS); (void)ts;
  const int pair = w >> 4, b = pair >> 3, kvh = pair & 7, s = w & 15;
  const int r = lane >> 4, cc = lane & 15;
  const int col0 = 2 * ((w & 7) * 32 + (w >> 3));
  STAMP(0);
  const unsigned short* cs = (const unsigned short*)c; const unsigned short* xs = (const unsigned short*)x;
  const int wx = w & 7, wi = w >> 3;
  const bool has_mod = wi < 24;
  const int mslice = wi < 8 ? wx * 8 + wi : (wi < 24 ? 64 + wx * 16 + (wi - 8) : 0);
  const int qslice = wx * 32 + wi;
  const bool has_gu = wi < 28;
  const int wgu = has_gu ? wx * 28 + wi : 0;
  unsigned* cnt = (unsigned*)(ws + WS_CNT + (size_t)pair * 128);
  if (w == 0 && tid < 16) __hip_atomic_store((unsigned*)(ws + WS_CNT + (size_t)tid * 128), 0u, __ATOMIC_RELAXED, __HIP_MEMORY_SCOPE_AGENT);
  const float c0 = bf2f(cs[tid]), c1 = bf2f(cs[512 + tid]);
  const float x0 = bf2f(xs[tid]), x1 = bf2f(xs[512 + tid]);
  u32x4 wm[2], wqv[2];
#pragma unroll
  for (int i = 0; i < 2; ++i) {
    const int row = (tid >> 1) + 256 * i;
    wm[i] = *reinterpret_cast<const u32x4*>(w_mod + (size_t)row * (6 * DM) + mslice * 16 + (tid & 1) * 8);
    wqv[i] = *reinterpret_cast<const u32x4*>(wq + (size_t)row * NQ + qslice * 16 + (tid & 1) * 8);
  }
  lds[L_A + tid] = rbf(c0 * frcp(1.f + __expf(-c0))); lds[L_A + 512 + tid] = rbf(c1 * frcp(1.f + __expf(-c1)));
  lds[L_X + tid] = x0; lds[L_X + 512 + tid] = x1;
  bar();
  gemv16_part(lds, tid, wm);
  float n0, n1;
  ln_norm(lds, lane, x0, x1, n0, n1);
  bar();
  if (has_mod && tid < 16) { const unsigned rr = gemv16_fin(lds, tid); asm volatile("s_waitcnt vmcnt(0)" ::: "memory"); put(g1 + mslice * 16 + tid, tag, rr); }
  STAMP(1);
  const size_t base = ((size_t)(b * HKV + kvh) * SK) * DH + (size_t)r * DH + 8 * cc;
  const bf16* kb = kc + base; const bf16* vb = vc + base;
  u32x4 KA[4], VA[4], KB[4], VB[4];
#define ISSUE(KX, VX, pg_) do { const size_t off_ = (size_t)(pg_) * PAGE * DH; \
    _Pragma("unroll") for (int t_ = 0; t_ < 4; ++t_) { KX[t_] = __builtin_nontemporal_load(reinterpret_cast<const u32x4*>(kb + off_ + t_ * 4 * DH)); VX[t_] = __builtin_nontemporal_load(reinterpret_cast<const u32x4*>(vb + off_ + t_ * 4 * DH)); } } while (0)
  unsigned st[20];
#define STASH_LOAD() do { \
    _Pragma("unroll") for (int i = 0; i < 8; ++i) st[i] = *reinterpret_cast<const unsigned*>(wo + (size_t)(tid + 512 * i) * DM + col0); \
    { const u32x4 a_ = *reinterpret_cast<const u32x4*>(wg + (size_t)tid * F + 8 * wgu); st[8] = a_[0]; st[9] = a_[1]; st[10] = a_[2]; st[11] = a_[3]; } \
    { const u32x4 a_ = *reinterpret_cast<const u32x4*>(wu + (size_t)tid * F + 8 * wgu); st[12] = a_[0]; st[13] = a_[1]; st[14] = a_[2]; st[15] = a_[3]; } \
    _Pragma("unroll") for (int i = 0; i < 4; ++i) { const int k_ = tid + 512 * i; st[16 + i] = *reinterpret_cast<const unsigned*>(wd + (size_t)(k_ < F ? k_ : 0) * DM + col0); } } while (0)
#define STASH_STORE() do { _Pragma("unroll") for (int i = 0; i < 20; i += 4) *reinterpret_cast<u32x4*>(lds + L_WST + (i * 128 + tid) * 4 - 0 + 0 * i) = u32x4{st[i], st[i + 1], st[i + 2], st[i + 3]}; } while (0)
  const int sbase = s * SPG;
  const int pfirst = sbase + wave - 1;
#define TICKET_RAW(dst_) do { if (lane == 0) dst_ = __hip_atomic_fetch_add(cnt, 1u, __ATOMIC_RELAXED, __HIP_MEMORY_SCOPE_AGENT); } while (0)
  if (wave != 0) {
    ISSUE(KA, VA, pfirst);
  } else {
    unsigned v[16];
    sweep<16>(g1 + lane, 64, tag, v);
#pragma unroll
    for (int j = 0; j < 16; ++j) ((unsigned*)(lds + L_MODS))[lane + 64 * j] = v[j];
  }
  bar();
  STAMP(2);
  { const unsigned sh = ((const unsigned*)(lds + L_MODS))[tid], sc = ((const unsigned*)(lds + L_MODS))[512 + tid];
    lds[L_A + tid] = rbf(rbf(n0 * rbf(1.f + bflo(sc))) + bflo(sh));
    lds[L_A + 512 + tid] = rbf(rbf(n1 * rbf(1.f + bfhi(sc))) + bfhi(sh)); }
  bar();
  gemv16_part(lds, tid, wqv);
  bar();
  if (wave != 0) {
    ISSUE(KB, VB, pfirst + 7);
  } else {
    if (tid < 16) put(g2 + qslice * 16 + tid, tag, gemv16_fin(lds, tid));
    STAMP(3);
    { unsigned v[16];
      sweep<16>(g1 + 1536 + lane, 64, tag, v);
#pragma unroll
      for (int j = 0; j < 16; ++j) ((unsigned*)(lds + L_MODS))[1536 + lane + 64 * j] = v[j]; }
    { unsigned v[1];
      sweep<1>(g1 + (lane < 2 ? 1024 + col0 + lane : 2560 + col0 + (lane & 1)), 0, tag, v);
      if (lane < 4) ((unsigned*)(lds + L_MODS))[1024 + lane] = v[0]; }
    unsigned qq[8];
    sweep<8>(g2 + kvh * 512 + lane, 64, tag, qq);
#pragma unroll
    for (int j = 0; j < 8; ++j) ((unsigned short*)(lds + L_Q))[lane + 64 * j] = (unsigned short)(b ? (qq[j] >> 16) : (qq[j] & 0xffffu));
  }
  bar();
  STAMP(4);
  u32x4 qpk[4];
#pragma unroll
  for (int g = 0; g < 4; ++g) qpk[g] = *reinterpret_cast<const u32x4*>((const unsigned short*)(lds + L_Q) + g * DH + 8 * cc);
  constexpr float C2 = 0.088388347648318440f * 1.4426950408889634f;
  float m = -INFINITY, l = 0.f, acc[32];
#pragma unroll
  for (int k = 0; k < 32; ++k) acc[k] = 0.f;
  float* prow = lds + L_P + (wave * 4 + r) * 16;
#define TR_STEP(HALF, CTRL, BIT) _Pragma("unroll") for (int h = 0; h < (HALF); ++h) { \
      const float lo_ = xx[h], hi_ = xx[h + (HALF)]; const float t1_ = lo_ + dpp<CTRL>(lo_), t2_ = hi_ + dpp<CTRL>(hi_); \
      xx[h] = (cc & (BIT)) ? t2_ : t1_; }
#define PROCESS(KX, VX) do { \
    float xx[16]; \
    _Pragma("unroll") for (int t = 0; t < 4; ++t) _Pragma("unroll") for (int g = 0; g < 4; ++g) { float d = 0.f; \
        _Pragma("unroll") for (int k = 0; k < 4; ++k) d = dot2(KX[t][k], qpk[g][k], d); xx[t * 4 + g] = d; } \
    TR_STEP(8, XOR8, 8) TR_STEP(4, XOR7, 4) TR_STEP(2, XOR2, 2) TR_STEP(1, XOR1, 1) \
    const float sc = xx[0] * C2; \
    float pm = sc; pm = fmaxf(pm, dpp<ROR4>(pm)); pm = fmaxf(pm, dpp<XOR8>(pm)); pm = xrow16_max(pm); \
    const float mn = fmaxf(m, pm); \
    const float alpha = __builtin_amdgcn_exp2f(m - mn), p = __builtin_amdgcn_exp2f(sc - mn); \
    l = fmaf(l, alpha, p); m = mn; \
    _Pragma("unroll") for (int q4 = 0; q4 < 4; ++q4) { \
      const float al = q4 == 0 ? dpp<0x00>(alpha) : q4 == 1 ? dpp<0x55>(alpha) : q4 == 2 ? dpp<0xAA>(alpha) : dpp<0xFF>(alpha); \
      _Pragma("unroll") for (int k = 0; k < 8; ++k) acc[q4 * 8 + k] *= al; } \
    prow[cc] = p; \
    asm volatile("s_waitcnt lgkmcnt(0)" ::: "memory"); \
    f32x4 pr[4]; \
    _Pragma("unroll") for (int u = 0; u < 4; ++u) pr[u] = *reinterpret_cast<const f32x4*>(prow + 4 * u); \
    _Pragma("unroll") for (int t = 0; t < 4; ++t) { \
      float vf[8]; \
      _Pragma("unroll") for (int k = 0; k < 4; ++k) { vf[2 * k] = bflo(VX[t][k]); vf[2 * k + 1] = bfhi(VX[t][k]); } \
      _Pragma("unroll") for (int g = 0; g < 4; ++g) { const float pg_ = pr[t][g]; \
        _Pragma("unroll") for (int k = 0; k < 8; ++k) acc[g * 8 + k] = fmaf(pg_, vf[k], acc[g * 8 + k]); } } \
  } while (0)
#define STASH(it_) do { \
    if ((it_) == 1) { unsigned st[8]; \
      _Pragma("unroll") for (int i = 0; i < 8; ++i) st[i] = *reinterpret_cast<const unsigned*>(wo + (size_t)(tid + 512 * i) * DM + col0); \
      *reinterpret_cast<u32x4*>(lds + L_WST + (0 * 512 + tid) * 4) = u32x4{st[0], st[1], st[2], st[3]}; \
      *reinterpret_cast<u32x4*>(lds + L_WST + (1 * 512 + tid) * 4) = u32x4{st[4], st[5], st[6], st[7]}; \
    } else if ((it_) == 2) { \
      const u32x4 a_ = *reinterpret_cast<const u32x4*>(wg + (size_t)tid * F + 8 * wgu), b_ = *reinterpret_cast<const u32x4*>(wu + (size_t)tid * F + 8 * wgu); \
      *reinterpret_cast<u32x4*>(lds + L_WST + (2 * 512 + tid) * 4) = a_; \
      *reinterpret_cast<u32x4*>(lds + L_WST + (3 * 512 + tid) * 4) = b_; \
    } else if ((it_) == 3) { unsigned st[4]; \
      _Pragma("unroll") for (int i = 0; i < 4; ++i) { const int k_ = tid + 512 * i; st[i] = *reinterpret_cast<const unsigned*>(wd + (size_t)(k_ < F ? k_ : 0) * DM + col0); } \
      *reinterpret_cast<u32x4*>(lds + L_WST + (4 * 512 + tid) * 4) = u32x4{st[0], st[1], st[2], st[3]}; \
    } } while (0)
#define WAVE_REDUCE() do { _Pragma("unroll") for (int k = 0; k < 32; ++k) acc[k] = xrow16_sum(acc[k]); \
    { float t = l; t += dpp<ROR4>(t); t += dpp<XOR8>(t); l = xrow16_sum(t); } } while (0)
#define ISSUE_SEL(KX, VX, pg_, ok_) do { const size_t off_ = (size_t)(pg_) * PAGE * DH; \
    const bf16* kp_ = (ok_) ? kb + off_ : kc; const bf16* vp_ = (ok_) ? vb + off_ : vc; \
    _Pragma("unroll") for (int t_ = 0; t_ < 4; ++t_) { KX[t_] = __builtin_nontemporal_load(reinterpret_cast<const u32x4*>(kp_ + t_ * 4 * DH)); VX[t_] = __builtin_nontemporal_load(reinterpret_cast<const u32x4*>(vp_ + t_ * 4 * DH)); } } while (0)
  constexpr int FBASE = 16 * SPG;
  float* mrg = lds + L_MRG;
  bool is_static = wave != 0;
  int segleft, pa, pb, step, curfc = 0;
  bool go = true;
  if (wave != 0) { segleft = 8; pa = pfirst; pb = pfirst + 7; step = 14; }
  else {
    { unsigned t0 = 0; TICKET_RAW(t0); curfc = (int)__builtin_amdgcn_readfirstlane(t0); }
    go = curfc < NFL;
    segleft = FCH / 2; pa = FBASE + curfc * FCH; pb = pa + 1; step = 2;
    ISSUE_SEL(KA, VA, pa, go);
    ISSUE_SEL(KB, VB, pb, go);
    if (r == 0) {
#pragma unroll
      for (int g = 0; g < 4; ++g) { float* rec = mrg + g * 132;
        *reinterpret_cast<f32x4*>(rec + 4 + 8 * cc) = f32x4{0.f, 0.f, 0.f, 0.f}; *reinterpret_cast<f32x4*>(rec + 8 + 8 * cc) = f32x4{0.f, 0.f, 0.f, 0.f};
        if (cc == g) { rec[0] = -INFINITY; rec[1] = 0.f; } } }
  }
  unsigned tkraw = 0; TICKET_RAW(tkraw);
  int itc = 0;
  while (go) {
    PROCESS(KA, VA);
    const bool last = segleft == 1;
    int na, nb, nfc = 0; bool nvalid = true;
    if (!last) { na = pa + step; nb = pb + step; }
    else { nfc = (int)__builtin_amdgcn_readfirstlane(tkraw); nvalid = nfc < NFL; na = FBASE + nfc * FCH; nb = na + 1; }
    ISSUE_SEL(KA, VA, na, nvalid);
    STASH(itc); ++itc;
    PROCESS(KB, VB);
    ISSUE_SEL(KB, VB, nb, nvalid);
    if (last) {
      WAVE_REDUCE();
      if (is_static) {
        if (r == 0) {
#pragma unroll
          for (int g = 0; g < 4; ++g) {
            float* rec = mrg + (wave * 4 + g) * 132;
#pragma unroll
            for (int k = 0; k < 8; k += 4) *reinterpret_cast<f32x4*>(rec + 4 + 8 * cc + k) = f32x4{acc[g * 8 + k], acc[g * 8 + k + 1], acc[g * 8 + k + 2], acc[g * 8 + k + 3]};
            if (cc == g) { rec[0] = m; rec[1] = l; } } }
      } else {
        const float mg = __shfl(m, r), lg = __shfl(l, r);
        u64* dst = g3 + (size_t)((kvh * 4 + r) * 8 + (cc >> 1)) * RECW + (b * NREC + 16 + curfc) * 18;
        float av[8];
#pragma unroll
        for (int k = 0; k < 8; ++k) av[k] = r == 0 ? acc[k] : r == 1 ? acc[8 + k] : r == 2 ? acc[16 + k] : acc[24 + k];
#pragma unroll
        for (int k = 0; k < 8; ++k) put(dst + 2 + (cc & 1) * 8 + k, tag, __float_as_uint(av[k]));
        if ((cc & 1) == 0) { put(dst, tag, __float_as_uint(mg)); put(dst + 1, tag, __float_as_uint(lg)); }
      }
      if (!nvalid) break;
      m = -INFINITY; l = 0.f;
#pragma unroll
      for (int k = 0; k < 32; ++k) acc[k] = 0.f;
      is_static = false; curfc = nfc; segleft = FCH / 2; step = 2;
      TICKET_RAW(tkraw);
    } else --segleft;
    pa = na; pb = nb;
  }
  for (int j = itc; j <= 3; ++j) STASH(j);
#undef WAVE_REDUCE
#undef PROCESS
#undef TR_STEP
#undef ISSUE
  STAMP(5);
  bar();
  { const int g = tid >> 7, d = tid & 127;
    float M = -INFINITY;
#pragma unroll
    for (int ww = 0; ww < NW; ++ww) M = fmaxf(M, mrg[(ww * 4 + g) * 132]);
    float L = 0.f, o0 = 0.f;
#pragma unroll
    for (int ww = 0; ww < NW; ++ww) { const float* rec = mrg + (ww * 4 + g) * 132;
      const float wgt = __builtin_amdgcn_exp2f(rec[0] - M);
      L = fmaf(wgt, rec[1], L); o0 = fmaf(wgt, rec[4 + d], o0); }
    u64* dst = g3 + (size_t)((kvh * 4 + g) * 8 + (d >> 4)) * RECW + (b * NREC + s) * 18;
    put(dst + 2 + (d & 15), tag, __float_as_uint(o0));
    if ((d & 15) == 0) { put(dst, tag, __float_as_uint(M)); put(dst + 1, tag, __float_as_uint(L)); }
  }
  STAMP(6);
  { unsigned v[3]; sweep<3>(g3 + (size_t)w * RECW + tid, 512, tag, v);
#pragma unroll
    for (int k = 0; k < 3; ++k) lds[L_PART + 512 * k + tid] = __uint_as_float(v[k]);
    if (wave < 3) { unsigned v1[1]; sweep<1>(g3 + (size_t)w * RECW + 1536 + tid, 0, tag, v1); lds[L_PART + 1536 + tid] = __uint_as_float(v1[0]); } }
  bar();
  STAMP(7);
  if (tid < 32) {
    const int bb = tid >> 4, d = tid & 15;
    const float* pp = lds + L_PART + bb * NREC * 18;
    float M = -INFINITY;
#pragma unroll
    for (int ss = 0; ss < NREC; ++ss) M = fmaxf(M, pp[ss * 18]);
    float L = 0.f, o0 = 0.f;
#pragma unroll
    for (int ss = 0; ss < NREC; ++ss) { const float wgt = __builtin_amdgcn_exp2f(pp[ss * 18] - M);
      L = fmaf(wgt, pp[ss * 18 + 1], L); o0 = fmaf(wgt, pp[ss * 18 + 2 + d], o0); }
    const unsigned ob = cvtpk(o0 * frcp(L), 0.f) & 0xffffu;
    const unsigned other = __shfl_down(ob, 16);
    if (tid < 16) put(g4 + w * 16 + tid, tag, ob | (other << 16));
  }
  STAMP(8);
  float x1own0 = 0.f, x1own1 = 0.f;
  { unsigned ov[8]; sweep<8>(g4 + tid, 512, tag, ov);
    STAMP(9);
    float a00 = 0.f, a01 = 0.f, a10 = 0.f, a11 = 0.f;
#pragma unroll
    for (int i = 0; i < 8; ++i) {
      const unsigned wv = ((const unsigned*)(lds + L_WST))[((i >> 2) * 512 + tid) * 4 + (i & 3)];
      const float w0 = bflo(wv), w1 = bfhi(wv), o0 = bflo(ov[i]), o1 = bfhi(ov[i]);
      a00 = fmaf(o0, w0, a00); a01 = fmaf(o0, w1, a01); a10 = fmaf(o1, w0, a10); a11 = fmaf(o1, w1, a11);
    }
    a00 = wave_sum(a00); a01 = wave_sum(a01); a10 = wave_sum(a10); a11 = wave_sum(a11);
    if (lane == 0) *reinterpret_cast<f32x4*>(lds + L_RED + wave * 4) = f32x4{a00, a01, a10, a11};
    bar();
    if (tid < 2) {
      float a0 = 0.f, a1 = 0.f;
#pragma unroll
      for (int ww = 0; ww < NW; ++ww) { a0 += lds[L_RED + ww * 4 + tid]; a1 += lds[L_RED + ww * 4 + 2 + tid]; }
      const int n = col0 + tid;
      const unsigned ga = ((const unsigned*)(lds + L_MODS))[1024 + tid];
      x1own0 = rbf(bf2f(xs[n]) + rbf(bflo(ga) * rbf(a0)));
      x1own1 = rbf(bf2f(xs[512 + n]) + rbf(bfhi(ga) * rbf(a1)));
      put(g5 + n, tag, cvtpk(x1own0, x1own1));
    } }
  STAMP(10);
  { unsigned v[1]; sweep<1>(g5 + tid, 0, tag, v);
    STAMP(11);
    const float y0 = bflo(v[0]), y1 = bfhi(v[0]);
    lds[L_X + tid] = y0; lds[L_X + 512 + tid] = y1;
    bar();
    if (has_gu) {
      float m0, m1; ln_norm(lds, lane, y0, y1, m0, m1);
      const unsigned sh = ((const unsigned*)(lds + L_MODS))[1536 + tid], sc = ((const unsigned*)(lds + L_MODS))[2048 + tid];
      const float a0 = rbf(rbf(m0 * rbf(1.f + bflo(sc))) + bflo(sh)), a1 = rbf(rbf(m1 * rbf(1.f + bfhi(sc))) + bfhi(sh));
      const u32x4 wgv = *reinterpret_cast<const u32x4*>(lds + L_WST + (2 * 512 + tid) * 4), wuv = *reinterpret_cast<const u32x4*>(lds + L_WST + (3 * 512 + tid) * 4);
      float v2[32];
#pragma unroll
      for (int j = 0; j < 4; ++j) {
        v2[2 * j] = a0 * bflo(wgv[j]); v2[2 * j + 1] = a0 * bfhi(wgv[j]); v2[8 + 2 * j] = a0 * bflo(wuv[j]); v2[8 + 2 * j + 1] = a0 * bfhi(wuv[j]);
        v2[16 + 2 * j] = a1 * bflo(wgv[j]); v2[16 + 2 * j + 1] = a1 * bfhi(wgv[j]); v2[24 + 2 * j] = a1 * bflo(wuv[j]); v2[24 + 2 * j + 1] = a1 * bfhi(wuv[j]);
      }
#pragma unroll
      for (int h = 0; h < 16; ++h) { auto sw = __builtin_amdgcn_permlane32_swap(__float_as_uint(v2[h]), __float_as_uint(v2[h + 16]), false, false); v2[h] = __uint_as_float(sw[0]) + __uint_as_float(sw[1]); }
#pragma unroll
      for (int h = 0; h < 8; ++h) { auto sw = __builtin_amdgcn_permlane16_swap(__float_as_uint(v2[h]), __float_as_uint(v2[h + 8]), false, false); v2[h] = __uint_as_float(sw[0]) + __uint_as_float(sw[1]); }
#define TRS(HALF, CTRL, BIT) _Pragma("unroll") for (int h = 0; h < (HALF); ++h) { \
      const float lo_ = v2[h], hi_ = v2[h + (HALF)]; const float t1_ = lo_ + dpp<CTRL>(lo_), t2_ = hi_ + dpp<CTRL>(hi_); \
      v2[h] = (lane & (BIT)) ? t2_ : t1_; }
      TRS(4, XOR8, 8) TRS(2, XOR7, 4) TRS(1, XOR2, 2)
#undef TRS
      v2[0] += dpp<XOR1>(v2[0]);
      if ((lane & 1) == 0) lds[L_RED + wave * 32 + (lane >> 1)] = v2[0];
    }
    bar();
    if (has_gu && tid < 8) {
      float g0 = 0.f, u0 = 0.f, g1s = 0.f, u1 = 0.f;
#pragma unroll
      for (int ww = 0; ww < NW; ++ww) { g0 += lds[L_RED + ww * 32 + tid]; u0 += lds[L_RED + ww * 32 + 8 + tid]; g1s += lds[L_RED + ww * 32 + 16 + tid]; u1 += lds[L_RED + ww * 32 + 24 + tid]; }
      put(g6 + 8 * wgu + tid, tag, cvtpk(rbf(g0) * rbf(u0), rbf(g1s) * rbf(u1)));
    } }
  STAMP(12);
  { unsigned hv[4];
    for (unsigned n = 0;; ++n) {
      bool ok = true;
#pragma unroll
      for (int i = 0; i < 4; ++i) { const int k = tid + 512 * i; const u64 a = __hip_atomic_load(g6 + (k < F ? k : 0), __ATOMIC_RELAXED, __HIP_MEMORY_SCOPE_AGENT); hv[i] = k < F ? (unsigned)a : 0u; ok &= (unsigned)(a >> 32) == tag; }
      if (__all(ok) || n > SPIN_CAP) break;
      __builtin_amdgcn_s_sleep(1);
    }
    STAMP(13);
    float a00 = 0.f, a01 = 0.f, a10 = 0.f, a11 = 0.f;
    const u32x4 wdv = *reinterpret_cast<const u32x4*>(lds + L_WST + (4 * 512 + tid) * 4);
#pragma unroll
    for (int i = 0; i < 4; ++i) {
      const float w0 = bflo(wdv[i]), w1 = bfhi(wdv[i]), o0 = bflo(hv[i]), o1 = bfhi(hv[i]);
      a00 = fmaf(o0, w0, a00); a01 = fmaf(o0, w1, a01); a10 = fmaf(o1, w0, a10); a11 = fmaf(o1, w1, a11);
    }
    a00 = wave_sum(a00); a01 = wave_sum(a01); a10 = wave_sum(a10); a11 = wave_sum(a11);
    if (lane == 0) *reinterpret_cast<f32x4*>(lds + L_RED + wave * 4) = f32x4{a00, a01, a10, a11};
    bar();
    if (tid < 2) {
      float a0 = 0.f, a1 = 0.f;
#pragma unroll
      for (int ww = 0; ww < NW; ++ww) { a0 += lds[L_RED + ww * 4 + tid]; a1 += lds[L_RED + ww * 4 + 2 + tid]; }
      const int n = col0 + tid;
      const unsigned gb = ((const unsigned*)(lds + L_MODS))[1026 + tid];
      out[n] = __float2bfloat16(x1own0 + rbf(bflo(gb) * rbf(a0)));
      out[512 + n] = __float2bfloat16(x1own1 + rbf(bfhi(gb) * rbf(a1)));
    }
    if (w == 0 && tid == 0) __hip_atomic_store((unsigned*)(ws + WS_EPOCH), tag, __ATOMIC_RELAXED, __HIP_MEMORY_SCOPE_AGENT);
  }
  STAMP(14);
}

extern "C" void kernel_launch(void* const* d_in, const int* in_sizes, int n_in, void* d_out, int out_size, void* d_ws, size_t ws_size, hipStream_t stream) {
  (void)hipFuncSetAttribute((const void*)mega, hipFuncAttributeMaxDynamicSharedMemorySize, L_TOTAL * 4);
  mega<<<NWG, NT, L_TOTAL * 4, stream>>>((const bf16*)d_in[0], (const bf16*)d_in[1], (const bf16*)d_in[2], (const bf16*)d_in[3], (const bf16*)d_in[4],
                                        (const bf16*)d_in[6], (const bf16*)d_in[7], (const bf16*)d_in[8], (const bf16*)d_in[9], (const bf16*)d_in[10],
                                        (bf16*)d_out, (char*)d_ws);
}
```

```cpp
#include <hip/hip_runtime.h>
#include <hip/hip_bf16.h>
#include <cstdint>

using bf16 = __hip_bfloat16;
typedef unsigned long long u64;
constexpr int B = 2, DM = 512, HQ = 32, HKV = 8, G = 4, SK = 32768, DH = 128, F = 1792;
constexpr int NQ = HQ * DH;
constexpr int NWG = 256, NT = 512, NW = 8;
constexpr int NSPLIT = 16, PAGE = 16;
constexpr int NPG = SK / PAGE;
constexpr int SPG = 106;
constexpr int FCH = 8;
constexpr int NFL = (NPG - 16 * SPG) / FCH;
constexpr int NREC = 16 + NFL;
constexpr int RECW = 2 * NREC * 18;
constexpr int NGU = F / 8;
constexpr unsigned SPIN_CAP = 1u << 22;

using u32x4 = __attribute__((ext_vector_type(4))) unsigned;
using f32x4 = __attribute__((ext_vector_type(4))) float;
using bf16x2 = __attribute__((ext_vector_type(2))) __bf16;

constexpr size_t WS_G1 = 0;
constexpr size_t WS_G2 = 32768;
constexpr size_t WS_G4 = 65536;
constexpr size_t WS_G5 = 98304;
constexpr size_t WS_G6 = 102400;
constexpr size_t WS_EPOCH = 131072;
constexpr size_t WS_CNT = 135168;
constexpr size_t WS_G3 = 1u << 20;
constexpr size_t WS_TS = 8u << 20;

__device__ __forceinline__ float bf2f(unsigned h) { return __builtin_bit_cast(float, h << 16); }
__device__ __forceinline__ float bflo(unsigned w) { return __builtin_bit_cast(float, w << 16); }
__device__ __forceinline__ float bfhi(unsigned w) { return __builtin_bit_cast(float, w & 0xffff0000u); }
typedef __attribute__((ext_vector_type(2))) float f32x2;
__device__ __forceinline__ unsigned cvtpk(float lo, float hi) {
  return __builtin_bit_cast(unsigned, __builtin_convertvector(f32x2{lo, hi}, bf16x2));
}
__device__ __forceinline__ float rbf(float f) { return __builtin_bit_cast(float, cvtpk(f, f) << 16); }
__device__ __forceinline__ float frcp(float x) { return __builtin_amdgcn_rcpf(x); }
__device__ __forceinline__ float dot2(unsigned k, unsigned q, float acc) {
  return __builtin_amdgcn_fdot2_f32_bf16(__builtin_bit_cast(bf16x2, k), __builtin_bit_cast(bf16x2, q), acc, false);
}
template <int CTRL> __device__ __forceinline__ float dpp(float x) {
  return __builtin_bit_cast(float, __builtin_amdgcn_mov_dpp(__builtin_bit_cast(int, x), CTRL, 0xf, 0xf, true));
}
constexpr int XOR1 = 0xB1, XOR2 = 0x4E, XOR7 = 0x141, XOR8 = 0x128, ROR4 = 0x124;
__device__ __forceinline__ float xrow16_max(float x) {
  auto s = __builtin_amdgcn_permlane16_swap(__float_as_uint(x), __float_as_uint(x), false, false);
  x = fmaxf(__uint_as_float(s[0]), __uint_as_float(s[1]));
  auto t = __builtin_amdgcn_permlane32_swap(__float_as_uint(x), __float_as_uint(x), false, false);
  return fmaxf(__uint_as_float(t[0]), __uint_as_float(t[1]));
}
__device__ __forceinline__ float xrow16_sum(float x) {
  auto s = __builtin_amdgcn_permlane16_swap(__float_as_uint(x), __float_as_uint(x), false, false);
  x = __uint_as_float(s[0]) + __uint_as_float(s[1]);
  auto t = __builtin_amdgcn_permlane32_swap(__float_as_uint(x), __float_as_uint(x), false, false);
  return __uint_as_float(t[0]) + __uint_as_float(t[1]);
}
__device__ __forceinline__ float wave_sum(float x) {
  x += dpp<XOR1>(x); x += dpp<XOR2>(x); x += dpp<XOR7>(x); x += dpp<XOR8>(x);
  return xrow16_sum(x);
}
__device__ __forceinline__ float par_sum(float x) {
  x += dpp<XOR2>(x); x += dpp<ROR4>(x); x += dpp<XOR8>(x);
  return xrow16_sum(x);
}
__device__ __forceinline__ void bar() { asm volatile("s_waitcnt lgkmcnt(0)\n\ts_barrier" ::: "memory"); }
__device__ __forceinline__ void put(u64* p, unsigned tag, unsigned v) {
  __hip_atomic_store(p, ((u64)tag << 32) | v, __ATOMIC_RELAXED, __HIP_MEMORY_SCOPE_AGENT);
}
template <int N>
__device__ __forceinline__ void sweep(const u64* p, int stride, unsigned tag, unsigned (&v)[N]) {
  for (unsigned n = 0;; ++n) {
    bool ok = true;
#pragma unroll
    for (int k = 0; k < N; ++k) { const u64 a = __hip_atomic_load(p + k * stride, __ATOMIC_RELAXED, __HIP_MEMORY_SCOPE_AGENT); v[k] = (unsigned)a; ok &= (unsigned)(a >> 32) == tag; }
    if (__all(ok) || n > SPIN_CAP) break;
    __builtin_amdgcn_s_sleep(1);
  }
}

#define STAMP(i) do { } while (0)

constexpr int L_A = 0;
constexpr int L_X = 1024;
constexpr int L_RED = 2048;
constexpr int L_RES = 2304;
constexpr int L_Q = 2368;
constexpr int L_P = 2624;
constexpr int L_TICK = 3136;
constexpr int L_MODS = 3140;
constexpr int L_PART = L_MODS + 3072;
constexpr int L_WST = L_PART + 2176;
constexpr int L_MRG = L_WST + 20 * 512;
constexpr int L_TOTAL = L_MRG + 4224 + 6144;

__device__ __forceinline__ void ln_norm(const float* lds, int lane, float x0, float x1, float& n0, float& n1) {
#pragma unroll
  for (int bb = 0; bb < 2; ++bb) {
    float v[8], s = 0.f;
#pragma unroll
    for (int j = 0; j < 8; ++j) { v[j] = lds[L_X + bb * 512 + lane + 64 * j]; s += v[j]; }
    const float mu = wave_sum(s) * (1.f / DM);
    float q = 0.f;
#pragma unroll
    for (int j = 0; j < 8; ++j) { const float d = v[j] - mu; q += d * d; }
    const float var = rbf(wave_sum(q) * (1.f / DM));
    const float sd = rbf(__builtin_amdgcn_sqrtf(rbf(var + 1e-5f)));
    const float t = rbf(rbf((bb ? x1 : x0) - rbf(mu)) * frcp(sd));
    if (bb) n1 = t; else n0 = t;
  }
}
__device__ __forceinline__ void gemv16_part(float* lds, int tid, const u32x4* wv) {
  const int lane = tid & 63, wave = tid >> 6;
  float acc[16];
#pragma unroll
  for (int j = 0; j < 16; ++j) acc[j] = 0.f;
#pragma unroll
  for (int i = 0; i < 2; ++i) {
    const int row = (tid >> 1) + 256 * i;
    const float a0 = lds[L_A + row], a1 = lds[L_A + 512 + row];
#pragma unroll
    for (int j = 0; j < 4; ++j) {
      const float lo = bflo(wv[i][j]), hi = bfhi(wv[i][j]);
      acc[2 * j] = fmaf(a0, lo, acc[2 * j]); acc[2 * j + 1] = fmaf(a0, hi, acc[2 * j + 1]);
      acc[8 + 2 * j] = fmaf(a1, lo, acc[8 + 2 * j]); acc[8 + 2 * j + 1] = fmaf(a1, hi, acc[8 + 2 * j + 1]);
    }
  }
#pragma unroll
  for (int h = 0; h < 8; ++h) { auto sw = __builtin_amdgcn_permlane32_swap(__float_as_uint(acc[h]), __float_as_uint(acc[h + 8]), false, false); acc[h] = __uint_as_float(sw[0]) + __uint_as_float(sw[1]); }
#pragma unroll
  for (int h = 0; h < 4; ++h) { auto sw = __builtin_amdgcn_permlane16_swap(__float_as_uint(acc[h]), __float_as_uint(acc[h + 4]), false, false); acc[h] = __uint_as_float(sw[0]) + __uint_as_float(sw[1]); }
#pragma unroll
  for (int h = 0; h < 4; ++h) { float v = acc[h]; v += dpp<XOR2>(v); v += dpp<ROR4>(v); v += dpp<XOR8>(v); acc[h] = v; }
  if ((lane & 14) == 0) *reinterpret_cast<f32x4*>(lds + L_RED + wave * 32 + (lane & 1) * 16 + (lane >> 4) * 4) = f32x4{acc[0], acc[1], acc[2], acc[3]};
}
__device__ __forceinline__ unsigned gemv16_fin(const float* lds, int t) {
  const int half = t >> 3, j = t & 7;
  float s0 = 0.f, s1 = 0.f;
#pragma unroll
  for (int ww = 0; ww < 8; ++ww) { s0 += lds[L_RED + ww * 32 + half * 16 + j]; s1 += lds[L_RED + ww * 32 + half * 16 + 8 + j]; }
  return cvtpk(s0, s1);
}

__global__ void __launch_bounds__(NT, 2) mega(const bf16* __restrict__ x, const bf16* __restrict__ kc, const bf16* __restrict__ vc,
                                              const bf16* __restrict__ wq, const bf16* __restrict__ wo, const bf16* __restrict__ wg,
                                              const bf16* __restrict__ wu, const bf16* __restrict__ wd, const bf16* __restrict__ c,
                                              const bf16* __restrict__ w_mod, bf16* __restrict__ out, char* __restrict__ ws) {
  extern __shared__ __attribute__((aligned(16))) float lds[];
  const int tid = threadIdx.x, lane = tid & 63, wave = __builtin_amdgcn_readfirstlane(tid >> 6);
  const int w = blockIdx.x;
  const unsigned tag = __hip_atomic_load((const unsigned*)(ws + WS_EPOCH), __ATOMIC_RELAXED, __HIP_MEMORY_SCOPE_AGENT) + 1u;
  u64* g1 = (u64*)(ws + WS_G1); u64* g2 = (u64*)(ws + WS_G2); u64* g3 = (u64*)(ws + WS_G3);
  u64* g4 = (u64*)(ws + WS_G4); u64* g5 = (u64*)(ws + WS_G5); u64* g6 = (u64*)(ws + WS_G6);
  u64* ts = (u64*)(ws + WS_TS); (void)ts;
  const int pair = w >> 4, b = pair >> 3, kvh = pair & 7, s = w & 15;
  const int r = lane >> 4, cc = lane & 15;
  const int col0 = 2 * ((w & 7) * 32 + (w >> 3));
  STAMP(0);
  const unsigned short* cs = (const unsigned short*)c; const unsigned short* xs = (const unsigned short*)x;
  const int wx = w & 7, wi = w >> 3;
  const bool has_mod = wi < 24;
  const int mslice = wi < 8 ? wx * 8 + wi : (wi < 24 ? 64 + wx * 16 + (wi - 8) : 0);
  const int qslice = wx * 32 + wi;
  const bool has_gu = wi < 28;
  const int wgu = has_gu ? wx * 28 + wi : 0;
  unsigned* cnt = (unsigned*)(ws + WS_CNT + (size_t)pair * 128);
  if (w == 0 && tid < 16) __hip_atomic_store((unsigned*)(ws + WS_CNT + (size_t)tid * 128), 0u, __ATOMIC_RELAXED, __HIP_MEMORY_SCOPE_AGENT);
  const float c0 = bf2f(cs[tid]), c1 = bf2f(cs[512 + tid]);
  const float x0 = bf2f(xs[tid]), x1 = bf2f(xs[512 + tid]);
  u32x4 wm[2], wqv[2];
#pragma unroll
  for (int i = 0; i < 2; ++i) {
    const int row = (tid >> 1) + 256 * i;
    wm[i] = *reinterpret_cast<const u32x4*>(w_mod + (size_t)row * (6 * DM) + mslice * 16 + (tid & 1) * 8);
    wqv[i] = *reinterpret_cast<const u32x4*>(wq + (size_t)row * NQ + qslice * 16 + (tid & 1) * 8);
  }
  lds[L_A + tid] = rbf(c0 * frcp(1.f + __expf(-c0))); lds[L_A + 512 + tid] = rbf(c1 * frcp(1.f + __expf(-c1)));
  lds[L_X + tid] = x0; lds[L_X + 512 + tid] = x1;
  bar();
  gemv16_part(lds, tid, wm);
  float n0, n1;
  ln_norm(lds, lane, x0, x1, n0, n1);
  bar();
  if (has_mod && tid < 16) { const unsigned rr = gemv16_fin(lds, tid); asm volatile("s_waitcnt vmcnt(0)" ::: "memory"); put(g1 + mslice * 16 + tid, tag, rr); }
  STAMP(1);
  const size_t base = ((size_t)(b * HKV + kvh) * SK) * DH + (size_t)r * DH + 8 * cc;
  const bf16* kb = kc + base; const bf16* vb = vc + base;
  u32x4 KA[4], VA[4], KB[4], VB[4];
#define ISSUE(KX, VX, pg_) do { const size_t off_ = (size_t)(pg_) * PAGE * DH; \
    _Pragma("unroll") for (int t_ = 0; t_ < 4; ++t_) { KX[t_] = __builtin_nontemporal_load(reinterpret_cast<const u32x4*>(kb + off_ + t_ * 4 * DH)); VX[t_] = __builtin_nontemporal_load(reinterpret_cast<const u32x4*>(vb + off_ + t_ * 4 * DH)); } } while (0)
  unsigned st[20];
#define STASH_LOAD() do { \
    _Pragma("unroll") for (int i = 0; i < 8; ++i) st[i] = *reinterpret_cast<const unsigned*>(wo + (size_t)(tid + 512 * i) * DM + col0); \
    { const u32x4 a_ = *reinterpret_cast<const u32x4*>(wg + (size_t)tid * F + 8 * wgu); st[8] = a_[0]; st[9] = a_[1]; st[10] = a_[2]; st[11] = a_[3]; } \
    { const u32x4 a_ = *reinterpret_cast<const u32x4*>(wu + (size_t)tid * F + 8 * wgu); st[12] = a_[0]; st[13] = a_[1]; st[14] = a_[2]; st[15] = a_[3]; } \
    _Pragma("unroll") for (int i = 0; i < 4; ++i) { const int k_ = tid + 512 * i; st[16 + i] = *reinterpret_cast<const unsigned*>(wd + (size_t)(k_ < F ? k_ : 0) * DM + col0); } } while (0)
#define STASH_STORE() do { _Pragma("unroll") for (int i = 0; i < 20; i += 4) *reinterpret_cast<u32x4*>(lds + L_WST + (i * 128 + tid) * 4 - 0 + 0 * i) = u32x4{st[i], st[i + 1], st[i + 2], st[i + 3]}; } while (0)
  const int sbase = s * SPG;
  const int pfirst = wave ? sbase + wave - 1 : sbase + 98;
#define TICKET_RAW(dst_) do { if (lane == 0) dst_ = __hip_atomic_fetch_add(cnt, 1u, __ATOMIC_RELAXED, __HIP_MEMORY_SCOPE_AGENT); } while (0)
  if (wave != 0) {
    ISSUE(KA, VA, pfirst);
  } else {
    unsigned v[16];
    sweep<16>(g1 + lane, 64, tag, v);
#pragma unroll
    for (int j = 0; j < 16; ++j) ((unsigned*)(lds + L_MODS))[lane + 64 * j] = v[j];
  }
  bar();
  STAMP(2);
  { const unsigned sh = ((const unsigned*)(lds + L_MODS))[tid], sc = ((const unsigned*)(lds + L_MODS))[512 + tid];
    lds[L_A + tid] = rbf(rbf(n0 * rbf(1.f + bflo(sc))) + bflo(sh));
    lds[L_A + 512 + tid] = rbf(rbf(n1 * rbf(1.f + bfhi(sc))) + bfhi(sh)); }
  bar();
  gemv16_part(lds, tid, wqv);
  bar();
  if (wave != 0) {
    ISSUE(KB, VB, pfirst + 7);
  } else {
    if (tid < 16) put(g2 + qslice * 16 + tid, tag, gemv16_fin(lds, tid));
    STAMP(3);
    { unsigned v[16];
      sweep<16>(g1 + 1536 + lane, 64, tag, v);
#pragma unroll
      for (int j = 0; j < 16; ++j) ((unsigned*)(lds + L_MODS))[1536 + lane + 64 * j] = v[j]; }
    { unsigned v[1];
      sweep<1>(g1 + (lane < 2 ? 1024 + col0 + lane : 2560 + col0 + (lane & 1)), 0, tag, v);
      if (lane < 4) ((unsigned*)(lds + L_MODS))[1024 + lane] = v[0]; }
    unsigned qq[8];
    sweep<8>(g2 + kvh * 512 + lane, 64, tag, qq);
#pragma unroll
    for (int j = 0; j < 8; ++j) ((unsigned short*)(lds + L_Q))[lane + 64 * j] = (unsigned short)(b ? (qq[j] >> 16) : (qq[j] & 0xffffu));
  }
  bar();
  STAMP(4);
  u32x4 qpk[4];
#pragma unroll
  for (int g = 0; g < 4; ++g) qpk[g] = *reinterpret_cast<const u32x4*>((const unsigned short*)(lds + L_Q) + g * DH + 8 * cc);
  constexpr float C2 = 0.088388347648318440f * 1.4426950408889634f;
  float m = -INFINITY, l = 0.f, acc[32];
#pragma unroll
  for (int k = 0; k < 32; ++k) acc[k] = 0.f;
  float* prow = lds + L_P + (wave * 4 + r) * 16;
#define TR_STEP(HALF, CTRL, BIT) _Pragma("unroll") for (int h = 0; h < (HALF); ++h) { \
      const float lo_ = xx[h], hi_ = xx[h + (HALF)]; const float t1_ = lo_ + dpp<CTRL>(lo_), t2_ = hi_ + dpp<CTRL>(hi_); \
      xx[h] = (cc & (BIT)) ? t2_ : t1_; }
#define PROCESS(KX, VX) do { \
    float xx[16]; \
    _Pragma("unroll") for (int t = 0; t < 4; ++t) _Pragma("unroll") for (int g = 0; g < 4; ++g) { float d = 0.f; \
        _Pragma("unroll") for (int k = 0; k < 4; ++k) d = dot2(KX[t][k], qpk[g][k], d); xx[t * 4 + g] = d; } \
    TR_STEP(8, XOR8, 8) TR_STEP(4, XOR7, 4) TR_STEP(2, XOR2, 2) TR_STEP(1, XOR1, 1) \
    const float sc = xx[0] * C2; \
    float pm = sc; pm = fmaxf(pm, dpp<ROR4>(pm)); pm = fmaxf(pm, dpp<XOR8>(pm)); pm = xrow16_max(pm); \
    const float mn = fmaxf(m, pm); \
    const float alpha = __builtin_amdgcn_exp2f(m - mn), p = __builtin_amdgcn_exp2f(sc - mn); \
    l = fmaf(l, alpha, p); m = mn; \
    _Pragma("unroll") for (int q4 = 0; q4 < 4; ++q4) { \
      const float al = q4 == 0 ? dpp<0x00>(alpha) : q4 == 1 ? dpp<0x55>(alpha) : q4 == 2 ? dpp<0xAA>(alpha) : dpp<0xFF>(alpha); \
      _Pragma("unroll") for (int k = 0; k < 8; ++k) acc[q4 * 8 + k] *= al; } \
    prow[cc] = p; \
    asm volatile("s_waitcnt lgkmcnt(0)" ::: "memory"); \
    f32x4 pr[4]; \
    _Pragma("unroll") for (int u = 0; u < 4; ++u) pr[u] = *reinterpret_cast<const f32x4*>(prow + 4 * u); \
    _Pragma("unroll") for (int t = 0; t < 4; ++t) { \
      float vf[8]; \
      _Pragma("unroll") for (int k = 0; k < 4; ++k) { vf[2 * k] = bflo(VX[t][k]); vf[2 * k + 1] = bfhi(VX[t][k]); } \
      _Pragma("unroll") for (int g = 0; g < 4; ++g) { const float pg_ = pr[t][g]; \
        _Pragma("unroll") for (int k = 0; k < 8; ++k) acc[g * 8 + k] = fmaf(pg_, vf[k], acc[g * 8 + k]); } } \
  } while (0)
#define STASH(it_) do { \
    if ((it_) == 1) { unsigned st[8]; \
      _Pragma("unroll") for (int i = 0; i < 8; ++i) st[i] = *reinterpret_cast<const unsigned*>(wo + (size_t)(tid + 512 * i) * DM + col0); \
      *reinterpret_cast<u32x4*>(lds + L_WST + (0 * 512 + tid) * 4) = u32x4{st[0], st[1], st[2], st[3]}; \
      *reinterpret_cast<u32x4*>(lds + L_WST + (1 * 512 + tid) * 4) = u32x4{st[4], st[5], st[6], st[7]}; \
    } else if ((it_) == 2) { \
      const u32x4 a_ = *reinterpret_cast<const u32x4*>(wg + (size_t)tid * F + 8 * wgu), b_ = *reinterpret_cast<const u32x4*>(wu + (size_t)tid * F + 8 * wgu); \
      *reinterpret_cast<u32x4*>(lds + L_WST + (2 * 512 + tid) * 4) = a_; \
      *reinterpret_cast<u32x4*>(lds + L_WST + (3 * 512 + tid) * 4) = b_; \
    } else if ((it_) == 3) { unsigned st[4]; \
      _Pragma("unroll") for (int i = 0; i < 4; ++i) { const int k_ = tid + 512 * i; st[i] = *reinterpret_cast<const unsigned*>(wd + (size_t)(k_ < F ? k_ : 0) * DM + col0); } \
      *reinterpret_cast<u32x4*>(lds + L_WST + (4 * 512 + tid) * 4) = u32x4{st[0], st[1], st[2], st[3]}; \
    } } while (0)
#define WAVE_REDUCE() do { _Pragma("unroll") for (int k = 0; k < 32; ++k) acc[k] = xrow16_sum(acc[k]); \
    { float t = l; t += dpp<ROR4>(t); t += dpp<XOR8>(t); l = xrow16_sum(t); } } while (0)
#define ISSUE_SEL(KX, VX, pg_, ok_) do { const size_t off_ = (size_t)(pg_) * PAGE * DH; \
    const bf16* kp_ = (ok_) ? kb + off_ : kc; const bf16* vp_ = (ok_) ? vb + off_ : vc; \
    _Pragma("unroll") for (int t_ = 0; t_ < 4; ++t_) { KX[t_] = __builtin_nontemporal_load(reinterpret_cast<const u32x4*>(kp_ + t_ * 4 * DH)); VX[t_] = __builtin_nontemporal_load(reinterpret_cast<const u32x4*>(vp_ + t_ * 4 * DH)); } } while (0)
  constexpr int FBASE = 16 * SPG;
  float* mrg = lds + L_MRG;
  bool is_static = true;
  int segleft, pa, pb, step, curfc = 0;
  const bool go = true;
  if (wave != 0) { segleft = 7; pa = pfirst; pb = pfirst + 7; step = 14; }
  else {
    segleft = 4; pa = pfirst; pb = pa + 1; step = 2;
    ISSUE(KA, VA, pa);
    ISSUE(KB, VB, pb);
  }
  unsigned tkraw = 0;
  int itc = 0;
  while (go) {
    PROCESS(KA, VA);
    const bool last = segleft == 1;
    int na, nb, nfc = 0; bool nvalid = true;
    if (!last) { na = pa + step; nb = pb + step; }
    else { nfc = (int)__builtin_amdgcn_readfirstlane(tkraw); nvalid = nfc < NFL; na = FBASE + nfc * FCH; nb = na + 1; }
    ISSUE_SEL(KA, VA, na, nvalid);
    if (segleft == 2) TICKET_RAW(tkraw);
    STASH(itc); ++itc;
    PROCESS(KB, VB);
    ISSUE_SEL(KB, VB, nb, nvalid);
    if (last) {
      WAVE_REDUCE();
      if (is_static) {
        if (r == 0) {
#pragma unroll
          for (int g = 0; g < 4; ++g) {
            float* rec = mrg + (wave * 4 + g) * 132;
#pragma unroll
            for (int k = 0; k < 8; k += 4) *reinterpret_cast<f32x4*>(rec + 4 + 8 * cc + k) = f32x4{acc[g * 8 + k], acc[g * 8 + k + 1], acc[g * 8 + k + 2], acc[g * 8 + k + 3]};
            if (cc == g) { rec[0] = m; rec[1] = l; } } }
      } else {
        const float mg = __shfl(m, r), lg = __shfl(l, r);
        u64* dst = g3 + (size_t)((kvh * 4 + r) * 8 + (cc >> 1)) * RECW + (b * NREC + 16 + curfc) * 18;
        float av[8];
#pragma unroll
        for (int k = 0; k < 8; ++k) av[k] = r == 0 ? acc[k] : r == 1 ? acc[8 + k] : r == 2 ? acc[16 + k] : acc[24 + k];
#pragma unroll
        for (int k = 0; k < 8; ++k) put(dst + 2 + (cc & 1) * 8 + k, tag, __float_as_uint(av[k]));
        if ((cc & 1) == 0) { put(dst, tag, __float_as_uint(mg)); put(dst + 1, tag, __float_as_uint(lg)); }
      }
      if (!nvalid) break;
      m = -INFINITY; l = 0.f;
#pragma unroll
      for (int k = 0; k < 32; ++k) acc[k] = 0.f;
      is_static = false; curfc = nfc; segleft = FCH / 2; step = 2;
    } else --segleft;
    pa = na; pb = nb;
  }
  for (int j = itc; j <= 3; ++j) STASH(j);
#undef WAVE_REDUCE
#undef PROCESS
#undef TR_STEP
#undef ISSUE
  STAMP(5);
  bar();
  { const int g = tid >> 7, d = tid & 127;
    float M = -INFINITY;
#pragma unroll
    for (int ww = 0; ww < NW; ++ww) M = fmaxf(M, mrg[(ww * 4 + g) * 132]);
    float L = 0.f, o0 = 0.f;
#pragma unroll
    for (int ww = 0; ww < NW; ++ww) { const float* rec = mrg + (ww * 4 + g) * 132;
      const float wgt = __builtin_amdgcn_exp2f(rec[0] - M);
      L = fmaf(wgt, rec[1], L); o0 = fmaf(wgt, rec[4 + d], o0); }
    u64* dst = g3 + (size_t)((kvh * 4 + g) * 8 + (d >> 4)) * RECW + (b * NREC + s) * 18;
    put(dst + 2 + (d & 15), tag, __float_as_uint(o0));
    if ((d & 15) == 0) { put(dst, tag, __float_as_uint(M)); put(dst + 1, tag, __float_as_uint(L)); }
  }
  STAMP(6);
  { unsigned v[4]; sweep<4>(g3 + (size_t)w * RECW + tid, 512, tag, v);
#pragma unroll
    for (int k = 0; k < 4; ++k) lds[L_PART + 512 * k + tid] = __uint_as_float(v[k]);
    if (wave < 2) { unsigned v1[1]; const int idx = 2048 + tid < RECW ? 2048 + tid : 0; sweep<1>(g3 + (size_t)w * RECW + idx, 0, tag, v1); if (2048 + tid < RECW) lds[L_PART + 2048 + tid] = __uint_as_float(v1[0]); } }
  bar();
  STAMP(7);
  if (tid < 32) {
    const int bb = tid >> 4, d = tid & 15;
    const float* pp = lds + L_PART + bb * NREC * 18;
    float M = -INFINITY;
#pragma unroll
    for (int ss = 0; ss < NREC; ++ss) M = fmaxf(M, pp[ss * 18]);
    float L = 0.f, o0 = 0.f;
#pragma unroll
    for (int ss = 0; ss < NREC; ++ss) { const float wgt = __builtin_amdgcn_exp2f(pp[ss * 18] - M);
      L = fmaf(wgt, pp[ss * 18 + 1], L); o0 = fmaf(wgt, pp[ss * 18 + 2 + d], o0); }
    const unsigned ob = cvtpk(o0 * frcp(L), 0.f) & 0xffffu;
    const unsigned other = __shfl_down(ob, 16);
    if (tid < 16) put(g4 + w * 16 + tid, tag, ob | (other << 16));
  }
  STAMP(8);
  float x1own0 = 0.f, x1own1 = 0.f;
  { unsigned ov[8]; sweep<8>(g4 + tid, 512, tag, ov);
    STAMP(9);
    float a00 = 0.f, a01 = 0.f, a10 = 0.f, a11 = 0.f;
#pragma unroll
    for (int i = 0; i < 8; ++i) {
      const unsigned wv = ((const unsigned*)(lds + L_WST))[((i >> 2) * 512 + tid) * 4 + (i & 3)];
      const float w0 = bflo(wv), w1 = bfhi(wv), o0 = bflo(ov[i]), o1 = bfhi(ov[i]);
      a00 = fmaf(o0, w0, a00); a01 = fmaf(o0, w1, a01); a10 = fmaf(o1, w0, a10); a11 = fmaf(o1, w1, a11);
    }
    a00 = wave_sum(a00); a01 = wave_sum(a01); a10 = wave_sum(a10); a11 = wave_sum(a11);
    if (lane == 0) *reinterpret_cast<f32x4*>(lds + L_RED + wave * 4) = f32x4{a00, a01, a10, a11};
    bar();
    if (tid < 2) {
      float a0 = 0.f, a1 = 0.f;
#pragma unroll
      for (int ww = 0; ww < NW; ++ww) { a0 += lds[L_RED + ww * 4 + tid]; a1 += lds[L_RED + ww * 4 + 2 + tid]; }
      const int n = col0 + tid;
      const unsigned ga = ((const unsigned*)(lds + L_MODS))[1024 + tid];
      x1own0 = rbf(bf2f(xs[n]) + rbf(bflo(ga) * rbf(a0)));
      x1own1 = rbf(bf2f(xs[512 + n]) + rbf(bfhi(ga) * rbf(a1)));
      put(g5 + n, tag, cvtpk(x1own0, x1own1));
    } }
  STAMP(10);
  { unsigned v[1]; sweep<1>(g5 + tid, 0, tag, v);
    STAMP(11);
    const float y0 = bflo(v[0]), y1 = bfhi(v[0]);
    lds[L_X + tid] = y0; lds[L_X + 512 + tid] = y1;
    bar();
    if (has_gu) {
      float m0, m1; ln_norm(lds, lane, y0, y1, m0, m1);
      const unsigned sh = ((const unsigned*)(lds + L_MODS))[1536 + tid], sc = ((const unsigned*)(lds + L_MODS))[2048 + tid];
      const float a0 = rbf(rbf(m0 * rbf(1.f + bflo(sc))) + bflo(sh)), a1 = rbf(rbf(m1 * rbf(1.f + bfhi(sc))) + bfhi(sh));
      const u32x4 wgv = *reinterpret_cast<const u32x4*>(lds + L_WST + (2 * 512 + tid) * 4), wuv = *reinterpret_cast<const u32x4*>(lds + L_WST + (3 * 512 + tid) * 4);
      float v2[32];
#pragma unroll
      for (int j = 0; j < 4; ++j) {
        v2[2 * j] = a0 * bflo(wgv[j]); v2[2 * j + 1] = a0 * bfhi(wgv[j]); v2[8 + 2 * j] = a0 * bflo(wuv[j]); v2[8 + 2 * j + 1] = a0 * bfhi(wuv[j]);
        v2[16 + 2 * j] = a1 * bflo(wgv[j]); v2[16 + 2 * j + 1] = a1 * bfhi(wgv[j]); v2[24 + 2 * j] = a1 * bflo(wuv[j]); v2[24 + 2 * j + 1] = a1 * bfhi(wuv[j]);
      }
#pragma unroll
      for (int h = 0; h < 16; ++h) { auto sw = __builtin_amdgcn_permlane32_swap(__float_as_uint(v2[h]), __float_as_uint(v2[h + 16]), false, false); v2[h] = __uint_as_float(sw[0]) + __uint_as_float(sw[1]); }
#pragma unroll
      for (int h = 0; h < 8; ++h) { auto sw = __builtin_amdgcn_permlane16_swap(__float_as_uint(v2[h]), __float_as_uint(v2[h + 8]), false, false); v2[h] = __uint_as_float(sw[0]) + __uint_as_float(sw[1]); }
#define TRS(HALF, CTRL, BIT) _Pragma("unroll") for (int h = 0; h < (HALF); ++h) { \
      const float lo_ = v2[h], hi_ = v2[h + (HALF)]; const float t1_ = lo_ + dpp<CTRL>(lo_), t2_ = hi_ + dpp<CTRL>(hi_); \
      v2[h] = (lane & (BIT)) ? t2_ : t1_; }
      TRS(4, XOR8, 8) TRS(2, XOR7, 4) TRS(1, XOR2, 2)
#undef TRS
      v2[0] += dpp<XOR1>(v2[0]);
      if ((lane & 1) == 0) lds[L_RED + wave * 32 + (lane >> 1)] = v2[0];
    }
    bar();
    if (has_gu && tid < 8) {
      float g0 = 0.f, u0 = 0.f, g1s = 0.f, u1 = 0.f;
#pragma unroll
      for (int ww = 0; ww < NW; ++ww) { g0 += lds[L_RED + ww * 32 + tid]; u0 += lds[L_RED + ww * 32 + 8 + tid]; g1s += lds[L_RED + ww * 32 + 16 + tid]; u1 += lds[L_RED + ww * 32 + 24 + tid]; }
      put(g6 + 8 * wgu + tid, tag, cvtpk(rbf(g0) * rbf(u0), rbf(g1s) * rbf(u1)));
    } }
  STAMP(12);
  { unsigned hv[4];
    for (unsigned n = 0;; ++n) {
      bool ok = true;
#pragma unroll
      for (int i = 0; i < 4; ++i) { const int k = tid + 512 * i; const u64 a = __hip_atomic_load(g6 + (k < F ? k : 0), __ATOMIC_RELAXED, __HIP_MEMORY_SCOPE_AGENT); hv[i] = k < F ? (unsigned)a : 0u; ok &= (unsigned)(a >> 32) == tag; }
      if (__all(ok) || n > SPIN_CAP) break;
      __builtin_amdgcn_s_sleep(1);
    }
    STAMP(13);
    float a00 = 0.f, a01 = 0.f, a10 = 0.f, a11 = 0.f;
    const u32x4 wdv = *reinterpret_cast<const u32x4*>(lds + L_WST + (4 * 512 + tid) * 4);
#pragma unroll
    for (int i = 0; i < 4; ++i) {
      const float w0 = bflo(wdv[i]), w1 = bfhi(wdv[i]), o0 = bflo(hv[i]), o1 = bfhi(hv[i]);
      a00 = fmaf(o0, w0, a00); a01 = fmaf(o0, w1, a01); a10 = fmaf(o1, w0, a10); a11 = fmaf(o1, w1, a11);
    }
    a00 = wave_sum(a00); a01 = wave_sum(a01); a10 = wave_sum(a10); a11 = wave_sum(a11);
    if (lane == 0) *reinterpret_cast<f32x4*>(lds + L_RED + wave * 4) = f32x4{a00, a01, a10, a11};
    bar();
    if (tid < 2) {
      float a0 = 0.f, a1 = 0.f;
#pragma unroll
      for (int ww = 0; ww < NW; ++ww) { a0 += lds[L_RED + ww * 4 + tid]; a1 += lds[L_RED + ww * 4 + 2 + tid]; }
      const int n = col0 + tid;
      const unsigned gb = ((const unsigned*)(lds + L_MODS))[1026 + tid];
      out[n] = __float2bfloat16(x1own0 + rbf(bflo(gb) * rbf(a0)));
      out[512 + n] = __float2bfloat16(x1own1 + rbf(bfhi(gb) * rbf(a1)));
    }
    if (w == 0 && tid == 0) __hip_atomic_store((unsigned*)(ws + WS_EPOCH), tag, __ATOMIC_RELAXED, __HIP_MEMORY_SCOPE_AGENT);
  }
  STAMP(14);
}

extern "C" void kernel_launch(void* const* d_in, const int* in_sizes, int n_in, void* d_out, int out_size, void* d_ws, size_t ws_size, hipStream_t stream) {
  (void)hipFuncSetAttribute((const void*)mega, hipFuncAttributeMaxDynamicSharedMemorySize, L_TOTAL * 4);
  mega<<<NWG, NT, L_TOTAL * 4, stream>>>((const bf16*)d_in[0], (const bf16*)d_in[1], (const bf16*)d_in[2], (const bf16*)d_in[3], (const bf16*)d_in[4],
                                        (const bf16*)d_in[6], (const bf16*)d_in[7], (const bf16*)d_in[8], (const bf16*)d_in[9], (const bf16*)d_in[10],
                                        (bf16*)d_out, (char*)d_ws);
}
```

```cpp
#include <hip/hip_runtime.h>
#include <hip/hip_bf16.h>
#include <cstdint>

using bf16 = __hip_bfloat16;
typedef unsigned long long u64;
constexpr int B = 2, DM = 512, HQ = 32, HKV = 8, G = 4, SK = 32768, DH = 128, F = 1792;
constexpr int NQ = HQ * DH;
constexpr int NWG = 256, NT = 512, NW = 8;
constexpr int NSPLIT = 16, PAGE = 16;
constexpr int PPS = SK / PAGE / NSPLIT;
constexpr unsigned SPIN_CAP = 1u << 22;

using u32x4 = __attribute__((ext_vector_type(4))) unsigned;
using f32x4 = __attribute__((ext_vector_type(4))) float;
using f32x2 = __attribute__((ext_vector_type(2))) float;
using bf16x2 = __attribute__((ext_vector_type(2))) __bf16;

constexpr size_t WS_G1 = 0;
constexpr size_t WS_G2 = 32768;
constexpr size_t WS_G4 = 65536;
constexpr size_t WS_G5 = 98304;
constexpr size_t WS_G6 = 131072;
constexpr size_t WS_EPOCH = 163840;
constexpr size_t WS_G3 = 1u << 20;
constexpr size_t WS_TS = 8u << 20;

__device__ __forceinline__ float bf2f(unsigned h) { return __builtin_bit_cast(float, h << 16); }
__device__ __forceinline__ float bflo(unsigned w) { return __builtin_bit_cast(float, w << 16); }
__device__ __forceinline__ float bfhi(unsigned w) { return __builtin_bit_cast(float, w & 0xffff0000u); }
__device__ __forceinline__ unsigned cvtpk(float lo, float hi) {
  return __builtin_bit_cast(unsigned, __builtin_convertvector(f32x2{lo, hi}, bf16x2));
}
__device__ __forceinline__ float rbf(float f) { return __builtin_bit_cast(float, cvtpk(f, f) << 16); }
__device__ __forceinline__ float frcp(float x) { return __builtin_amdgcn_rcpf(x); }
__device__ __forceinline__ float dot2(unsigned k, unsigned q, float acc) {
  return __builtin_amdgcn_fdot2_f32_bf16(__builtin_bit_cast(bf16x2, k), __builtin_bit_cast(bf16x2, q), acc, false);
}
template <int CTRL> __device__ __forceinline__ float dpp(float x) {
  return __builtin_bit_cast(float, __builtin_amdgcn_mov_dpp(__builtin_bit_cast(int, x), CTRL, 0xf, 0xf, true));
}
constexpr int XOR1 = 0xB1, XOR2 = 0x4E, XOR7 = 0x141, XOR8 = 0x128, ROR4 = 0x124;
__device__ __forceinline__ float sum32(float a, float b) {
  auto s = __builtin_amdgcn_permlane32_swap(__float_as_uint(a), __float_as_uint(b), false, false);
  return __uint_as_float(s[0]) + __uint_as_float(s[1]);
}
__device__ __forceinline__ float sum16(float a, float b) {
  auto s = __builtin_amdgcn_permlane16_swap(__float_as_uint(a), __float_as_uint(b), false, false);
  return __uint_as_float(s[0]) + __uint_as_float(s[1]);
}
__device__ __forceinline__ float xrow16_max(float x) {
  auto s = __builtin_amdgcn_permlane16_swap(__float_as_uint(x), __float_as_uint(x), false, false);
  x = fmaxf(__uint_as_float(s[0]), __uint_as_float(s[1]));
  auto t = __builtin_amdgcn_permlane32_swap(__float_as_uint(x), __float_as_uint(x), false, false);
  return fmaxf(__uint_as_float(t[0]), __uint_as_float(t[1]));
}
__device__ __forceinline__ float xrow16_sum(float x) { x = sum16(x, x); return sum32(x, x); }
__device__ __forceinline__ float row_sum(float x) { x += dpp<XOR1>(x); x += dpp<XOR2>(x); x += dpp<XOR7>(x); x += dpp<XOR8>(x); return x; }
__device__ __forceinline__ float wave_sum(float x) { return xrow16_sum(row_sum(x)); }
__device__ __forceinline__ void bar() { asm volatile("s_waitcnt lgkmcnt(0)\n\ts_barrier" ::: "memory"); }
__device__ __forceinline__ void put(u64* p, unsigned tag, unsigned v) {
  __hip_atomic_store(p, ((u64)tag << 32) | v, __ATOMIC_RELAXED, __HIP_MEMORY_SCOPE_AGENT);
}
template <int N>
__device__ __forceinline__ void sweep(const u64* p, int stride, unsigned tag, unsigned (&v)[N]) {
  for (unsigned n = 0;; ++n) {
    bool ok = true;
#pragma unroll
    for (int k = 0; k < N; ++k) { const u64 a = __hip_atomic_load(p + k * stride, __ATOMIC_RELAXED, __HIP_MEMORY_SCOPE_AGENT); v[k] = (unsigned)a; ok &= (unsigned)(a >> 32) == tag; }
    if (__all(ok) || n > SPIN_CAP) break;
    __builtin_amdgcn_s_sleep(1);
  }
}
__device__ __forceinline__ void glds16(const void* gsrc, unsigned lds_dst) {
  unsigned keep;
  asm volatile("s_mov_b32 %0, m0\n\ts_mov_b32 m0, %2\n\ts_nop 0\n\tglobal_load_lds_dwordx4 %1, off nt\n\ts_mov_b32 m0, %0" : "=&s"(keep) : "v"(gsrc), "s"(lds_dst) : "memory");
}

#define STAMP(i) do { } while (0)

constexpr int L_A = 0;
constexpr int L_X = 1024;
constexpr int L_RED = 2048;
constexpr int L_Q = 2304;
constexpr int L_P = 2560;
constexpr int L_MODS = 3072;
constexpr int L_PART = L_MODS + 3072;
constexpr int L_WST = L_PART + 640;
constexpr int L_MRG = L_WST + 20 * 512;
constexpr int L_KV = L_MRG + 4224;
constexpr int L_TOTAL = L_KV + 8 * 2048;

__device__ __forceinline__ void ln_norm(const float* lds, int lane, float x0, float x1, float& n0, float& n1) {
#pragma unroll
  for (int bb = 0; bb < 2; ++bb) {
    float v[8], s = 0.f;
#pragma unroll
    for (int j = 0; j < 8; ++j) { v[j] = lds[L_X + bb * 512 + lane + 64 * j]; s += v[j]; }
    const float mu = wave_sum(s) * (1.f / DM);
    float q = 0.f;
#pragma unroll
    for (int j = 0; j < 8; ++j) { const float d = v[j] - mu; q += d * d; }
    const float var = rbf(wave_sum(q) * (1.f / DM));
    const float sd = rbf(__builtin_amdgcn_sqrtf(rbf(var + 1e-5f)));
    const float t = rbf(rbf((bb ? x1 : x0) - rbf(mu)) * frcp(sd));
    if (bb) n1 = t; else n0 = t;
  }
}
__device__ __forceinline__ void gemv16_part(float* lds, int tid, const u32x4* wv) {
  const int lane = tid & 63, wave = tid >> 6;
  float acc[16];
#pragma unroll
  for (int j = 0; j < 16; ++j) acc[j] = 0.f;
#pragma unroll
  for (int i = 0; i < 2; ++i) {
    const int row = (tid >> 1) + 256 * i;
    const float a0 = lds[L_A + row], a1 = lds[L_A + 512 + row];
#pragma unroll
    for (int j = 0; j < 4; ++j) {
      const float lo = bflo(wv[i][j]), hi = bfhi(wv[i][j]);
      acc[2 * j] = fmaf(a0, lo, acc[2 * j]); acc[2 * j + 1] = fmaf(a0, hi, acc[2 * j + 1]);
      acc[8 + 2 * j] = fmaf(a1, lo, acc[8 + 2 * j]); acc[8 + 2 * j + 1] = fmaf(a1, hi, acc[8 + 2 * j + 1]);
    }
  }
#pragma unroll
  for (int h = 0; h < 8; ++h) acc[h] = sum32(acc[h], acc[h + 8]);
#pragma unroll
  for (int h = 0; h < 4; ++h) acc[h] = sum16(acc[h], acc[h + 4]);
#pragma unroll
  for (int h = 0; h < 4; ++h) { float v = acc[h]; v += dpp<XOR2>(v); v += dpp<ROR4>(v); v += dpp<XOR8>(v); acc[h] = v; }
  if ((lane & 14) == 0) *reinterpret_cast<f32x4*>(lds + L_RED + wave * 32 + (lane & 1) * 16 + (lane >> 4) * 4) = f32x4{acc[0], acc[1], acc[2], acc[3]};
}
__device__ __forceinline__ unsigned gemv16_fin(const float* lds, int t) {
  const int half = t >> 3, j = t & 7;
  float s0 = 0.f, s1 = 0.f;
#pragma unroll
  for (int ww = 0; ww < 8; ++ww) { s0 += lds[L_RED + ww * 32 + half * 16 + j]; s1 += lds[L_RED + ww * 32 + half * 16 + 8 + j]; }
  return cvtpk(s0, s1);
}

__global__ void __launch_bounds__(NT, 2) mega(const bf16* __restrict__ x, const bf16* __restrict__ kc, const bf16* __restrict__ vc,
                                              const bf16* __restrict__ wq, const bf16* __restrict__ wo, const bf16* __restrict__ wg,
                                              const bf16* __restrict__ wu, const bf16* __restrict__ wd, const bf16* __restrict__ c,
                                              const bf16* __restrict__ w_mod, bf16* __restrict__ out, char* __restrict__ ws) {
  extern __shared__ __attribute__((aligned(16))) float lds[];
  const int tid = threadIdx.x, lane = tid & 63, wave = __builtin_amdgcn_readfirstlane(tid >> 6);
  const int w = blockIdx.x;
  const unsigned tag = __hip_atomic_load((const unsigned*)(ws + WS_EPOCH), __ATOMIC_RELAXED, __HIP_MEMORY_SCOPE_AGENT) + 1u;
  u64* g1 = (u64*)(ws + WS_G1); u64* g2 = (u64*)(ws + WS_G2); u64* g3 = (u64*)(ws + WS_G3);
  u64* g4 = (u64*)(ws + WS_G4); u64* g5 = (u64*)(ws + WS_G5); u64* g6 = (u64*)(ws + WS_G6);
  u64* ts = (u64*)(ws + WS_TS); (void)ts;
  const int pair = w >> 4, b = pair >> 3, kvh = pair & 7, s = w & 15;
  const int r = lane >> 4, cc = lane & 15;
  const int wx = w & 7, wi = w >> 3;
  const bool has_mod = wi < 24;
  const int mslice = wi < 8 ? wx * 8 + wi : (wi < 24 ? 64 + wx * 16 + (wi - 8) : 0);
  const int qslice = wx * 32 + wi;
  const int cg = wx * 8 + (wi & 7), kq = wi >> 3;
  const bool has_gu = wi < 28;
  const int wgu = has_gu ? wx * 28 + wi : 0;
  const int col0 = 2 * (wx * 32 + wi);
  STAMP(0);
  const unsigned short* cs = (const unsigned short*)c; const unsigned short* xs = (const unsigned short*)x;
  const float c0 = bf2f(cs[tid]), c1 = bf2f(cs[512 + tid]);
  const float x0 = bf2f(xs[tid]), x1 = bf2f(xs[512 + tid]);
  u32x4 wm[2], wqv[2];
#pragma unroll
  for (int i = 0; i < 2; ++i) {
    const int row = (tid >> 1) + 256 * i;
    wm[i] = *reinterpret_cast<const u32x4*>(w_mod + (size_t)row * (6 * DM) + mslice * 16 + (tid & 1) * 8);
    wqv[i] = *reinterpret_cast<const u32x4*>(wq + (size_t)row * NQ + qslice * 16 + (tid & 1) * 8);
  }
  lds[L_A + tid] = rbf(c0 * frcp(1.f + __expf(-c0))); lds[L_A + 512 + tid] = rbf(c1 * frcp(1.f + __expf(-c1)));
  lds[L_X + tid] = x0; lds[L_X + 512 + tid] = x1;
  bar();
  gemv16_part(lds, tid, wm);
  float n0, n1;
  ln_norm(lds, lane, x0, x1, n0, n1);
  bar();
  if (has_mod && tid < 16) put(g1 + mslice * 16 + tid, tag, gemv16_fin(lds, tid));
  STAMP(1);
  const size_t base = ((size_t)(b * HKV + kvh) * SK + (size_t)s * PPS * PAGE) * DH + (size_t)r * DH + 8 * cc;
  const bf16* kb = kc + base; const bf16* vb = vc + base;
  u32x4 KA[4], VA[4], KB[4], VB[4];
#define ISSUE(KX, VX, pg_) do { const size_t off_ = (size_t)(pg_) * PAGE * DH; \
    _Pragma("unroll") for (int t_ = 0; t_ < 4; ++t_) { KX[t_] = __builtin_nontemporal_load(reinterpret_cast<const u32x4*>(kb + off_ + t_ * 4 * DH)); VX[t_] = __builtin_nontemporal_load(reinterpret_cast<const u32x4*>(vb + off_ + t_ * 4 * DH)); } } while (0)
#define ISSUE_SEL(KX, VX, pg_, ok_) do { const size_t off_ = (size_t)(pg_) * PAGE * DH; \
    const bf16* kp_ = (ok_) ? kb + off_ : kc; const bf16* vp_ = (ok_) ? vb + off_ : vc; \
    _Pragma("unroll") for (int t_ = 0; t_ < 4; ++t_) { KX[t_] = __builtin_nontemporal_load(reinterpret_cast<const u32x4*>(kp_ + t_ * 4 * DH)); VX[t_] = __builtin_nontemporal_load(reinterpret_cast<const u32x4*>(vp_ + t_ * 4 * DH)); } } while (0)
  const unsigned lkv = (unsigned)(size_t)((__attribute__((address_space(3))) float*)(lds + L_KV));
#define DMA_PAGE(slot_) do { const size_t off_ = (size_t)(120 + (slot_)) * PAGE * DH; \
    _Pragma("unroll") for (int t_ = 0; t_ < 4; ++t_) { glds16(kb + off_ + t_ * 4 * DH, lkv + (slot_) * 8192 + t_ * 1024); glds16(vb + off_ + t_ * 4 * DH, lkv + (slot_) * 8192 + 4096 + t_ * 1024); } } while (0)
  const int pfirst = wave == 0 ? 106 : (wave <= 5 ? (wave - 1) * 16 : 64 + (wave - 5) * 14), nu = (wave >= 1 && wave <= 4) ? 8 : 7;
  constexpr int pstep = 1;
  if (wave != 0) {
    ISSUE(KA, VA, pfirst);
    DMA_PAGE(wave);
    if (wave == 1) DMA_PAGE(0);
  } else {
    unsigned v[16];
    sweep<16>(g1 + lane, 64, tag, v);
#pragma unroll
    for (int j = 0; j < 16; ++j) ((unsigned*)(lds + L_MODS))[lane + 64 * j] = v[j];
  }
  bar();
  STAMP(2);
  { const unsigned sh = ((const unsigned*)(lds + L_MODS))[tid], sc = ((const unsigned*)(lds + L_MODS))[512 + tid];
    lds[L_A + tid] = rbf(rbf(n0 * rbf(1.f + bflo(sc))) + bflo(sh));
    lds[L_A + 512 + tid] = rbf(rbf(n1 * rbf(1.f + bfhi(sc))) + bfhi(sh)); }
  bar();
  gemv16_part(lds, tid, wqv);
  bar();
  if (wave != 0) {
    ISSUE(KB, VB, pfirst + pstep);
    asm volatile("s_waitcnt vmcnt(8)" ::: "memory");
  } else {
    if (tid < 16) put(g2 + qslice * 16 + tid, tag, gemv16_fin(lds, tid));
    STAMP(3);
    unsigned qq[8];
    sweep<8>(g2 + kvh * 512 + lane, 64, tag, qq);
#pragma unroll
    for (int j = 0; j < 8; ++j) ((unsigned short*)(lds + L_Q))[lane + 64 * j] = (unsigned short)(b ? (qq[j] >> 16) : (qq[j] & 0xffffu));
    ISSUE(KA, VA, pfirst);
    ISSUE(KB, VB, pfirst + pstep);
  }
  bar();
  STAMP(4);
  u32x4 qpk[4];
#pragma unroll
  for (int g = 0; g < 4; ++g) qpk[g] = *reinterpret_cast<const u32x4*>((const unsigned short*)(lds + L_Q) + g * DH + 8 * cc);
  constexpr float C2 = 0.088388347648318440f * 1.4426950408889634f;
  float m = -INFINITY, l = 0.f, acc[32];
#pragma unroll
  for (int k = 0; k < 32; ++k) acc[k] = 0.f;
  float* prow = lds + L_P + (wave * 4 + r) * 16;
#define TR_STEP(HALF, CTRL, BIT) _Pragma("unroll") for (int h = 0; h < (HALF); ++h) { \
      const float lo_ = xx[h], hi_ = xx[h + (HALF)]; const float t1_ = lo_ + dpp<CTRL>(lo_), t2_ = hi_ + dpp<CTRL>(hi_); \
      xx[h] = (cc & (BIT)) ? t2_ : t1_; }
#define PROCESS(KX, VX) do { \
    float xx[16]; \
    _Pragma("unroll") for (int t = 0; t < 4; ++t) _Pragma("unroll") for (int g = 0; g < 4; ++g) { float d = 0.f; \
        _Pragma("unroll") for (int k = 0; k < 4; ++k) d = dot2(KX[t][k], qpk[g][k], d); xx[t * 4 + g] = d; } \
    TR_STEP(8, XOR8, 8) TR_STEP(4, XOR7, 4) TR_STEP(2, XOR2, 2) TR_STEP(1, XOR1, 1) \
    const float sc = xx[0] * C2; \
    float pm = sc; pm = fmaxf(pm, dpp<ROR4>(pm)); pm = fmaxf(pm, dpp<XOR8>(pm)); pm = xrow16_max(pm); \
    const float mn = fmaxf(m, pm); \
    const float alpha = __builtin_amdgcn_exp2f(m - mn), p = __builtin_amdgcn_exp2f(sc - mn); \
    l = fmaf(l, alpha, p); m = mn; \
    _Pragma("unroll") for (int q4 = 0; q4 < 4; ++q4) { \
      const float al = q4 == 0 ? dpp<0x00>(alpha) : q4 == 1 ? dpp<0x55>(alpha) : q4 == 2 ? dpp<0xAA>(alpha) : dpp<0xFF>(alpha); \
      _Pragma("unroll") for (int k = 0; k < 8; ++k) acc[q4 * 8 + k] *= al; } \
    prow[cc] = p; \
    asm volatile("s_waitcnt lgkmcnt(0)" ::: "memory"); \
    f32x4 pr[4]; \
    _Pragma("unroll") for (int u_ = 0; u_ < 4; ++u_) pr[u_] = *reinterpret_cast<const f32x4*>(prow + 4 * u_); \
    _Pragma("unroll") for (int t = 0; t < 4; ++t) { \
      float vf[8]; \
      _Pragma("unroll") for (int k = 0; k < 4; ++k) { vf[2 * k] = bflo(VX[t][k]); vf[2 * k + 1] = bfhi(VX[t][k]); } \
      _Pragma("unroll") for (int g = 0; g < 4; ++g) { const float pg_ = pr[t][g]; \
        _Pragma("unroll") for (int k = 0; k < 8; ++k) acc[g * 8 + k] = fmaf(pg_, vf[k], acc[g * 8 + k]); } } \
  } while (0)
  for (int u = 0; u < nu; ++u) {
    PROCESS(KA, VA);
    if (u == 0) {
      const float* kl = lds + L_KV + wave * 2048 + lane * 4;
#pragma unroll
      for (int t = 0; t < 4; ++t) { KA[t] = *reinterpret_cast<const u32x4*>(kl + t * 256); VA[t] = *reinterpret_cast<const u32x4*>(kl + 1024 + t * 256); }
      PROCESS(KA, VA);
    }
    ISSUE_SEL(KA, VA, pfirst + (2 * u + 2) * pstep, u + 1 < nu);
    if (u == 1) {
      const u32x4 a_ = *reinterpret_cast<const u32x4*>(wo + (size_t)(kq * 1024 + tid) * DM + cg * 8), b_ = *reinterpret_cast<const u32x4*>(wo + (size_t)(kq * 1024 + 512 + tid) * DM + cg * 8);
      *reinterpret_cast<u32x4*>(lds + L_WST + (0 * 512 + tid) * 4) = a_;
      *reinterpret_cast<u32x4*>(lds + L_WST + (1 * 512 + tid) * 4) = b_;
    } else if (u == 2) {
      const u32x4 a_ = *reinterpret_cast<const u32x4*>(wg + (size_t)tid * F + 8 * wgu), b_ = *reinterpret_cast<const u32x4*>(wu + (size_t)tid * F + 8 * wgu);
      *reinterpret_cast<u32x4*>(lds + L_WST + (2 * 512 + tid) * 4) = a_;
      *reinterpret_cast<u32x4*>(lds + L_WST + (3 * 512 + tid) * 4) = b_;
    } else if (u == 3) {
      unsigned st[4];
#pragma unroll
      for (int i = 0; i < 4; ++i) { const int k_ = tid + 512 * i; st[i] = *reinterpret_cast<const unsigned*>(wd + (size_t)(k_ < F ? k_ : 0) * DM + col0); }
      *reinterpret_cast<u32x4*>(lds + L_WST + (4 * 512 + tid) * 4) = u32x4{st[0], st[1], st[2], st[3]};
      if (wave == 0) {
#pragma unroll
        for (int hf = 0; hf < 2; ++hf) { unsigned v[16]; sweep<16>(g1 + 1024 + hf * 1024 + lane, 64, tag, v);
#pragma unroll
          for (int j = 0; j < 16; ++j) ((unsigned*)(lds + L_MODS))[1024 + hf * 1024 + lane + 64 * j] = v[j]; }
      }
    }
    PROCESS(KB, VB);
    ISSUE_SEL(KB, VB, pfirst + (2 * u + 3) * pstep, u + 1 < nu);
  }
#undef PROCESS
#undef TR_STEP
  STAMP(5);
#pragma unroll
  for (int k = 0; k < 32; ++k) acc[k] = xrow16_sum(acc[k]);
  { float t = l; t += dpp<ROR4>(t); t += dpp<XOR8>(t); l = xrow16_sum(t); }
  float* mrg = lds + L_MRG;
  if (r == 0) {
#pragma unroll
    for (int g = 0; g < 4; ++g) {
      float* rec = mrg + (wave * 4 + g) * 132;
#pragma unroll
      for (int k = 0; k < 8; k += 4) *reinterpret_cast<f32x4*>(rec + 4 + 8 * cc + k) = f32x4{acc[g * 8 + k], acc[g * 8 + k + 1], acc[g * 8 + k + 2], acc[g * 8 + k + 3]};
      if (cc == g) { rec[0] = m; rec[1] = l; } } }
  bar();
  { const int g = tid >> 7, d = tid & 127;
    float M = -INFINITY;
#pragma unroll
    for (int ww = 0; ww < NW; ++ww) M = fmaxf(M, mrg[(ww * 4 + g) * 132]);
    float L = 0.f, o0 = 0.f;
#pragma unroll
    for (int ww = 0; ww < NW; ++ww) { const float* rec = mrg + (ww * 4 + g) * 132;
      const float wgt = __builtin_amdgcn_exp2f(rec[0] - M);
      L = fmaf(wgt, rec[1], L); o0 = fmaf(wgt, rec[4 + d], o0); }
    u64* dst = g3 + (size_t)((kvh * 4 + g) * 8 + (d >> 4)) * 576 + (b * 16 + s) * 18;
    put(dst + 2 + (d & 15), tag, __float_as_uint(o0));
    if ((d & 15) == 0) { put(dst, tag, __float_as_uint(M)); put(dst + 1, tag, __float_as_uint(L)); }
  }
  STAMP(6);
  { unsigned v[1]; sweep<1>(g3 + (size_t)w * 576 + tid, 0, tag, v);
    lds[L_PART + tid] = __uint_as_float(v[0]);
    if (wave == 0) { sweep<1>(g3 + (size_t)w * 576 + 512 + tid, 0, tag, v); lds[L_PART + 512 + tid] = __uint_as_float(v[0]); } }
  bar();
  STAMP(7);
  if (tid < 32) {
    const int bb = tid >> 4, d = tid & 15;
    const float* pp = lds + L_PART + bb * 16 * 18;
    float M = -INFINITY;
#pragma unroll
    for (int ss = 0; ss < 16; ++ss) M = fmaxf(M, pp[ss * 18]);
    float L = 0.f, o0 = 0.f;
#pragma unroll
    for (int ss = 0; ss < 16; ++ss) { const float wgt = __builtin_amdgcn_exp2f(pp[ss * 18] - M);
      L = fmaf(wgt, pp[ss * 18 + 1], L); o0 = fmaf(wgt, pp[ss * 18 + 2 + d], o0); }
    const unsigned ob = cvtpk(o0 * frcp(L), 0.f) & 0xffffu;
    const unsigned other = __shfl_down(ob, 16);
    if (tid < 16) put(g4 + w * 16 + tid, tag, ob | (other << 16));
  }
  STAMP(8);
  { unsigned ov[2]; sweep<2>(g4 + kq * 1024 + tid, 512, tag, ov);
    STAMP(9);
    float v[16];
#pragma unroll
    for (int j = 0; j < 16; ++j) v[j] = 0.f;
#pragma unroll
    for (int i = 0; i < 2; ++i) {
      const u32x4 wv = *reinterpret_cast<const u32x4*>(lds + L_WST + (i * 512 + tid) * 4);
      const float o0 = bflo(ov[i]), o1 = bfhi(ov[i]);
#pragma unroll
      for (int j = 0; j < 4; ++j) { const float lo = bflo(wv[j]), hi = bfhi(wv[j]);
        v[2 * j] = fmaf(o0, lo, v[2 * j]); v[2 * j + 1] = fmaf(o0, hi, v[2 * j + 1]); v[8 + 2 * j] = fmaf(o1, lo, v[8 + 2 * j]); v[8 + 2 * j + 1] = fmaf(o1, hi, v[8 + 2 * j + 1]); }
    }
#pragma unroll
    for (int h = 0; h < 8; ++h) v[h] = sum32(v[h], v[h + 8]);
#pragma unroll
    for (int h = 0; h < 4; ++h) v[h] = sum16(v[h], v[h + 4]);
#pragma unroll
    for (int h = 0; h < 4; ++h) v[h] = row_sum(v[h]);
    if (cc == 0) *reinterpret_cast<f32x4*>(lds + L_RED + wave * 16 + r * 4) = f32x4{v[0], v[1], v[2], v[3]};
    bar();
    if (tid < 16) {
      float a = 0.f;
#pragma unroll
      for (int ww = 0; ww < NW; ++ww) a += lds[L_RED + ww * 16 + tid];
      put(g5 + (kq * 2 + (tid >> 3)) * 512 + cg * 8 + (tid & 7), tag, __float_as_uint(a));
    } }
  STAMP(10);
  { unsigned pv[8]; sweep<8>(g5 + tid, 512, tag, pv);
    STAMP(11);
    const float a0 = (__uint_as_float(pv[0]) + __uint_as_float(pv[2])) + (__uint_as_float(pv[4]) + __uint_as_float(pv[6]));
    const float a1 = (__uint_as_float(pv[1]) + __uint_as_float(pv[3])) + (__uint_as_float(pv[5]) + __uint_as_float(pv[7]));
    const unsigned ga = ((const unsigned*)(lds + L_MODS))[1024 + tid];
    const float y0 = rbf(x0 + rbf(bflo(ga) * rbf(a0))), y1 = rbf(x1 + rbf(bfhi(ga) * rbf(a1)));
    lds[L_X + tid] = y0; lds[L_X + 512 + tid] = y1;
    bar();
    if (has_gu) {
      float m0, m1; ln_norm(lds, lane, y0, y1, m0, m1);
      const unsigned sh = ((const unsigned*)(lds + L_MODS))[1536 + tid], sc = ((const unsigned*)(lds + L_MODS))[2048 + tid];
      const float h0 = rbf(rbf(m0 * rbf(1.f + bflo(sc))) + bflo(sh)), h1 = rbf(rbf(m1 * rbf(1.f + bfhi(sc))) + bfhi(sh));
      const u32x4 wgv = *reinterpret_cast<const u32x4*>(lds + L_WST + (2 * 512 + tid) * 4), wuv = *reinterpret_cast<const u32x4*>(lds + L_WST + (3 * 512 + tid) * 4);
      float v2[32];
#pragma unroll
      for (int j = 0; j < 4; ++j) {
        v2[2 * j] = h0 * bflo(wgv[j]); v2[2 * j + 1] = h0 * bfhi(wgv[j]); v2[8 + 2 * j] = h0 * bflo(wuv[j]); v2[8 + 2 * j + 1] = h0 * bfhi(wuv[j]);
        v2[16 + 2 * j] = h1 * bflo(wgv[j]); v2[16 + 2 * j + 1] = h1 * bfhi(wgv[j]); v2[24 + 2 * j] = h1 * bflo(wuv[j]); v2[24 + 2 * j + 1] = h1 * bfhi(wuv[j]);
      }
#pragma unroll
      for (int h = 0; h < 16; ++h) v2[h] = sum32(v2[h], v2[h + 16]);
#pragma unroll
      for (int h = 0; h < 8; ++h) v2[h] = sum16(v2[h], v2[h + 8]);
#define TRS(HALF, CTRL, BIT) _Pragma("unroll") for (int h = 0; h < (HALF); ++h) { \
      const float lo_ = v2[h], hi_ = v2[h + (HALF)]; const float t1_ = lo_ + dpp<CTRL>(lo_), t2_ = hi_ + dpp<CTRL>(hi_); \
      v2[h] = (lane & (BIT)) ? t2_ : t1_; }
      TRS(4, XOR8, 8) TRS(2, XOR7, 4) TRS(1, XOR2, 2)
#undef TRS
      v2[0] += dpp<XOR1>(v2[0]);
      if ((lane & 1) == 0) lds[L_RED + wave * 32 + (lane >> 1)] = v2[0];
    }
    bar();
    if (has_gu && tid < 8) {
      float g0 = 0.f, u0 = 0.f, g1s = 0.f, u1 = 0.f;
#pragma unroll
      for (int ww = 0; ww < NW; ++ww) { g0 += lds[L_RED + ww * 32 + tid]; u0 += lds[L_RED + ww * 32 + 8 + tid]; g1s += lds[L_RED + ww * 32 + 16 + tid]; u1 += lds[L_RED + ww * 32 + 24 + tid]; }
      put(g6 + 8 * wgu + tid, tag, cvtpk(rbf(g0) * rbf(u0), rbf(g1s) * rbf(u1)));
    } }
  STAMP(12);
  { unsigned hv[4];
    for (unsigned n = 0;; ++n) {
      bool ok = true;
#pragma unroll
      for (int i = 0; i < 4; ++i) { const int k = tid + 512 * i; const u64 a = __hip_atomic_load(g6 + (k < F ? k : 0), __ATOMIC_RELAXED, __HIP_MEMORY_SCOPE_AGENT); hv[i] = k < F ? (unsigned)a : 0u; ok &= (unsigned)(a >> 32) == tag; }
      if (__all(ok) || n > SPIN_CAP) break;
      __builtin_amdgcn_s_sleep(1);
    }
    STAMP(13);
    float a00 = 0.f, a01 = 0.f, a10 = 0.f, a11 = 0.f;
    const u32x4 wdv = *reinterpret_cast<const u32x4*>(lds + L_WST + (4 * 512 + tid) * 4);
#pragma unroll
    for (int i = 0; i < 4; ++i) {
      const float w0 = bflo(wdv[i]), w1 = bfhi(wdv[i]), o0 = bflo(hv[i]), o1 = bfhi(hv[i]);
      a00 = fmaf(o0, w0, a00); a01 = fmaf(o0, w1, a01); a10 = fmaf(o1, w0, a10); a11 = fmaf(o1, w1, a11);
    }
    a00 = wave_sum(a00); a01 = wave_sum(a01); a10 = wave_sum(a10); a11 = wave_sum(a11);
    if (lane == 0) *reinterpret_cast<f32x4*>(lds + L_RED + wave * 4) = f32x4{a00, a01, a10, a11};
    bar();
    if (tid < 2) {
      float a0 = 0.f, a1 = 0.f;
#pragma unroll
      for (int ww = 0; ww < NW; ++ww) { a0 += lds[L_RED + ww * 4 + tid]; a1 += lds[L_RED + ww * 4 + 2 + tid]; }
      const int n = col0 + tid;
      const unsigned gb = ((const unsigned*)(lds + L_MODS))[2560 + n];
      out[n] = __float2bfloat16(lds[L_X + n] + rbf(bflo(gb) * rbf(a0)));
      out[512 + n] = __float2bfloat16(lds[L_X + 512 + n] + rbf(bfhi(gb) * rbf(a1)));
    }
    if (w == 0 && tid == 0) __hip_atomic_store((unsigned*)(ws + WS_EPOCH), tag, __ATOMIC_RELAXED, __HIP_MEMORY_SCOPE_AGENT);
  }
  STAMP(14);
}

extern "C" void kernel_launch(void* const* d_in, const int* in_sizes, int n_in, void* d_out, int out_size, void* d_ws, size_t ws_size, hipStream_t stream) {
  (void)hipFuncSetAttribute((const void*)mega, hipFuncAttributeMaxDynamicSharedMemorySize, L_TOTAL * 4);
  mega<<<NWG, NT, L_TOTAL * 4, stream>>>((const bf16*)d_in[0], (const bf16*)d_in[1], (const bf16*)d_in[2], (const bf16*)d_in[3], (const bf16*)d_in[4],
                                        (const bf16*)d_in[6], (const bf16*)d_in[7], (const bf16*)d_in[8], (const bf16*)d_in[9], (const bf16*)d_in[10],
                                        (bf16*)d_out, (char*)d_ws);
}
```

```cpp
#include <hip/hip_runtime.h>
#include <hip/hip_bf16.h>
#include <cstdint>

using bf16 = __hip_bfloat16;
typedef unsigned long long u64;
constexpr int B = 2, DM = 512, HQ = 32, HKV = 8, G = 4, SK = 32768, DH = 128, F = 1792;
constexpr int NQ = HQ * DH;
constexpr int NWG = 256, NT = 512, NW = 8;
constexpr int NSPLIT = 16, PAGE = 16;
constexpr int PPS = SK / PAGE / NSPLIT;
constexpr unsigned SPIN_CAP = 1u << 22;

using u32x4 = __attribute__((ext_vector_type(4))) unsigned;
using f32x4 = __attribute__((ext_vector_type(4))) float;
using f32x2 = __attribute__((ext_vector_type(2))) float;
using bf16x2 = __attribute__((ext_vector_type(2))) __bf16;

constexpr size_t WS_G1 = 0;
constexpr size_t WS_G2 = 32768;
constexpr size_t WS_G4 = 65536;
constexpr size_t WS_G5 = 98304;
constexpr size_t WS_G6 = 131072;
constexpr size_t WS_EPOCH = 163840;
constexpr size_t WS_G3 = 1u << 20;
constexpr size_t WS_TS = 8u << 20;

__device__ __forceinline__ float bf2f(unsigned h) { return __builtin_bit_cast(float, h << 16); }
__device__ __forceinline__ float bflo(unsigned w) { return __builtin_bit_cast(float, w << 16); }
__device__ __forceinline__ float bfhi(unsigned w) { return __builtin_bit_cast(float, w & 0xffff0000u); }
__device__ __forceinline__ unsigned cvtpk(float lo, float hi) {
  return __builtin_bit_cast(unsigned, __builtin_convertvector(f32x2{lo, hi}, bf16x2));
}
__device__ __forceinline__ float rbf(float f) { return __builtin_bit_cast(float, cvtpk(f, f) << 16); }
__device__ __forceinline__ float frcp(float x) { return __builtin_amdgcn_rcpf(x); }
__device__ __forceinline__ float dot2(unsigned k, unsigned q, float acc) {
  return __builtin_amdgcn_fdot2_f32_bf16(__builtin_bit_cast(bf16x2, k), __builtin_bit_cast(bf16x2, q), acc, false);
}
template <int CTRL> __device__ __forceinline__ float dpp(float x) {
  return __builtin_bit_cast(float, __builtin_amdgcn_mov_dpp(__builtin_bit_cast(int, x), CTRL, 0xf, 0xf, true));
}
constexpr int XOR1 = 0xB1, XOR2 = 0x4E, XOR7 = 0x141, XOR8 = 0x128, ROR4 = 0x124;
__device__ __forceinline__ float sum32(float a, float b) {
  auto s = __builtin_amdgcn_permlane32_swap(__float_as_uint(a), __float_as_uint(b), false, false);
  return __uint_as_float(s[0]) + __uint_as_float(s[1]);
}
__device__ __forceinline__ float sum16(float a, float b) {
  auto s = __builtin_amdgcn_permlane16_swap(__float_as_uint(a), __float_as_uint(b), false, false);
  return __uint_as_float(s[0]) + __uint_as_float(s[1]);
}
__device__ __forceinline__ float xrow16_max(float x) {
  auto s = __builtin_amdgcn_permlane16_swap(__float_as_uint(x), __float_as_uint(x), false, false);
  x = fmaxf(__uint_as_float(s[0]), __uint_as_float(s[1]));
  auto t = __builtin_amdgcn_permlane32_swap(__float_as_uint(x), __float_as_uint(x), false, false);
  return fmaxf(__uint_as_float(t[0]), __uint_as_float(t[1]));
}
__device__ __forceinline__ float xrow16_sum(float x) { x = sum16(x, x); return sum32(x, x); }
__device__ __forceinline__ float row_sum(float x) { x += dpp<XOR1>(x); x += dpp<XOR2>(x); x += dpp<XOR7>(x); x += dpp<XOR8>(x); return x; }
__device__ __forceinline__ float wave_sum(float x) { return xrow16_sum(row_sum(x)); }
__device__ __forceinline__ void bar() { asm volatile("s_waitcnt lgkmcnt(0)\n\ts_barrier" ::: "memory"); }
__device__ __forceinline__ void put(u64* p, unsigned tag, unsigned v) {
  __hip_atomic_store(p, ((u64)tag << 32) | v, __ATOMIC_RELAXED, __HIP_MEMORY_SCOPE_AGENT);
}
template <int N>
__device__ __forceinline__ void sweep(const u64* p, int stride, unsigned tag, unsigned (&v)[N]) {
  for (unsigned n = 0;; ++n) {
    bool ok = true;
#pragma unroll
    for (int k = 0; k < N; ++k) { const u64 a = __hip_atomic_load(p + k * stride, __ATOMIC_RELAXED, __HIP_MEMORY_SCOPE_AGENT); v[k] = (unsigned)a; ok &= (unsigned)(a >> 32) == tag; }
    if (__all(ok) || n > SPIN_CAP) break;
    __builtin_amdgcn_s_sleep(1);
  }
}
__device__ __forceinline__ void glds16(const void* gsrc, unsigned lds_dst) {
  unsigned keep;
  asm volatile("s_mov_b32 %0, m0\n\ts_mov_b32 m0, %2\n\ts_nop 0\n\tglobal_load_lds_dwordx4 %1, off nt\n\ts_mov_b32 m0, %0" : "=&s"(keep) : "v"(gsrc), "s"(lds_dst) : "memory");
}

#define STAMP(i) do { } while (0)

constexpr int L_A = 0;
constexpr int L_X = 1024;
constexpr int L_RED = 2048;
constexpr int L_Q = 2304;
constexpr int L_P = 2560;
constexpr int L_MODS = 3072;
constexpr int L_PART = L_MODS + 3072;
constexpr int L_WST = L_PART + 640;
constexpr int L_MRG = L_WST + 20 * 512;
constexpr int L_KV = L_MRG + 4224;
constexpr int L_TOTAL = L_KV + 8 * 2048;

__device__ __forceinline__ void ln_norm(const float* lds, int lane, float x0, float x1, float& n0, float& n1) {
#pragma unroll
  for (int bb = 0; bb < 2; ++bb) {
    float v[8], s = 0.f;
#pragma unroll
    for (int j = 0; j < 8; ++j) { v[j] = lds[L_X + bb * 512 + lane + 64 * j]; s += v[j]; }
    const float mu = wave_sum(s) * (1.f / DM);
    float q = 0.f;
#pragma unroll
    for (int j = 0; j < 8; ++j) { const float d = v[j] - mu; q += d * d; }
    const float var = rbf(wave_sum(q) * (1.f / DM));
    const float sd = rbf(__builtin_amdgcn_sqrtf(rbf(var + 1e-5f)));
    const float t = rbf(rbf((bb ? x1 : x0) - rbf(mu)) * frcp(sd));
    if (bb) n1 = t; else n0 = t;
  }
}
__device__ __forceinline__ void gemv16_part(float* lds, int tid, const u32x4* wv) {
  const int lane = tid & 63, wave = tid >> 6;
  float acc[16];
#pragma unroll
  for (int j = 0; j < 16; ++j) acc[j] = 0.f;
#pragma unroll
  for (int i = 0; i < 2; ++i) {
    const int row = (tid >> 1) + 256 * i;
    const float a0 = lds[L_A + row], a1 = lds[L_A + 512 + row];
#pragma unroll
    for (int j = 0; j < 4; ++j) {
      const float lo = bflo(wv[i][j]), hi = bfhi(wv[i][j]);
      acc[2 * j] = fmaf(a0, lo, acc[2 * j]); acc[2 * j + 1] = fmaf(a0, hi, acc[2 * j + 1]);
      acc[8 + 2 * j] = fmaf(a1, lo, acc[8 + 2 * j]); acc[8 + 2 * j + 1] = fmaf(a1, hi, acc[8 + 2 * j + 1]);
    }
  }
#pragma unroll
  for (int h = 0; h < 8; ++h) acc[h] = sum32(acc[h], acc[h + 8]);
#pragma unroll
  for (int h = 0; h < 4; ++h) acc[h] = sum16(acc[h], acc[h + 4]);
#pragma unroll
  for (int h = 0; h < 4; ++h) { float v = acc[h]; v += dpp<XOR2>(v); v += dpp<ROR4>(v); v += dpp<XOR8>(v); acc[h] = v; }
  if ((lane & 14) == 0) *reinterpret_cast<f32x4*>(lds + L_RED + wave * 32 + (lane & 1) * 16 + (lane >> 4) * 4) = f32x4{acc[0], acc[1], acc[2], acc[3]};
}
__device__ __forceinline__ unsigned gemv16_fin(const float* lds, int t) {
  const int half = t >> 3, j = t & 7;
  float s0 = 0.f, s1 = 0.f;
#pragma unroll
  for (int ww = 0; ww < 8; ++ww) { s0 += lds[L_RED + ww * 32 + half * 16 + j]; s1 += lds[L_RED + ww * 32 + half * 16 + 8 + j]; }
  return cvtpk(s0, s1);
}

__global__ void __launch_bounds__(NT, 2) mega(const bf16* __restrict__ x, const bf16* __restrict__ kc, const bf16* __restrict__ vc,
                                              const bf16* __restrict__ wq, const bf16* __restrict__ wo, const bf16* __restrict__ wg,
                                              const bf16* __restrict__ wu, const bf16* __restrict__ wd, const bf16* __restrict__ c,
                                              const bf16* __restrict__ w_mod, bf16* __restrict__ out, char* __restrict__ ws) {
  extern __shared__ __attribute__((aligned(16))) float lds[];
  const int tid = threadIdx.x, lane = tid & 63, wave = __builtin_amdgcn_readfirstlane(tid >> 6);
  const int w = blockIdx.x;
  const unsigned tag = __hip_atomic_load((const unsigned*)(ws + WS_EPOCH), __ATOMIC_RELAXED, __HIP_MEMORY_SCOPE_AGENT) + 1u;
  u64* g1 = (u64*)(ws + WS_G1); u64* g2 = (u64*)(ws + WS_G2); u64* g3 = (u64*)(ws + WS_G3);
  u64* g4 = (u64*)(ws + WS_G4); u64* g5 = (u64*)(ws + WS_G5); u64* g6 = (u64*)(ws + WS_G6);
  u64* ts = (u64*)(ws + WS_TS); (void)ts;
  const int pair = w >> 4, b = pair >> 3, kvh = pair & 7, s = w & 15;
  const int r = lane >> 4, cc = lane & 15;
  const int wx = w & 7, wi = w >> 3;
  const bool has_mod = wi < 24;
  const int mslice = wi < 8 ? wx * 8 + wi : (wi < 24 ? 64 + wx * 16 + (wi - 8) : 0);
  const int qslice = wx * 32 + wi;
  const int cg = wx * 8 + (wi & 7), kq = wi >> 3;
  const bool has_gu = wi < 28;
  const int wgu = has_gu ? wx * 28 + wi : 0;
  const int col0 = 2 * (wx * 32 + wi);
  STAMP(0);
  const unsigned short* cs = (const unsigned short*)c; const unsigned short* xs = (const unsigned short*)x;
  const float c0 = bf2f(cs[tid]), c1 = bf2f(cs[512 + tid]);
  const float x0 = bf2f(xs[tid]), x1 = bf2f(xs[512 + tid]);
  u32x4 wm[2], wqv[2];
#pragma unroll
  for (int i = 0; i < 2; ++i) {
    const int row = (tid >> 1) + 256 * i;
    wm[i] = *reinterpret_cast<const u32x4*>(w_mod + (size_t)row * (6 * DM) + mslice * 16 + (tid & 1) * 8);
    wqv[i] = *reinterpret_cast<const u32x4*>(wq + (size_t)row * NQ + qslice * 16 + (tid & 1) * 8);
  }
  lds[L_A + tid] = rbf(c0 * frcp(1.f + __expf(-c0))); lds[L_A + 512 + tid] = rbf(c1 * frcp(1.f + __expf(-c1)));
  lds[L_X + tid] = x0; lds[L_X + 512 + tid] = x1;
  bar();
  gemv16_part(lds, tid, wm);
  float n0, n1;
  ln_norm(lds, lane, x0, x1, n0, n1);
  bar();
  if (has_mod && tid < 16) put(g1 + mslice * 16 + tid, tag, gemv16_fin(lds, tid));
  STAMP(1);
  const size_t base = ((size_t)(b * HKV + kvh) * SK + (size_t)s * PPS * PAGE) * DH + (size_t)r * DH + 8 * cc;
  const bf16* kb = kc + base; const bf16* vb = vc + base;
  u32x4 KA[4], VA[4], KB[4], VB[4];
#define ISSUE(KX, VX, pg_) do { const size_t off_ = (size_t)(pg_) * PAGE * DH; \
    _Pragma("unroll") for (int t_ = 0; t_ < 4; ++t_) { KX[t_] = __builtin_nontemporal_load(reinterpret_cast<const u32x4*>(kb + off_ + t_ * 4 * DH)); VX[t_] = __builtin_nontemporal_load(reinterpret_cast<const u32x4*>(vb + off_ + t_ * 4 * DH)); } } while (0)
#define ISSUE_SEL(KX, VX, pg_, ok_) do { const size_t off_ = (size_t)(pg_) * PAGE * DH; \
    const bf16* kp_ = (ok_) ? kb + off_ : kc; const bf16* vp_ = (ok_) ? vb + off_ : vc; \
    _Pragma("unroll") for (int t_ = 0; t_ < 4; ++t_) { KX[t_] = __builtin_nontemporal_load(reinterpret_cast<const u32x4*>(kp_ + t_ * 4 * DH)); VX[t_] = __builtin_nontemporal_load(reinterpret_cast<const u32x4*>(vp_ + t_ * 4 * DH)); } } while (0)
  const unsigned lkv = (unsigned)(size_t)((__attribute__((address_space(3))) float*)(lds + L_KV));
#define DMA_PAGE(pg_) do { const size_t off_ = (size_t)(pg_) * PAGE * DH; \
    _Pragma("unroll") for (int t_ = 0; t_ < 4; ++t_) { glds16(kb + off_ + t_ * 4 * DH, lkv + wave * 8192 + t_ * 1024); glds16(vb + off_ + t_ * 4 * DH, lkv + wave * 8192 + 4096 + t_ * 1024); } } while (0)
  const int p0 = wave == 0 ? 115 : (wave <= 4 ? (wave - 1) * 17 : 51 + (wave - 4) * 16), np = wave == 0 ? 13 : (wave <= 3 ? 17 : 16);
  if (wave != 0) {
    ISSUE(KA, VA, p0);
    DMA_PAGE(p0 + 1);
  } else {
    unsigned v[16];
    sweep<16>(g1 + lane, 64, tag, v);
#pragma unroll
    for (int j = 0; j < 16; ++j) ((unsigned*)(lds + L_MODS))[lane + 64 * j] = v[j];
  }
  bar();
  STAMP(2);
  { const unsigned sh = ((const unsigned*)(lds + L_MODS))[tid], sc = ((const unsigned*)(lds + L_MODS))[512 + tid];
    lds[L_A + tid] = rbf(rbf(n0 * rbf(1.f + bflo(sc))) + bflo(sh));
    lds[L_A + 512 + tid] = rbf(rbf(n1 * rbf(1.f + bfhi(sc))) + bfhi(sh)); }
  bar();
  gemv16_part(lds, tid, wqv);
  bar();
  if (wave != 0) {
    ISSUE(KB, VB, p0 + 2);
    asm volatile("s_waitcnt vmcnt(8)" ::: "memory");
  } else {
    if (tid < 16) put(g2 + qslice * 16 + tid, tag, gemv16_fin(lds, tid));
    STAMP(3);
    unsigned qq[8];
    sweep<8>(g2 + kvh * 512 + lane, 64, tag, qq);
#pragma unroll
    for (int j = 0; j < 8; ++j) ((unsigned short*)(lds + L_Q))[lane + 64 * j] = (unsigned short)(b ? (qq[j] >> 16) : (qq[j] & 0xffffu));
    DMA_PAGE(p0 + 1);
    ISSUE(KA, VA, p0);
    ISSUE(KB, VB, p0 + 2);
  }
  bar();
  STAMP(4);
  u32x4 qpk[4];
#pragma unroll
  for (int g = 0; g < 4; ++g) qpk[g] = *reinterpret_cast<const u32x4*>((const unsigned short*)(lds + L_Q) + g * DH + 8 * cc);
  constexpr float C2 = 0.088388347648318440f * 1.4426950408889634f;
  float m = -INFINITY, l = 0.f, acc[32];
#pragma unroll
  for (int k = 0; k < 32; ++k) acc[k] = 0.f;
  float* prow = lds + L_P + (wave * 4 + r) * 16;
#define TR_STEP(HALF, CTRL, BIT) _Pragma("unroll") for (int h = 0; h < (HALF); ++h) { \
      const float lo_ = xx[h], hi_ = xx[h + (HALF)]; const float t1_ = lo_ + dpp<CTRL>(lo_), t2_ = hi_ + dpp<CTRL>(hi_); \
      xx[h] = (cc & (BIT)) ? t2_ : t1_; }
#define PROCESS(KX, VX) do { \
    float xx[16]; \
    _Pragma("unroll") for (int t = 0; t < 4; ++t) _Pragma("unroll") for (int g = 0; g < 4; ++g) { float d = 0.f; \
        _Pragma("unroll") for (int k = 0; k < 4; ++k) d = dot2(KX[t][k], qpk[g][k], d); xx[t * 4 + g] = d; } \
    TR_STEP(8, XOR8, 8) TR_STEP(4, XOR7, 4) TR_STEP(2, XOR2, 2) TR_STEP(1, XOR1, 1) \
    const float sc = xx[0] * C2; \
    float pm = sc; pm = fmaxf(pm, dpp<ROR4>(pm)); pm = fmaxf(pm, dpp<XOR8>(pm)); pm = xrow16_max(pm); \
    const float mn = fmaxf(m, pm); \
    const float alpha = __builtin_amdgcn_exp2f(m - mn), p = __builtin_amdgcn_exp2f(sc - mn); \
    l = fmaf(l, alpha, p); m = mn; \
    _Pragma("unroll") for (int q4 = 0; q4 < 4; ++q4) { \
      const float al = q4 == 0 ? dpp<0x00>(alpha) : q4 == 1 ? dpp<0x55>(alpha) : q4 == 2 ? dpp<0xAA>(alpha) : dpp<0xFF>(alpha); \
      _Pragma("unroll") for (int k = 0; k < 8; ++k) acc[q4 * 8 + k] *= al; } \
    prow[cc] = p; \
    asm volatile("s_waitcnt lgkmcnt(0)" ::: "memory"); \
    f32x4 pr[4]; \
    _Pragma("unroll") for (int u_ = 0; u_ < 4; ++u_) pr[u_] = *reinterpret_cast<const f32x4*>(prow + 4 * u_); \
    _Pragma("unroll") for (int t = 0; t < 4; ++t) { \
      float vf[8]; \
      _Pragma("unroll") for (int k = 0; k < 4; ++k) { vf[2 * k] = bflo(VX[t][k]); vf[2 * k + 1] = bfhi(VX[t][k]); } \
      _Pragma("unroll") for (int g = 0; g < 4; ++g) { const float pg_ = pr[t][g]; \
        _Pragma("unroll") for (int k = 0; k < 8; ++k) acc[g * 8 + k] = fmaf(pg_, vf[k], acc[g * 8 + k]); } } \
  } while (0)
  const float* kl = lds + L_KV + wave * 2048 + lane * 4;
  for (int i = 0, u = 0; i < np; i += 3, ++u) {
    PROCESS(KA, VA);
    if (i + 1 < np) {
#pragma unroll
      for (int t = 0; t < 4; ++t) { KA[t] = *reinterpret_cast<const u32x4*>(kl + t * 256); VA[t] = *reinterpret_cast<const u32x4*>(kl + 1024 + t * 256); }
      PROCESS(KA, VA);
      if (i + 4 < np) DMA_PAGE(p0 + i + 4);
    }
    ISSUE_SEL(KA, VA, p0 + i + 3, i + 3 < np);
    if (u == 1) {
      const u32x4 a_ = *reinterpret_cast<const u32x4*>(wo + (size_t)(kq * 1024 + tid) * DM + cg * 8), b_ = *reinterpret_cast<const u32x4*>(wo + (size_t)(kq * 1024 + 512 + tid) * DM + cg * 8);
      *reinterpret_cast<u32x4*>(lds + L_WST + (0 * 512 + tid) * 4) = a_;
      *reinterpret_cast<u32x4*>(lds + L_WST + (1 * 512 + tid) * 4) = b_;
    } else if (u == 2) {
      const u32x4 a_ = *reinterpret_cast<const u32x4*>(wg + (size_t)tid * F + 8 * wgu), b_ = *reinterpret_cast<const u32x4*>(wu + (size_t)tid * F + 8 * wgu);
      *reinterpret_cast<u32x4*>(lds + L_WST + (2 * 512 + tid) * 4) = a_;
      *reinterpret_cast<u32x4*>(lds + L_WST + (3 * 512 + tid) * 4) = b_;
    } else if (u == 3) {
      unsigned st[4];
#pragma unroll
      for (int i2 = 0; i2 < 4; ++i2) { const int k_ = tid + 512 * i2; st[i2] = *reinterpret_cast<const unsigned*>(wd + (size_t)(k_ < F ? k_ : 0) * DM + col0); }
      *reinterpret_cast<u32x4*>(lds + L_WST + (4 * 512 + tid) * 4) = u32x4{st[0], st[1], st[2], st[3]};
      if (wave == 0) {
#pragma unroll
        for (int hf = 0; hf < 2; ++hf) { unsigned v[16]; sweep<16>(g1 + 1024 + hf * 1024 + lane, 64, tag, v);
#pragma unroll
          for (int j = 0; j < 16; ++j) ((unsigned*)(lds + L_MODS))[1024 + hf * 1024 + lane + 64 * j] = v[j]; }
      }
    }
    if (i + 2 < np) PROCESS(KB, VB);
    ISSUE_SEL(KB, VB, p0 + i + 5, i + 5 < np);
  }
#undef PROCESS
#undef TR_STEP
  STAMP(5);
#pragma unroll
  for (int k = 0; k < 32; ++k) acc[k] = xrow16_sum(acc[k]);
  { float t = l; t += dpp<ROR4>(t); t += dpp<XOR8>(t); l = xrow16_sum(t); }
  float* mrg = lds + L_MRG;
  if (r == 0) {
#pragma unroll
    for (int g = 0; g < 4; ++g) {
      float* rec = mrg + (wave * 4 + g) * 132;
#pragma unroll
      for (int k = 0; k < 8; k += 4) *reinterpret_cast<f32x4*>(rec + 4 + 8 * cc + k) = f32x4{acc[g * 8 + k], acc[g * 8 + k + 1], acc[g * 8 + k + 2], acc[g * 8 + k + 3]};
      if (cc == g) { rec[0] = m; rec[1] = l; } } }
  bar();
  { const int g = tid >> 7, d = tid & 127;
    float M = -INFINITY;
#pragma unroll
    for (int ww = 0; ww < NW; ++ww) M = fmaxf(M, mrg[(ww * 4 + g) * 132]);
    float L = 0.f, o0 = 0.f;
#pragma unroll
    for (int ww = 0; ww < NW; ++ww) { const float* rec = mrg + (ww * 4 + g) * 132;
      const float wgt = __builtin_amdgcn_exp2f(rec[0] - M);
      L = fmaf(wgt, rec[1], L); o0 = fmaf(wgt, rec[4 + d], o0); }
    u64* dst = g3 + (size_t)((kvh * 4 + g) * 8 + (d >> 4)) * 576 + (b * 16 + s) * 18;
    put(dst + 2 + (d & 15), tag, __float_as_uint(o0));
    if ((d & 15) == 0) { put(dst, tag, __float_as_uint(M)); put(dst + 1, tag, __float_as_uint(L)); }
  }
  STAMP(6);
  { unsigned v[1]; sweep<1>(g3 + (size_t)w * 576 + tid, 0, tag, v);
    lds[L_PART + tid] = __uint_as_float(v[0]);
    if (wave == 0) { sweep<1>(g3 + (size_t)w * 576 + 512 + tid, 0, tag, v); lds[L_PART + 512 + tid] = __uint_as_float(v[0]); } }
  bar();
  STAMP(7);
  if (tid < 32) {
    const int bb = tid >> 4, d = tid & 15;
    const float* pp = lds + L_PART + bb * 16 * 18;
    float M = -INFINITY;
#pragma unroll
    for (int ss = 0; ss < 16; ++ss) M = fmaxf(M, pp[ss * 18]);
    float L = 0.f, o0 = 0.f;
#pragma unroll
    for (int ss = 0; ss < 16; ++ss) { const float wgt = __builtin_amdgcn_exp2f(pp[ss * 18] - M);
      L = fmaf(wgt, pp[ss * 18 + 1], L); o0 = fmaf(wgt, pp[ss * 18 + 2 + d], o0); }
    const unsigned ob = cvtpk(o0 * frcp(L), 0.f) & 0xffffu;
    const unsigned other = __shfl_down(ob, 16);
    if (tid < 16) put(g4 + w * 16 + tid, tag, ob | (other << 16));
  }
  STAMP(8);
  { unsigned ov[2]; sweep<2>(g4 + kq * 1024 + tid, 512, tag, ov);
    STAMP(9);
    float v[16];
#pragma unroll
    for (int j = 0; j < 16; ++j) v[j] = 0.f;
#pragma unroll
    for (int i = 0; i < 2; ++i) {
      const u32x4 wv = *reinterpret_cast<const u32x4*>(lds + L_WST + (i * 512 + tid) * 4);
      const float o0 = bflo(ov[i]), o1 = bfhi(ov[i]);
#pragma unroll
      for (int j = 0; j < 4; ++j) { const float lo = bflo(wv[j]), hi = bfhi(wv[j]);
        v[2 * j] = fmaf(o0, lo, v[2 * j]); v[2 * j + 1] = fmaf(o0, hi, v[2 * j + 1]); v[8 + 2 * j] = fmaf(o1, lo, v[8 + 2 * j]); v[8 + 2 * j + 1] = fmaf(o1, hi, v[8 + 2 * j + 1]); }
    }
#pragma unroll
    for (int h = 0; h < 8; ++h) v[h] = sum32(v[h], v[h + 8]);
#pragma unroll
    for (int h = 0; h < 4; ++h) v[h] = sum16(v[h], v[h + 4]);
#pragma unroll
    for (int h = 0; h < 4; ++h) v[h] = row_sum(v[h]);
    if (cc == 0) *reinterpret_cast<f32x4*>(lds + L_RED + wave * 16 + r * 4) = f32x4{v[0], v[1], v[2], v[3]};
    bar();
    if (tid < 16) {
      float a = 0.f;
#pragma unroll
      for (int ww = 0; ww < NW; ++ww) a += lds[L_RED + ww * 16 + tid];
      put(g5 + (kq * 2 + (tid >> 3)) * 512 + cg * 8 + (tid & 7), tag, __float_as_uint(a));
    } }
  STAMP(10);
  { unsigned pv[8]; sweep<8>(g5 + tid, 512, tag, pv);
    STAMP(11);
    const float a0 = (__uint_as_float(pv[0]) + __uint_as_float(pv[2])) + (__uint_as_float(pv[4]) + __uint_as_float(pv[6]));
    const float a1 = (__uint_as_float(pv[1]) + __uint_as_float(pv[3])) + (__uint_as_float(pv[5]) + __uint_as_float(pv[7]));
    const unsigned ga = ((const unsigned*)(lds + L_MODS))[1024 + tid];
    const float y0 = rbf(x0 + rbf(bflo(ga) * rbf(a0))), y1 = rbf(x1 + rbf(bfhi(ga) * rbf(a1)));
    lds[L_X + tid] = y0; lds[L_X + 512 + tid] = y1;
    bar();
    if (has_gu) {
      float m0, m1; ln_norm(lds, lane, y0, y1, m0, m1);
      const unsigned sh = ((const unsigned*)(lds + L_MODS))[1536 + tid], sc = ((const unsigned*)(lds + L_MODS))[2048 + tid];
      const float h0 = rbf(rbf(m0 * rbf(1.f + bflo(sc))) + bflo(sh)), h1 = rbf(rbf(m1 * rbf(1.f + bfhi(sc))) + bfhi(sh));
      const u32x4 wgv = *reinterpret_cast<const u32x4*>(lds + L_WST + (2 * 512 + tid) * 4), wuv = *reinterpret_cast<const u32x4*>(lds + L_WST + (3 * 512 + tid) * 4);
      float v2[32];
#pragma unroll
      for (int j = 0; j < 4; ++j) {
        v2[2 * j] = h0 * bflo(wgv[j]); v2[2 * j + 1] = h0 * bfhi(wgv[j]); v2[8 + 2 * j] = h0 * bflo(wuv[j]); v2[8 + 2 * j + 1] = h0 * bfhi(wuv[j]);
        v2[16 + 2 * j] = h1 * bflo(wgv[j]); v2[16 + 2 * j + 1] = h1 * bfhi(wgv[j]); v2[24 + 2 * j] = h1 * bflo(wuv[j]); v2[24 + 2 * j + 1] = h1 * bfhi(wuv[j]);
      }
#pragma unroll
      for (int h = 0; h < 16; ++h) v2[h] = sum32(v2[h], v2[h + 16]);
#pragma unroll
      for (int h = 0; h < 8; ++h) v2[h] = sum16(v2[h], v2[h + 8]);
#define TRS(HALF, CTRL, BIT) _Pragma("unroll") for (int h = 0; h < (HALF); ++h) { \
      const float lo_ = v2[h], hi_ = v2[h + (HALF)]; const float t1_ = lo_ + dpp<CTRL>(lo_), t2_ = hi_ + dpp<CTRL>(hi_); \
      v2[h] = (lane & (BIT)) ? t2_ : t1_; }
      TRS(4, XOR8, 8) TRS(2, XOR7, 4) TRS(1, XOR2, 2)
#undef TRS
      v2[0] += dpp<XOR1>(v2[0]);
      if ((lane & 1) == 0) lds[L_RED + wave * 32 + (lane >> 1)] = v2[0];
    }
    bar();
    if (has_gu && tid < 8) {
      float g0 = 0.f, u0 = 0.f, g1s = 0.f, u1 = 0.f;
#pragma unroll
      for (int ww = 0; ww < NW; ++ww) { g0 += lds[L_RED + ww * 32 + tid]; u0 += lds[L_RED + ww * 32 + 8 + tid]; g1s += lds[L_RED + ww * 32 + 16 + tid]; u1 += lds[L_RED + ww * 32 + 24 + tid]; }
      put(g6 + 8 * wgu + tid, tag, cvtpk(rbf(g0) * rbf(u0), rbf(g1s) * rbf(u1)));
    } }
  STAMP(12);
  { unsigned hv[4];
    for (unsigned n = 0;; ++n) {
      bool ok = true;
#pragma unroll
      for (int i = 0; i < 4; ++i) { const int k = tid + 512 * i; const u64 a = __hip_atomic_load(g6 + (k < F ? k : 0), __ATOMIC_RELAXED, __HIP_MEMORY_SCOPE_AGENT); hv[i] = k < F ? (unsigned)a : 0u; ok &= (unsigned)(a >> 32) == tag; }
      if (__all(ok) || n > SPIN_CAP) break;
      __builtin_amdgcn_s_sleep(1);
    }
    STAMP(13);
    float a00 = 0.f, a01 = 0.f, a10 = 0.f, a11 = 0.f;
    const u32x4 wdv = *reinterpret_cast<const u32x4*>(lds + L_WST + (4 * 512 + tid) * 4);
#pragma unroll
    for (int i = 0; i < 4; ++i) {
      const float w0 = bflo(wdv[i]), w1 = bfhi(wdv[i]), o0 = bflo(hv[i]), o1 = bfhi(hv[i]);
      a00 = fmaf(o0, w0, a00); a01 = fmaf(o0, w1, a01); a10 = fmaf(o1, w0, a10); a11 = fmaf(o1, w1, a11);
    }
    a00 = wave_sum(a00); a01 = wave_sum(a01); a10 = wave_sum(a10); a11 = wave_sum(a11);
    if (lane == 0) *reinterpret_cast<f32x4*>(lds + L_RED + wave * 4) = f32x4{a00, a01, a10, a11};
    bar();
    if (tid < 2) {
      float a0 = 0.f, a1 = 0.f;
#pragma unroll
      for (int ww = 0; ww < NW; ++ww) { a0 += lds[L_RED + ww * 4 + tid]; a1 += lds[L_RED + ww * 4 + 2 + tid]; }
      const int n = col0 + tid;
      const unsigned gb = ((const unsigned*)(lds + L_MODS))[2560 + n];
      out[n] = __float2bfloat16(lds[L_X + n] + rbf(bflo(gb) * rbf(a0)));
      out[512 + n] = __float2bfloat16(lds[L_X + 512 + n] + rbf(bfhi(gb) * rbf(a1)));
    }
    if (w == 0 && tid == 0) __hip_atomic_store((unsigned*)(ws + WS_EPOCH), tag, __ATOMIC_RELAXED, __HIP_MEMORY_SCOPE_AGENT);
  }
  STAMP(14);
}

extern "C" void kernel_launch(void* const* d_in, const int* in_sizes, int n_in, void* d_out, int out_size, void* d_ws, size_t ws_size, hipStream_t stream) {
  (void)hipFuncSetAttribute((const void*)mega, hipFuncAttributeMaxDynamicSharedMemorySize, L_TOTAL * 4);
  mega<<<NWG, NT, L_TOTAL * 4, stream>>>((const bf16*)d_in[0], (const bf16*)d_in[1], (const bf16*)d_in[2], (const bf16*)d_in[3], (const bf16*)d_in[4],
                                        (const bf16*)d_in[6], (const bf16*)d_in[7], (const bf16*)d_in[8], (const bf16*)d_in[9], (const bf16*)d_in[10],
                                        (bf16*)d_out, (char*)d_ws);
}
```

```cpp
#include <hip/hip_runtime.h>
#include <hip/hip_bf16.h>
#include <cstdint>

using bf16 = __hip_bfloat16;
typedef unsigned long long u64;
constexpr int B = 2, DM = 512, HQ = 32, HKV = 8, G = 4, SK = 32768, DH = 128, F = 1792;
constexpr int NQ = HQ * DH;
constexpr int NWG = 256, NT = 512, NW = 8;
constexpr int NSPLIT = 16, PAGE = 16;
constexpr int PPS = SK / PAGE / NSPLIT;
constexpr unsigned SPIN_CAP = 1u << 22;

using u32x4 = __attribute__((ext_vector_type(4))) unsigned;
using f32x4 = __attribute__((ext_vector_type(4))) float;
using f32x2 = __attribute__((ext_vector_type(2))) float;
using bf16x2 = __attribute__((ext_vector_type(2))) __bf16;

constexpr size_t WS_G1 = 0;
constexpr size_t WS_G2 = 32768;
constexpr size_t WS_G4 = 65536;
constexpr size_t WS_G5 = 98304;
constexpr size_t WS_G6 = 131072;
constexpr size_t WS_EPOCH = 163840;
constexpr size_t WS_G3 = 1u << 20;
constexpr size_t WS_TS = 8u << 20;

__device__ __forceinline__ float bf2f(unsigned h) { return __builtin_bit_cast(float, h << 16); }
__device__ __forceinline__ float bflo(unsigned w) { return __builtin_bit_cast(float, w << 16); }
__device__ __forceinline__ float bfhi(unsigned w) { return __builtin_bit_cast(float, w & 0xffff0000u); }
__device__ __forceinline__ unsigned cvtpk(float lo, float hi) {
  return __builtin_bit_cast(unsigned, __builtin_convertvector(f32x2{lo, hi}, bf16x2));
}
__device__ __forceinline__ float rbf(float f) { return __builtin_bit_cast(float, cvtpk(f, f) << 16); }
__device__ __forceinline__ float frcp(float x) { return __builtin_amdgcn_rcpf(x); }
__device__ __forceinline__ float dot2(unsigned k, unsigned q, float acc) {
  return __builtin_amdgcn_fdot2_f32_bf16(__builtin_bit_cast(bf16x2, k), __builtin_bit_cast(bf16x2, q), acc, false);
}
template <int CTRL> __device__ __forceinline__ float dpp(float x) {
  return __builtin_bit_cast(float, __builtin_amdgcn_mov_dpp(__builtin_bit_cast(int, x), CTRL, 0xf, 0xf, true));
}
constexpr int XOR1 = 0xB1, XOR2 = 0x4E, XOR7 = 0x141, XOR8 = 0x128, ROR4 = 0x124;
__device__ __forceinline__ float sum32(float a, float b) {
  auto s = __builtin_amdgcn_permlane32_swap(__float_as_uint(a), __float_as_uint(b), false, false);
  return __uint_as_float(s[0]) + __uint_as_float(s[1]);
}
__device__ __forceinline__ float sum16(float a, float b) {
  auto s = __builtin_amdgcn_permlane16_swap(__float_as_uint(a), __float_as_uint(b), false, false);
  return __uint_as_float(s[0]) + __uint_as_float(s[1]);
}
__device__ __forceinline__ float xrow16_max(float x) {
  auto s = __builtin_amdgcn_permlane16_swap(__float_as_uint(x), __float_as_uint(x), false, false);
  x = fmaxf(__uint_as_float(s[0]), __uint_as_float(s[1]));
  auto t = __builtin_amdgcn_permlane32_swap(__float_as_uint(x), __float_as_uint(x), false, false);
  return fmaxf(__uint_as_float(t[0]), __uint_as_float(t[1]));
}
__device__ __forceinline__ float xrow16_sum(float x) { x = sum16(x, x); return sum32(x, x); }
__device__ __forceinline__ float row_sum(float x) { x += dpp<XOR1>(x); x += dpp<XOR2>(x); x += dpp<XOR7>(x); x += dpp<XOR8>(x); return x; }
__device__ __forceinline__ float wave_sum(float x) { return xrow16_sum(row_sum(x)); }
__device__ __forceinline__ void bar() { asm volatile("s_waitcnt lgkmcnt(0)\n\ts_barrier" ::: "memory"); }
__device__ __forceinline__ void put(u64* p, unsigned tag, unsigned v) {
  __hip_atomic_store(p, ((u64)tag << 32) | v, __ATOMIC_RELAXED, __HIP_MEMORY_SCOPE_AGENT);
}
template <int N>
__device__ __forceinline__ void sweep(const u64* p, int stride, unsigned tag, unsigned (&v)[N]) {
  for (unsigned n = 0;; ++n) {
    bool ok = true;
#pragma unroll
    for (int k = 0; k < N; ++k) { const u64 a = __hip_atomic_load(p + k * stride, __ATOMIC_RELAXED, __HIP_MEMORY_SCOPE_AGENT); v[k] = (unsigned)a; ok &= (unsigned)(a >> 32) == tag; }
    if (__all(ok) || n > SPIN_CAP) break;
    __builtin_amdgcn_s_sleep(1);
  }
}
__device__ __forceinline__ void glds16(const void* gsrc, unsigned lds_dst) {
  unsigned keep;
  asm volatile("s_mov_b32 %0, m0\n\ts_mov_b32 m0, %2\n\ts_nop 0\n\tglobal_load_lds_dwordx4 %1, off nt\n\ts_mov_b32 m0, %0" : "=&s"(keep) : "v"(gsrc), "s"(lds_dst) : "memory");
}

__device__ __forceinline__ void glds16d(const void* gsrc, unsigned lds_dst) {
  unsigned keep;
  asm volatile("s_mov_b32 %0, m0\n\ts_mov_b32 m0, %2\n\ts_nop 0\n\tglobal_load_lds_dwordx4 %1, off\n\ts_mov_b32 m0, %0" : "=&s"(keep) : "v"(gsrc), "s"(lds_dst) : "memory");
}
__device__ __forceinline__ void glds4d(const void* gsrc, unsigned lds_dst) {
  unsigned keep;
  asm volatile("s_mov_b32 %0, m0\n\ts_mov_b32 m0, %2\n\ts_nop 0\n\tglobal_load_lds_dword %1, off\n\ts_mov_b32 m0, %0" : "=&s"(keep) : "v"(gsrc), "s"(lds_dst) : "memory");
}

#define STAMP(i) do { } while (0)

constexpr int L_A = 0;
constexpr int L_X = 1024;
constexpr int L_RED = 2048;
constexpr int L_Q = 2304;
constexpr int L_P = 2560;
constexpr int L_MODS = 3072;
constexpr int L_PART = L_MODS + 3072;
constexpr int L_WST = L_PART + 640;
constexpr int L_MRG = L_WST + 20 * 512;
constexpr int L_KV = L_MRG + 4224;
constexpr int L_TOTAL = L_KV + 8 * 2048;

__device__ __forceinline__ void ln_norm(const float* lds, int lane, float x0, float x1, float& n0, float& n1) {
#pragma unroll
  for (int bb = 0; bb < 2; ++bb) {
    float v[8], s = 0.f;
#pragma unroll
    for (int j = 0; j < 8; ++j) { v[j] = lds[L_X + bb * 512 + lane + 64 * j]; s += v[j]; }
    const float mu = wave_sum(s) * (1.f / DM);
    float q = 0.f;
#pragma unroll
    for (int j = 0; j < 8; ++j) { const float d = v[j] - mu; q += d * d; }
    const float var = rbf(wave_sum(q) * (1.f / DM));
    const float sd = rbf(__builtin_amdgcn_sqrtf(rbf(var + 1e-5f)));
    const float t = rbf(rbf((bb ? x1 : x0) - rbf(mu)) * frcp(sd));
    if (bb) n1 = t; else n0 = t;
  }
}
__device__ __forceinline__ void gemv16_part(float* lds, int tid, const u32x4* wv) {
  const int lane = tid & 63, wave = tid >> 6;
  float acc[16];
#pragma unroll
  for (int j = 0; j < 16; ++j) acc[j] = 0.f;
#pragma unroll
  for (int i = 0; i < 2; ++i) {
    const int row = (tid >> 1) + 256 * i;
    const float a0 = lds[L_A + row], a1 = lds[L_A + 512 + row];
#pragma unroll
    for (int j = 0; j < 4; ++j) {
      const float lo = bflo(wv[i][j]), hi = bfhi(wv[i][j]);
      acc[2 * j] = fmaf(a0, lo, acc[2 * j]); acc[2 * j + 1] = fmaf(a0, hi, acc[2 * j + 1]);
      acc[8 + 2 * j] = fmaf(a1, lo, acc[8 + 2 * j]); acc[8 + 2 * j + 1] = fmaf(a1, hi, acc[8 + 2 * j + 1]);
    }
  }
#pragma unroll
  for (int h = 0; h < 8; ++h) acc[h] = sum32(acc[h], acc[h + 8]);
#pragma unroll
  for (int h = 0; h < 4; ++h) acc[h] = sum16(acc[h], acc[h + 4]);
#pragma unroll
  for (int h = 0; h < 4; ++h) { float v = acc[h]; v += dpp<XOR2>(v); v += dpp<ROR4>(v); v += dpp<XOR8>(v); acc[h] = v; }
  if ((lane & 14) == 0) *reinterpret_cast<f32x4*>(lds + L_RED + wave * 32 + (lane & 1) * 16 + (lane >> 4) * 4) = f32x4{acc[0], acc[1], acc[2], acc[3]};
}
__device__ __forceinline__ unsigned gemv16_fin(const float* lds, int t) {
  const int half = t >> 3, j = t & 7;
  float s0 = 0.f, s1 = 0.f;
#pragma unroll
  for (int ww = 0; ww < 8; ++ww) { s0 += lds[L_RED + ww * 32 + half * 16 + j]; s1 += lds[L_RED + ww * 32 + half * 16 + 8 + j]; }
  return cvtpk(s0, s1);
}

__global__ void __launch_bounds__(NT, 2) mega(const bf16* __restrict__ x, const bf16* __restrict__ kc, const bf16* __restrict__ vc,
                                              const bf16* __restrict__ wq, const bf16* __restrict__ wo, const bf16* __restrict__ wg,
                                              const bf16* __restrict__ wu, const bf16* __restrict__ wd, const bf16* __restrict__ c,
                                              const bf16* __restrict__ w_mod, bf16* __restrict__ out, char* __restrict__ ws) {
  extern __shared__ __attribute__((aligned(16))) float lds[];
  const int tid = threadIdx.x, lane = tid & 63, wave = __builtin_amdgcn_readfirstlane(tid >> 6);
  const int w = blockIdx.x;
  const unsigned tag = __hip_atomic_load((const unsigned*)(ws + WS_EPOCH), __ATOMIC_RELAXED, __HIP_MEMORY_SCOPE_AGENT) + 1u;
  u64* g1 = (u64*)(ws + WS_G1); u64* g2 = (u64*)(ws + WS_G2); u64* g3 = (u64*)(ws + WS_G3);
  u64* g4 = (u64*)(ws + WS_G4); u64* g5 = (u64*)(ws + WS_G5); u64* g6 = (u64*)(ws + WS_G6);
  u64* ts = (u64*)(ws + WS_TS); (void)ts;
  const int pair = w >> 4, b = pair >> 3, kvh = pair & 7, s = w & 15;
  const int r = lane >> 4, cc = lane & 15;
  const int wx = w & 7, wi = w >> 3;
  const bool has_mod = wi < 24;
  const int mslice = wi < 8 ? wx * 8 + wi : (wi < 24 ? 64 + wx * 16 + (wi - 8) : 0);
  const int qslice = wx * 32 + wi;
  const int cg = wx * 8 + (wi & 7), kq = wi >> 3;
  const bool has_gu = wi < 28;
  const int wgu = has_gu ? wx * 28 + wi : 0;
  const int col0 = 2 * (wx * 32 + wi);
  STAMP(0);
  const unsigned short* cs = (const unsigned short*)c; const unsigned short* xs = (const unsigned short*)x;
  const float c0 = bf2f(cs[tid]), c1 = bf2f(cs[512 + tid]);
  const float x0 = bf2f(xs[tid]), x1 = bf2f(xs[512 + tid]);
  u32x4 wm[2], wqv[2];
#pragma unroll
  for (int i = 0; i < 2; ++i) {
    const int row = (tid >> 1) + 256 * i;
    wm[i] = *reinterpret_cast<const u32x4*>(w_mod + (size_t)row * (6 * DM) + mslice * 16 + (tid & 1) * 8);
    wqv[i] = *reinterpret_cast<const u32x4*>(wq + (size_t)row * NQ + qslice * 16 + (tid & 1) * 8);
  }
  lds[L_A + tid] = rbf(c0 * frcp(1.f + __expf(-c0))); lds[L_A + 512 + tid] = rbf(c1 * frcp(1.f + __expf(-c1)));
  lds[L_X + tid] = x0; lds[L_X + 512 + tid] = x1;
  bar();
  gemv16_part(lds, tid, wm);
  float n0, n1;
  ln_norm(lds, lane, x0, x1, n0, n1);
  bar();
  if (has_mod && tid < 16) put(g1 + mslice * 16 + tid, tag, gemv16_fin(lds, tid));
  STAMP(1);
  const size_t base = ((size_t)(b * HKV + kvh) * SK + (size_t)s * PPS * PAGE) * DH + (size_t)r * DH + 8 * cc;
  const bf16* kb = kc + base; const bf16* vb = vc + base;
  u32x4 KA[4], VA[4], KB[4], VB[4];
#define ISSUE(KX, VX, pg_) do { const size_t off_ = (size_t)(pg_) * PAGE * DH; \
    _Pragma("unroll") for (int t_ = 0; t_ < 4; ++t_) { KX[t_] = __builtin_nontemporal_load(reinterpret_cast<const u32x4*>(kb + off_ + t_ * 4 * DH)); VX[t_] = __builtin_nontemporal_load(reinterpret_cast<const u32x4*>(vb + off_ + t_ * 4 * DH)); } } while (0)
#define ISSUE_SEL(KX, VX, pg_, ok_) do { const size_t off_ = (size_t)(pg_) * PAGE * DH; \
    const bf16* kp_ = (ok_) ? kb + off_ : kc; const bf16* vp_ = (ok_) ? vb + off_ : vc; \
    _Pragma("unroll") for (int t_ = 0; t_ < 4; ++t_) { KX[t_] = __builtin_nontemporal_load(reinterpret_cast<const u32x4*>(kp_ + t_ * 4 * DH)); VX[t_] = __builtin_nontemporal_load(reinterpret_cast<const u32x4*>(vp_ + t_ * 4 * DH)); } } while (0)
  const unsigned lkv = (unsigned)(size_t)((__attribute__((address_space(3))) float*)(lds + L_KV));
#define DMA_PAGE(slot_) do { const size_t off_ = (size_t)(120 + (slot_)) * PAGE * DH; \
    _Pragma("unroll") for (int t_ = 0; t_ < 4; ++t_) { glds16(kb + off_ + t_ * 4 * DH, lkv + (slot_) * 8192 + t_ * 1024); glds16(vb + off_ + t_ * 4 * DH, lkv + (slot_) * 8192 + 4096 + t_ * 1024); } } while (0)
  const int pfirst = wave == 0 ? 106 : (wave <= 5 ? (wave - 1) * 16 : 64 + (wave - 5) * 14), nu = (wave >= 1 && wave <= 4) ? 8 : 7;
  constexpr int pstep = 1;
  if (wave != 0) {
    ISSUE(KA, VA, pfirst);
    DMA_PAGE(wave);
    if (wave == 1) DMA_PAGE(0);
  } else {
    unsigned v[16];
    sweep<16>(g1 + lane, 64, tag, v);
#pragma unroll
    for (int j = 0; j < 16; ++j) ((unsigned*)(lds + L_MODS))[lane + 64 * j] = v[j];
  }
  bar();
  STAMP(2);
  { const unsigned sh = ((const unsigned*)(lds + L_MODS))[tid], sc = ((const unsigned*)(lds + L_MODS))[512 + tid];
    lds[L_A + tid] = rbf(rbf(n0 * rbf(1.f + bflo(sc))) + bflo(sh));
    lds[L_A + 512 + tid] = rbf(rbf(n1 * rbf(1.f + bfhi(sc))) + bfhi(sh)); }
  bar();
  gemv16_part(lds, tid, wqv);
  bar();
  if (wave != 0) {
    ISSUE(KB, VB, pfirst + pstep);
    asm volatile("s_waitcnt vmcnt(8)" ::: "memory");
  } else {
    if (tid < 16) put(g2 + qslice * 16 + tid, tag, gemv16_fin(lds, tid));
    STAMP(3);
    unsigned qq[8];
    sweep<8>(g2 + kvh * 512 + lane, 64, tag, qq);
#pragma unroll
    for (int j = 0; j < 8; ++j) ((unsigned short*)(lds + L_Q))[lane + 64 * j] = (unsigned short)(b ? (qq[j] >> 16) : (qq[j] & 0xffffu));
    ISSUE(KA, VA, pfirst);
    ISSUE(KB, VB, pfirst + pstep);
  }
  bar();
  STAMP(4);
  u32x4 qpk[4];
#pragma unroll
  for (int g = 0; g < 4; ++g) qpk[g] = *reinterpret_cast<const u32x4*>((const unsigned short*)(lds + L_Q) + g * DH + 8 * cc);
  constexpr float C2 = 0.088388347648318440f * 1.4426950408889634f;
  float m = -INFINITY, l = 0.f, acc[32];
#pragma unroll
  for (int k = 0; k < 32; ++k) acc[k] = 0.f;
  float* prow = lds + L_P + (wave * 4 + r) * 16;
#define TR_STEP(HALF, CTRL, BIT) _Pragma("unroll") for (int h = 0; h < (HALF); ++h) { \
      const float lo_ = xx[h], hi_ = xx[h + (HALF)]; const float t1_ = lo_ + dpp<CTRL>(lo_), t2_ = hi_ + dpp<CTRL>(hi_); \
      xx[h] = (cc & (BIT)) ? t2_ : t1_; }
#define PROCESS(KX, VX) do { \
    float xx[16]; \
    _Pragma("unroll") for (int t = 0; t < 4; ++t) _Pragma("unroll") for (int g = 0; g < 4; ++g) { float d = 0.f; \
        _Pragma("unroll") for (int k = 0; k < 4; ++k) d = dot2(KX[t][k], qpk[g][k], d); xx[t * 4 + g] = d; } \
    TR_STEP(8, XOR8, 8) TR_STEP(4, XOR7, 4) TR_STEP(2, XOR2, 2) TR_STEP(1, XOR1, 1) \
    const float sc = xx[0] * C2; \
    float pm = sc; pm = fmaxf(pm, dpp<ROR4>(pm)); pm = fmaxf(pm, dpp<XOR8>(pm)); pm = xrow16_max(pm); \
    const float mn = fmaxf(m, pm); \
    const float alpha = __builtin_amdgcn_exp2f(m - mn), p = __builtin_amdgcn_exp2f(sc - mn); \
    l = fmaf(l, alpha, p); m = mn; \
    _Pragma("unroll") for (int q4 = 0; q4 < 4; ++q4) { \
      const float al = q4 == 0 ? dpp<0x00>(alpha) : q4 == 1 ? dpp<0x55>(alpha) : q4 == 2 ? dpp<0xAA>(alpha) : dpp<0xFF>(alpha); \
      _Pragma("unroll") for (int k = 0; k < 8; ++k) acc[q4 * 8 + k] *= al; } \
    prow[cc] = p; \
    asm volatile("s_waitcnt lgkmcnt(0)" ::: "memory"); \
    f32x4 pr[4]; \
    _Pragma("unroll") for (int u_ = 0; u_ < 4; ++u_) pr[u_] = *reinterpret_cast<const f32x4*>(prow + 4 * u_); \
    _Pragma("unroll") for (int t = 0; t < 4; ++t) { \
      float vf[8]; \
      _Pragma("unroll") for (int k = 0; k < 4; ++k) { vf[2 * k] = bflo(VX[t][k]); vf[2 * k + 1] = bfhi(VX[t][k]); } \
      _Pragma("unroll") for (int g = 0; g < 4; ++g) { const float pg_ = pr[t][g]; \
        _Pragma("unroll") for (int k = 0; k < 8; ++k) acc[g * 8 + k] = fmaf(pg_, vf[k], acc[g * 8 + k]); } } \
  } while (0)
  for (int u = 0; u < nu; ++u) {
    PROCESS(KA, VA);
    if (u == 0) {
      const float* kl = lds + L_KV + wave * 2048 + lane * 4;
#pragma unroll
      for (int t = 0; t < 4; ++t) { KA[t] = *reinterpret_cast<const u32x4*>(kl + t * 256); VA[t] = *reinterpret_cast<const u32x4*>(kl + 1024 + t * 256); }
      PROCESS(KA, VA);
    }
    ISSUE_SEL(KA, VA, pfirst + (2 * u + 2) * pstep, u + 1 < nu);
    if (u == 1) {
      const unsigned lst = (unsigned)(size_t)((__attribute__((address_space(3))) float*)(lds + L_WST)) + wave * 1024;
      glds16d(wo + (size_t)(kq * 1024 + tid) * DM + cg * 8, lst);
      glds16d(wo + (size_t)(kq * 1024 + 512 + tid) * DM + cg * 8, lst + 8192);
      glds16d(wg + (size_t)tid * F + 8 * wgu, lst + 2 * 8192);
      glds16d(wu + (size_t)tid * F + 8 * wgu, lst + 3 * 8192);
      const unsigned lsd = (unsigned)(size_t)((__attribute__((address_space(3))) float*)(lds + L_WST)) + 4 * 8192 + wave * 256;
#pragma unroll
      for (int i = 0; i < 4; ++i) { const int k_ = tid + 512 * i; glds4d(wd + (size_t)(k_ < F ? k_ : 0) * DM + col0, lsd + i * 2048); }
    } else if (u == 3 && wave == 0) {
#pragma unroll
      for (int hf = 0; hf < 2; ++hf) { unsigned v[16]; sweep<16>(g1 + 1024 + hf * 1024 + lane, 64, tag, v);
#pragma unroll
        for (int j = 0; j < 16; ++j) ((unsigned*)(lds + L_MODS))[1024 + hf * 1024 + lane + 64 * j] = v[j]; }
    }
    PROCESS(KB, VB);
    ISSUE_SEL(KB, VB, pfirst + (2 * u + 3) * pstep, u + 1 < nu);
  }
#undef PROCESS
#undef TR_STEP
  STAMP(5);
  asm volatile("s_waitcnt vmcnt(0)" ::: "memory");
#pragma unroll
  for (int k = 0; k < 32; ++k) acc[k] = xrow16_sum(acc[k]);
  { float t = l; t += dpp<ROR4>(t); t += dpp<XOR8>(t); l = xrow16_sum(t); }
  float* mrg = lds + L_MRG;
  if (r == 0) {
#pragma unroll
    for (int g = 0; g < 4; ++g) {
      float* rec = mrg + (wave * 4 + g) * 132;
#pragma unroll
      for (int k = 0; k < 8; k += 4) *reinterpret_cast<f32x4*>(rec + 4 + 8 * cc + k) = f32x4{acc[g * 8 + k], acc[g * 8 + k + 1], acc[g * 8 + k + 2], acc[g * 8 + k + 3]};
      if (cc == g) { rec[0] = m; rec[1] = l; } } }
  bar();
  { const int g = tid >> 7, d = tid & 127;
    float M = -INFINITY;
#pragma unroll
    for (int ww = 0; ww < NW; ++ww) M = fmaxf(M, mrg[(ww * 4 + g) * 132]);
    float L = 0.f, o0 = 0.f;
#pragma unroll
    for (int ww = 0; ww < NW; ++ww) { const float* rec = mrg + (ww * 4 + g) * 132;
      const float wgt = __builtin_amdgcn_exp2f(rec[0] - M);
      L = fmaf(wgt, rec[1], L); o0 = fmaf(wgt, rec[4 + d], o0); }
    u64* dst = g3 + (size_t)((kvh * 4 + g) * 8 + (d >> 4)) * 576 + (b * 16 + s) * 18;
    put(dst + 2 + (d & 15), tag, __float_as_uint(o0));
    if ((d & 15) == 0) { put(dst, tag, __float_as_uint(M)); put(dst + 1, tag, __float_as_uint(L)); }
  }
  STAMP(6);
  { unsigned v[1]; sweep<1>(g3 + (size_t)w * 576 + tid, 0, tag, v);
    lds[L_PART + tid] = __uint_as_float(v[0]);
    if (wave == 0) { sweep<1>(g3 + (size_t)w * 576 + 512 + tid, 0, tag, v); lds[L_PART + 512 + tid] = __uint_as_float(v[0]); } }
  bar();
  STAMP(7);
  if (tid < 32) {
    const int bb = tid >> 4, d = tid & 15;
    const float* pp = lds + L_PART + bb * 16 * 18;
    float M = -INFINITY;
#pragma unroll
    for (int ss = 0; ss < 16; ++ss) M = fmaxf(M, pp[ss * 18]);
    float L = 0.f, o0 = 0.f;
#pragma unroll
    for (int ss = 0; ss < 16; ++ss) { const float wgt = __builtin_amdgcn_exp2f(pp[ss * 18] - M);
      L = fmaf(wgt, pp[ss * 18 + 1], L); o0 = fmaf(wgt, pp[ss * 18 + 2 + d], o0); }
    const unsigned ob = cvtpk(o0 * frcp(L), 0.f) & 0xffffu;
    const unsigned other = __shfl_down(ob, 16);
    if (tid < 16) put(g4 + w * 16 + tid, tag, ob | (other << 16));
  }
  STAMP(8);
  { unsigned ov[2]; sweep<2>(g4 + kq * 1024 + tid, 512, tag, ov);
    STAMP(9);
    float v[16];
#pragma unroll
    for (int j = 0; j < 16; ++j) v[j] = 0.f;
#pragma unroll
    for (int i = 0; i < 2; ++i) {
      const u32x4 wv = *reinterpret_cast<const u32x4*>(lds + L_WST + (i * 512 + tid) * 4);
      const float o0 = bflo(ov[i]), o1 = bfhi(ov[i]);
#pragma unroll
      for (int j = 0; j < 4; ++j) { const float lo = bflo(wv[j]), hi = bfhi(wv[j]);
        v[2 * j] = fmaf(o0, lo, v[2 * j]); v[2 * j + 1] = fmaf(o0, hi, v[2 * j + 1]); v[8 + 2 * j] = fmaf(o1, lo, v[8 + 2 * j]); v[8 + 2 * j + 1] = fmaf(o1, hi, v[8 + 2 * j + 1]); }
    }
#pragma unroll
    for (int h = 0; h < 8; ++h) v[h] = sum32(v[h], v[h + 8]);
#pragma unroll
    for (int h = 0; h < 4; ++h) v[h] = sum16(v[h], v[h + 4]);
#pragma unroll
    for (int h = 0; h < 4; ++h) v[h] = row_sum(v[h]);
    if (cc == 0) *reinterpret_cast<f32x4*>(lds + L_RED + wave * 16 + r * 4) = f32x4{v[0], v[1], v[2], v[3]};
    bar();
    if (tid < 16) {
      float a = 0.f;
#pragma unroll
      for (int ww = 0; ww < NW; ++ww) a += lds[L_RED + ww * 16 + tid];
      put(g5 + (kq * 2 + (tid >> 3)) * 512 + cg * 8 + (tid & 7), tag, __float_as_uint(a));
    } }
  STAMP(10);
  { unsigned pv[8]; sweep<8>(g5 + tid, 512, tag, pv);
    STAMP(11);
    const float a0 = (__uint_as_float(pv[0]) + __uint_as_float(pv[2])) + (__uint_as_float(pv[4]) + __uint_as_float(pv[6]));
    const float a1 = (__uint_as_float(pv[1]) + __uint_as_float(pv[3])) + (__uint_as_float(pv[5]) + __uint_as_float(pv[7]));
    const unsigned ga = ((const unsigned*)(lds + L_MODS))[1024 + tid];
    const float y0 = rbf(x0 + rbf(bflo(ga) * rbf(a0))), y1 = rbf(x1 + rbf(bfhi(ga) * rbf(a1)));
    lds[L_X + tid] = y0; lds[L_X + 512 + tid] = y1;
    bar();
    if (has_gu) {
      float m0, m1; ln_norm(lds, lane, y0, y1, m0, m1);
      const unsigned sh = ((const unsigned*)(lds + L_MODS))[1536 + tid], sc = ((const unsigned*)(lds + L_MODS))[2048 + tid];
      const float h0 = rbf(rbf(m0 * rbf(1.f + bflo(sc))) + bflo(sh)), h1 = rbf(rbf(m1 * rbf(1.f + bfhi(sc))) + bfhi(sh));
      const u32x4 wgv = *reinterpret_cast<const u32x4*>(lds + L_WST + (2 * 512 + tid) * 4), wuv = *reinterpret_cast<const u32x4*>(lds + L_WST + (3 * 512 + tid) * 4);
      float v2[32];
#pragma unroll
      for (int j = 0; j < 4; ++j) {
        v2[2 * j] = h0 * bflo(wgv[j]); v2[2 * j + 1] = h0 * bfhi(wgv[j]); v2[8 + 2 * j] = h0 * bflo(wuv[j]); v2[8 + 2 * j + 1] = h0 * bfhi(wuv[j]);
        v2[16 + 2 * j] = h1 * bflo(wgv[j]); v2[16 + 2 * j + 1] = h1 * bfhi(wgv[j]); v2[24 + 2 * j] = h1 * bflo(wuv[j]); v2[24 + 2 * j + 1] = h1 * bfhi(wuv[j]);
      }
#pragma unroll
      for (int h = 0; h < 16; ++h) v2[h] = sum32(v2[h], v2[h + 16]);
#pragma unroll
      for (int h = 0; h < 8; ++h) v2[h] = sum16(v2[h], v2[h + 8]);
#define TRS(HALF, CTRL, BIT) _Pragma("unroll") for (int h = 0; h < (HALF); ++h) { \
      const float lo_ = v2[h], hi_ = v2[h + (HALF)]; const float t1_ = lo_ + dpp<CTRL>(lo_), t2_ = hi_ + dpp<CTRL>(hi_); \
      v2[h] = (lane & (BIT)) ? t2_ : t1_; }
      TRS(4, XOR8, 8) TRS(2, XOR7, 4) TRS(1, XOR2, 2)
#undef TRS
      v2[0] += dpp<XOR1>(v2[0]);
      if ((lane & 1) == 0) lds[L_RED + wave * 32 + (lane >> 1)] = v2[0];
    }
    bar();
    if (has_gu && tid < 8) {
      float g0 = 0.f, u0 = 0.f, g1s = 0.f, u1 = 0.f;
#pragma unroll
      for (int ww = 0; ww < NW; ++ww) { g0 += lds[L_RED + ww * 32 + tid]; u0 += lds[L_RED + ww * 32 + 8 + tid]; g1s += lds[L_RED + ww * 32 + 16 + tid]; u1 += lds[L_RED + ww * 32 + 24 + tid]; }
      put(g6 + 8 * wgu + tid, tag, cvtpk(rbf(g0) * rbf(u0), rbf(g1s) * rbf(u1)));
    } }
  STAMP(12);
  { unsigned hv[4];
    for (unsigned n = 0;; ++n) {
      bool ok = true;
#pragma unroll
      for (int i = 0; i < 4; ++i) { const int k = tid + 512 * i; const u64 a = __hip_atomic_load(g6 + (k < F ? k : 0), __ATOMIC_RELAXED, __HIP_MEMORY_SCOPE_AGENT); hv[i] = k < F ? (unsigned)a : 0u; ok &= (unsigned)(a >> 32) == tag; }
      if (__all(ok) || n > SPIN_CAP) break;
      __builtin_amdgcn_s_sleep(1);
    }
    STAMP(13);
    float a00 = 0.f, a01 = 0.f, a10 = 0.f, a11 = 0.f;
    const u32x4 wdv = u32x4{((const unsigned*)(lds + L_WST))[4 * 2048 + tid], ((const unsigned*)(lds + L_WST))[4 * 2048 + 512 + tid], ((const unsigned*)(lds + L_WST))[4 * 2048 + 1024 + tid], ((const unsigned*)(lds + L_WST))[4 * 2048 + 1536 + tid]};
#pragma unroll
    for (int i = 0; i < 4; ++i) {
      const float w0 = bflo(wdv[i]), w1 = bfhi(wdv[i]), o0 = bflo(hv[i]), o1 = bfhi(hv[i]);
      a00 = fmaf(o0, w0, a00); a01 = fmaf(o0, w1, a01); a10 = fmaf(o1, w0, a10); a11 = fmaf(o1, w1, a11);
    }
    a00 = wave_sum(a00); a01 = wave_sum(a01); a10 = wave_sum(a10); a11 = wave_sum(a11);
    if (lane == 0) *reinterpret_cast<f32x4*>(lds + L_RED + wave * 4) = f32x4{a00, a01, a10, a11};
    bar();
    if (tid < 2) {
      float a0 = 0.f, a1 = 0.f;
#pragma unroll
      for (int ww = 0; ww < NW; ++ww) { a0 += lds[L_RED + ww * 4 + tid]; a1 += lds[L_RED + ww * 4 + 2 + tid]; }
      const int n = col0 + tid;
      const unsigned gb = ((const unsigned*)(lds + L_MODS))[2560 + n];
      out[n] = __float2bfloat16(lds[L_X + n] + rbf(bflo(gb) * rbf(a0)));
      out[512 + n] = __float2bfloat16(lds[L_X + 512 + n] + rbf(bfhi(gb) * rbf(a1)));
    }
    if (w == 0 && tid == 0) __hip_atomic_store((unsigned*)(ws + WS_EPOCH), tag, __ATOMIC_RELAXED, __HIP_MEMORY_SCOPE_AGENT);
  }
  STAMP(14);
}

extern "C" void kernel_launch(void* const* d_in, const int* in_sizes, int n_in, void* d_out, int out_size, void* d_ws, size_t ws_size, hipStream_t stream) {
  (void)hipFuncSetAttribute((const void*)mega, hipFuncAttributeMaxDynamicSharedMemorySize, L_TOTAL * 4);
  mega<<<NWG, NT, L_TOTAL * 4, stream>>>((const bf16*)d_in[0], (const bf16*)d_in[1], (const bf16*)d_in[2], (const bf16*)d_in[3], (const bf16*)d_in[4],
                                        (const bf16*)d_in[6], (const bf16*)d_in[7], (const bf16*)d_in[8], (const bf16*)d_in[9], (const bf16*)d_in[10],
                                        (bf16*)d_out, (char*)d_ws);
}
```

```cpp
#include <hip/hip_runtime.h>
#include <hip/hip_bf16.h>
#include <cstdint>

using bf16 = __hip_bfloat16;
typedef unsigned long long u64;
constexpr int B = 2, DM = 512, HQ = 32, HKV = 8, G = 4, SK = 32768, DH = 128, F = 1792;
constexpr int NQ = HQ * DH;
constexpr int NWG = 256, NT = 512, NW = 8;
constexpr int NSPLIT = 16, PAGE = 16;
constexpr int PPS = SK / PAGE / NSPLIT;
constexpr unsigned SPIN_CAP = 1u << 22;

using u32x4 = __attribute__((ext_vector_type(4))) unsigned;
using f32x4 = __attribute__((ext_vector_type(4))) float;
using f32x2 = __attribute__((ext_vector_type(2))) float;
using bf16x2 = __attribute__((ext_vector_type(2))) __bf16;

constexpr size_t WS_G1 = 0;
constexpr size_t WS_G2 = 32768;
constexpr size_t WS_G4 = 65536;
constexpr size_t WS_G5 = 98304;
constexpr size_t WS_G6 = 131072;
constexpr size_t WS_EPOCH = 163840;
constexpr size_t WS_G3 = 1u << 20;
constexpr size_t WS_TS = 8u << 20;

__device__ __forceinline__ float bf2f(unsigned h) { return __builtin_bit_cast(float, h << 16); }
__device__ __forceinline__ float bflo(unsigned w) { return __builtin_bit_cast(float, w << 16); }
__device__ __forceinline__ float bfhi(unsigned w) { return __builtin_bit_cast(float, w & 0xffff0000u); }
__device__ __forceinline__ unsigned cvtpk(float lo, float hi) {
  return __builtin_bit_cast(unsigned, __builtin_convertvector(f32x2{lo, hi}, bf16x2));
}
__device__ __forceinline__ float rbf(float f) { return __builtin_bit_cast(float, cvtpk(f, f) << 16); }
__device__ __forceinline__ float frcp(float x) { return __builtin_amdgcn_rcpf(x); }
__device__ __forceinline__ float dot2(unsigned k, unsigned q, float acc) {
  return __builtin_amdgcn_fdot2_f32_bf16(__builtin_bit_cast(bf16x2, k), __builtin_bit_cast(bf16x2, q), acc, false);
}
template <int CTRL> __device__ __forceinline__ float dpp(float x) {
  return __builtin_bit_cast(float, __builtin_amdgcn_mov_dpp(__builtin_bit_cast(int, x), CTRL, 0xf, 0xf, true));
}
constexpr int XOR1 = 0xB1, XOR2 = 0x4E, XOR7 = 0x141, XOR8 = 0x128, ROR4 = 0x124;
__device__ __forceinline__ float sum32(float a, float b) {
  auto s = __builtin_amdgcn_permlane32_swap(__float_as_uint(a), __float_as_uint(b), false, false);
  return __uint_as_float(s[0]) + __uint_as_float(s[1]);
}
__device__ __forceinline__ float sum16(float a, float b) {
  auto s = __builtin_amdgcn_permlane16_swap(__float_as_uint(a), __float_as_uint(b), false, false);
  return __uint_as_float(s[0]) + __uint_as_float(s[1]);
}
__device__ __forceinline__ float xrow16_max(float x) {
  auto s = __builtin_amdgcn_permlane16_swap(__float_as_uint(x), __float_as_uint(x), false, false);
  x = fmaxf(__uint_as_float(s[0]), __uint_as_float(s[1]));
  auto t = __builtin_amdgcn_permlane32_swap(__float_as_uint(x), __float_as_uint(x), false, false);
  return fmaxf(__uint_as_float(t[0]), __uint_as_float(t[1]));
}
__device__ __forceinline__ float xrow16_sum(float x) { x = sum16(x, x); return sum32(x, x); }
__device__ __forceinline__ float row_sum(float x) { x += dpp<XOR1>(x); x += dpp<XOR2>(x); x += dpp<XOR7>(x); x += dpp<XOR8>(x); return x; }
__device__ __forceinline__ float wave_sum(float x) { return xrow16_sum(row_sum(x)); }
__device__ __forceinline__ void bar() { asm volatile("s_waitcnt lgkmcnt(0)\n\ts_barrier" ::: "memory"); }
__device__ __forceinline__ void put(u64* p, unsigned tag, unsigned v) {
  __hip_atomic_store(p, ((u64)tag << 32) | v, __ATOMIC_RELAXED, __HIP_MEMORY_SCOPE_AGENT);
}
template <int N>
__device__ __forceinline__ void sweep(const u64* p, int stride, unsigned tag, unsigned (&v)[N]) {
  for (unsigned n = 0;; ++n) {
    bool ok = true;
#pragma unroll
    for (int k = 0; k < N; ++k) { const u64 a = __hip_atomic_load(p + k * stride, __ATOMIC_RELAXED, __HIP_MEMORY_SCOPE_AGENT); v[k] = (unsigned)a; ok &= (unsigned)(a >> 32) == tag; }
    if (__all(ok) || n > SPIN_CAP) break;
    __builtin_amdgcn_s_sleep(1);
  }
}
__device__ __forceinline__ void glds16(const void* gsrc, unsigned lds_dst) {
  unsigned keep;
  asm volatile("s_mov_b32 %0, m0\n\ts_mov_b32 m0, %2\n\ts_nop 0\n\tglobal_load_lds_dwordx4 %1, off nt\n\ts_mov_b32 m0, %0" : "=&s"(keep) : "v"(gsrc), "s"(lds_dst) : "memory");
}

__device__ __forceinline__ void glds16d(const void* gsrc, unsigned lds_dst) {
  unsigned keep;
  asm volatile("s_mov_b32 %0, m0\n\ts_mov_b32 m0, %2\n\ts_nop 0\n\tglobal_load_lds_dwordx4 %1, off\n\ts_mov_b32 m0, %0" : "=&s"(keep) : "v"(gsrc), "s"(lds_dst) : "memory");
}
__device__ __forceinline__ void glds4d(const void* gsrc, unsigned lds_dst) {
  unsigned keep;
  asm volatile("s_mov_b32 %0, m0\n\ts_mov_b32 m0, %2\n\ts_nop 0\n\tglobal_load_lds_dword %1, off\n\ts_mov_b32 m0, %0" : "=&s"(keep) : "v"(gsrc), "s"(lds_dst) : "memory");
}

#define STAMP(i) do { } while (0)

constexpr int L_A = 0;
constexpr int L_X = 1024;
constexpr int L_RED = 2048;
constexpr int L_Q = 2304;
constexpr int L_P = 2560;
constexpr int L_MODS = 3072;
constexpr int L_PART = L_MODS + 3072;
constexpr int L_WST = L_PART + 640;
constexpr int L_MRG = L_WST + 20 * 512;
constexpr int L_KV = L_MRG + 4224;
constexpr int L_TOTAL = L_KV + 8 * 2048;

__device__ __forceinline__ void ln_norm(const float* lds, int lane, float x0, float x1, float& n0, float& n1) {
#pragma unroll
  for (int bb = 0; bb < 2; ++bb) {
    float v[8], s = 0.f;
#pragma unroll
    for (int j = 0; j < 8; ++j) { v[j] = lds[L_X + bb * 512 + lane + 64 * j]; s += v[j]; }
    const float mu = wave_sum(s) * (1.f / DM);
    float q = 0.f;
#pragma unroll
    for (int j = 0; j < 8; ++j) { const float d = v[j] - mu; q += d * d; }
    const float var = rbf(wave_sum(q) * (1.f / DM));
    const float sd = rbf(__builtin_amdgcn_sqrtf(rbf(var + 1e-5f)));
    const float t = rbf(rbf((bb ? x1 : x0) - rbf(mu)) * frcp(sd));
    if (bb) n1 = t; else n0 = t;
  }
}
__device__ __forceinline__ void gemv16_part(float* lds, int tid, const u32x4* wv) {
  const int lane = tid & 63, wave = tid >> 6;
  float acc[16];
#pragma unroll
  for (int j = 0; j < 16; ++j) acc[j] = 0.f;
#pragma unroll
  for (int i = 0; i < 2; ++i) {
    const int row = (tid >> 1) + 256 * i;
    const float a0 = lds[L_A + row], a1 = lds[L_A + 512 + row];
#pragma unroll
    for (int j = 0; j < 4; ++j) {
      const float lo = bflo(wv[i][j]), hi = bfhi(wv[i][j]);
      acc[2 * j] = fmaf(a0, lo, acc[2 * j]); acc[2 * j + 1] = fmaf(a0, hi, acc[2 * j + 1]);
      acc[8 + 2 * j] = fmaf(a1, lo, acc[8 + 2 * j]); acc[8 + 2 * j + 1] = fmaf(a1, hi, acc[8 + 2 * j + 1]);
    }
  }
#pragma unroll
  for (int h = 0; h < 8; ++h) acc[h] = sum32(acc[h], acc[h + 8]);
#pragma unroll
  for (int h = 0; h < 4; ++h) acc[h] = sum16(acc[h], acc[h + 4]);
#pragma unroll
  for (int h = 0; h < 4; ++h) { float v = acc[h]; v += dpp<XOR2>(v); v += dpp<ROR4>(v); v += dpp<XOR8>(v); acc[h] = v; }
  if ((lane & 14) == 0) *reinterpret_cast<f32x4*>(lds + L_RED + wave * 32 + (lane & 1) * 16 + (lane >> 4) * 4) = f32x4{acc[0], acc[1], acc[2], acc[3]};
}
__device__ __forceinline__ unsigned gemv16_fin(const float* lds, int t) {
  const int half = t >> 3, j = t & 7;
  float s0 = 0.f, s1 = 0.f;
#pragma unroll
  for (int ww = 0; ww < 8; ++ww) { s0 += lds[L_RED + ww * 32 + half * 16 + j]; s1 += lds[L_RED + ww * 32 + half * 16 + 8 + j]; }
  return cvtpk(s0, s1);
}

__global__ void __launch_bounds__(NT, 2) mega(const bf16* __restrict__ x, const bf16* __restrict__ kc, const bf16* __restrict__ vc,
                                              const bf16* __restrict__ wq, const bf16* __restrict__ wo, const bf16* __restrict__ wg,
                                              const bf16* __restrict__ wu, const bf16* __restrict__ wd, const bf16* __restrict__ c,
                                              const bf16* __restrict__ w_mod, bf16* __restrict__ out, char* __restrict__ ws) {
  extern __shared__ __attribute__((aligned(16))) float lds[];
  const int tid = threadIdx.x, lane = tid & 63, wave = __builtin_amdgcn_readfirstlane(tid >> 6);
  const int w = blockIdx.x;
  const unsigned tag = __hip_atomic_load((const unsigned*)(ws + WS_EPOCH), __ATOMIC_RELAXED, __HIP_MEMORY_SCOPE_AGENT) + 1u;
  u64* g1 = (u64*)(ws + WS_G1); u64* g2 = (u64*)(ws + WS_G2); u64* g3 = (u64*)(ws + WS_G3);
  u64* g4 = (u64*)(ws + WS_G4); u64* g5 = (u64*)(ws + WS_G5); u64* g6 = (u64*)(ws + WS_G6);
  u64* ts = (u64*)(ws + WS_TS); (void)ts;
  const int pair = w >> 4, b = pair >> 3, kvh = pair & 7, s = w & 15;
  const int r = lane >> 4, cc = lane & 15;
  const int wx = w & 7, wi = w >> 3;
  const bool has_mod = wi < 24;
  const int mslice = wi < 8 ? wx * 8 + wi : (wi < 24 ? 64 + wx * 16 + (wi - 8) : 0);
  const int qslice = wx * 32 + wi;
  const int cg = wx * 8 + (wi & 7), kq = wi >> 3;
  const bool has_gu = wi < 28;
  const int wgu = has_gu ? wx * 28 + wi : 0;
  const int col0 = 2 * (wx * 32 + wi);
  STAMP(0);
  const unsigned short* cs = (const unsigned short*)c; const unsigned short* xs = (const unsigned short*)x;
  const float c0 = bf2f(cs[tid]), c1 = bf2f(cs[512 + tid]);
  const float x0 = bf2f(xs[tid]), x1 = bf2f(xs[512 + tid]);
  u32x4 wm[2], wqv[2];
#pragma unroll
  for (int i = 0; i < 2; ++i) {
    const int row = (tid >> 1) + 256 * i;
    wm[i] = *reinterpret_cast<const u32x4*>(w_mod + (size_t)row * (6 * DM) + mslice * 16 + (tid & 1) * 8);
    wqv[i] = *reinterpret_cast<const u32x4*>(wq + (size_t)row * NQ + qslice * 16 + (tid & 1) * 8);
  }
  lds[L_A + tid] = rbf(c0 * frcp(1.f + __expf(-c0))); lds[L_A + 512 + tid] = rbf(c1 * frcp(1.f + __expf(-c1)));
  lds[L_X + tid] = x0; lds[L_X + 512 + tid] = x1;
  bar();
  gemv16_part(lds, tid, wm);
  float n0, n1;
  ln_norm(lds, lane, x0, x1, n0, n1);
  bar();
  if (has_mod && tid < 16) put(g1 + mslice * 16 + tid, tag, gemv16_fin(lds, tid));
  STAMP(1);
  const size_t base = ((size_t)(b * HKV + kvh) * SK + (size_t)s * PPS * PAGE) * DH + (size_t)r * DH + 8 * cc;
  const bf16* kb = kc + base; const bf16* vb = vc + base;
  u32x4 KA[4], VA[4], KB[4], VB[4];
#define ISSUE(KX, VX, pg_) do { const size_t off_ = (size_t)(pg_) * PAGE * DH; \
    _Pragma("unroll") for (int t_ = 0; t_ < 4; ++t_) { KX[t_] = __builtin_nontemporal_load(reinterpret_cast<const u32x4*>(kb + off_ + t_ * 4 * DH)); VX[t_] = __builtin_nontemporal_load(reinterpret_cast<const u32x4*>(vb + off_ + t_ * 4 * DH)); } } while (0)
#define ISSUE_SEL(KX, VX, pg_, ok_) do { const size_t off_ = (size_t)(pg_) * PAGE * DH; \
    const bf16* kp_ = (ok_) ? kb + off_ : kc; const bf16* vp_ = (ok_) ? vb + off_ : vc; \
    _Pragma("unroll") for (int t_ = 0; t_ < 4; ++t_) { KX[t_] = __builtin_nontemporal_load(reinterpret_cast<const u32x4*>(kp_ + t_ * 4 * DH)); VX[t_] = __builtin_nontemporal_load(reinterpret_cast<const u32x4*>(vp_ + t_ * 4 * DH)); } } while (0)
  const unsigned lkv = (unsigned)(size_t)((__attribute__((address_space(3))) float*)(lds + L_KV));
#define DMA_PAGE(slot_) do { const size_t off_ = (size_t)(120 + (slot_)) * PAGE * DH; \
    _Pragma("unroll") for (int t_ = 0; t_ < 4; ++t_) { glds16(kb + off_ + t_ * 4 * DH, lkv + (slot_) * 8192 + t_ * 1024); glds16(vb + off_ + t_ * 4 * DH, lkv + (slot_) * 8192 + 4096 + t_ * 1024); } } while (0)
#define STASH_DMA() do {     \
      const unsigned lst = (unsigned)(size_t)((__attribute__((address_space(3))) float*)(lds + L_WST)) + wave * 1024; \
      glds16d(wo + (size_t)(kq * 1024 + tid) * DM + cg * 8, lst); \
      glds16d(wo + (size_t)(kq * 1024 + 512 + tid) * DM + cg * 8, lst + 8192); \
      glds16d(wg + (size_t)tid * F + 8 * wgu, lst + 2 * 8192); \
      glds16d(wu + (size_t)tid * F + 8 * wgu, lst + 3 * 8192); \
      const unsigned lsd = (unsigned)(size_t)((__attribute__((address_space(3))) float*)(lds + L_WST)) + 4 * 8192 + wave * 256; \
      _Pragma("unroll") for (int i = 0; i < 4; ++i) { const int k_ = tid + 512 * i; glds4d(wd + (size_t)(k_ < F ? k_ : 0) * DM + col0, lsd + i * 2048); } } while (0)
  const int pfirst = wave == 0 ? 106 : (wave <= 5 ? (wave - 1) * 16 : 64 + (wave - 5) * 14), nu = (wave >= 1 && wave <= 4) ? 8 : 7;
  constexpr int pstep = 1;
  if (wave != 0) {
    ISSUE(KA, VA, pfirst);
    DMA_PAGE(wave);
    if (wave == 1) DMA_PAGE(0);
  } else {
    unsigned v[16];
    sweep<16>(g1 + lane, 64, tag, v);
#pragma unroll
    for (int j = 0; j < 16; ++j) ((unsigned*)(lds + L_MODS))[lane + 64 * j] = v[j];
  }
  bar();
  STAMP(2);
  { const unsigned sh = ((const unsigned*)(lds + L_MODS))[tid], sc = ((const unsigned*)(lds + L_MODS))[512 + tid];
    lds[L_A + tid] = rbf(rbf(n0 * rbf(1.f + bflo(sc))) + bflo(sh));
    lds[L_A + 512 + tid] = rbf(rbf(n1 * rbf(1.f + bfhi(sc))) + bfhi(sh)); }
  bar();
  gemv16_part(lds, tid, wqv);
  bar();
  if (wave != 0) {
    ISSUE(KB, VB, pfirst + pstep);
    STASH_DMA();
    asm volatile("s_waitcnt vmcnt(16)" ::: "memory");
  } else {
    if (tid < 16) put(g2 + qslice * 16 + tid, tag, gemv16_fin(lds, tid));
    STAMP(3);
    unsigned qq[8];
    sweep<8>(g2 + kvh * 512 + lane, 64, tag, qq);
#pragma unroll
    for (int j = 0; j < 8; ++j) ((unsigned short*)(lds + L_Q))[lane + 64 * j] = (unsigned short)(b ? (qq[j] >> 16) : (qq[j] & 0xffffu));
    ISSUE(KA, VA, pfirst);
    ISSUE(KB, VB, pfirst + pstep);
  }
  bar();
  STAMP(4);
  u32x4 qpk[4];
#pragma unroll
  for (int g = 0; g < 4; ++g) qpk[g] = *reinterpret_cast<const u32x4*>((const unsigned short*)(lds + L_Q) + g * DH + 8 * cc);
  constexpr float C2 = 0.088388347648318440f * 1.4426950408889634f;
  float m = -INFINITY, l = 0.f, acc[32];
#pragma unroll
  for (int k = 0; k < 32; ++k) acc[k] = 0.f;
  float* prow = lds + L_P + (wave * 4 + r) * 16;
#define TR_STEP(HALF, CTRL, BIT) _Pragma("unroll") for (int h = 0; h < (HALF); ++h) { \
      const float lo_ = xx[h], hi_ = xx[h + (HALF)]; const float t1_ = lo_ + dpp<CTRL>(lo_), t2_ = hi_ + dpp<CTRL>(hi_); \
      xx[h] = (cc & (BIT)) ? t2_ : t1_; }
#define PROCESS(KX, VX) do { \
    float xx[16]; \
    _Pragma("unroll") for (int t = 0; t < 4; ++t) _Pragma("unroll") for (int g = 0; g < 4; ++g) { float d = 0.f; \
        _Pragma("unroll") for (int k = 0; k < 4; ++k) d = dot2(KX[t][k], qpk[g][k], d); xx[t * 4 + g] = d; } \
    TR_STEP(8, XOR8, 8) TR_STEP(4, XOR7, 4) TR_STEP(2, XOR2, 2) TR_STEP(1, XOR1, 1) \
    const float sc = xx[0] * C2; \
    float pm = sc; pm = fmaxf(pm, dpp<ROR4>(pm)); pm = fmaxf(pm, dpp<XOR8>(pm)); pm = xrow16_max(pm); \
    const float mn = fmaxf(m, pm); \
    const float alpha = __builtin_amdgcn_exp2f(m - mn), p = __builtin_amdgcn_exp2f(sc - mn); \
    l = fmaf(l, alpha, p); m = mn; \
    _Pragma("unroll") for (int q4 = 0; q4 < 4; ++q4) { \
      const float al = q4 == 0 ? dpp<0x00>(alpha) : q4 == 1 ? dpp<0x55>(alpha) : q4 == 2 ? dpp<0xAA>(alpha) : dpp<0xFF>(alpha); \
      _Pragma("unroll") for (int k = 0; k < 8; ++k) acc[q4 * 8 + k] *= al; } \
    prow[cc] = p; \
    asm volatile("s_waitcnt lgkmcnt(0)" ::: "memory"); \
    f32x4 pr[4]; \
    _Pragma("unroll") for (int u_ = 0; u_ < 4; ++u_) pr[u_] = *reinterpret_cast<const f32x4*>(prow + 4 * u_); \
    _Pragma("unroll") for (int t = 0; t < 4; ++t) { \
      float vf[8]; \
      _Pragma("unroll") for (int k = 0; k < 4; ++k) { vf[2 * k] = bflo(VX[t][k]); vf[2 * k + 1] = bfhi(VX[t][k]); } \
      _Pragma("unroll") for (int g = 0; g < 4; ++g) { const float pg_ = pr[t][g]; \
        _Pragma("unroll") for (int k = 0; k < 8; ++k) acc[g * 8 + k] = fmaf(pg_, vf[k], acc[g * 8 + k]); } } \
  } while (0)
  for (int u = 0; u < nu; ++u) {
    PROCESS(KA, VA);
    if (u == 0) {
      const float* kl = lds + L_KV + wave * 2048 + lane * 4;
#pragma unroll
      for (int t = 0; t < 4; ++t) { KA[t] = *reinterpret_cast<const u32x4*>(kl + t * 256); VA[t] = *reinterpret_cast<const u32x4*>(kl + 1024 + t * 256); }
      PROCESS(KA, VA);
    }
    ISSUE_SEL(KA, VA, pfirst + (2 * u + 2) * pstep, u + 1 < nu);
    if (u == 1 && wave == 0) {
      STASH_DMA();
    } else if (u == 3 && wave == 0) {
#pragma unroll
      for (int hf = 0; hf < 2; ++hf) { unsigned v[16]; sweep<16>(g1 + 1024 + hf * 1024 + lane, 64, tag, v);
#pragma unroll
        for (int j = 0; j < 16; ++j) ((unsigned*)(lds + L_MODS))[1024 + hf * 1024 + lane + 64 * j] = v[j]; }
    }
    PROCESS(KB, VB);
    ISSUE_SEL(KB, VB, pfirst + (2 * u + 3) * pstep, u + 1 < nu);
  }
#undef PROCESS
#undef TR_STEP
  STAMP(5);
  asm volatile("s_waitcnt vmcnt(0)" ::: "memory");
#pragma unroll
  for (int k = 0; k < 32; ++k) acc[k] = xrow16_sum(acc[k]);
  { float t = l; t += dpp<ROR4>(t); t += dpp<XOR8>(t); l = xrow16_sum(t); }
  float* mrg = lds + L_MRG;
  if (r == 0) {
#pragma unroll
    for (int g = 0; g < 4; ++g) {
      float* rec = mrg + (wave * 4 + g) * 132;
#pragma unroll
      for (int k = 0; k < 8; k += 4) *reinterpret_cast<f32x4*>(rec + 4 + 8 * cc + k) = f32x4{acc[g * 8 + k], acc[g * 8 + k + 1], acc[g * 8 + k + 2], acc[g * 8 + k + 3]};
      if (cc == g) { rec[0] = m; rec[1] = l; } } }
  bar();
  { const int g = tid >> 7, d = tid & 127;
    float M = -INFINITY;
#pragma unroll
    for (int ww = 0; ww < NW; ++ww) M = fmaxf(M, mrg[(ww * 4 + g) * 132]);
    float L = 0.f, o0 = 0.f;
#pragma unroll
    for (int ww = 0; ww < NW; ++ww) { const float* rec = mrg + (ww * 4 + g) * 132;
      const float wgt = __builtin_amdgcn_exp2f(rec[0] - M);
      L = fmaf(wgt, rec[1], L); o0 = fmaf(wgt, rec[4 + d], o0); }
    u64* dst = g3 + (size_t)((kvh * 4 + g) * 8 + (d >> 4)) * 576 + (b * 16 + s) * 18;
    put(dst + 2 + (d & 15), tag, __float_as_uint(o0));
    if ((d & 15) == 0) { put(dst, tag, __float_as_uint(M)); put(dst + 1, tag, __float_as_uint(L)); }
  }
  STAMP(6);
  { unsigned v[1]; sweep<1>(g3 + (size_t)w * 576 + tid, 0, tag, v);
    lds[L_PART + tid] = __uint_as_float(v[0]);
    if (wave == 0) { sweep<1>(g3 + (size_t)w * 576 + 512 + tid, 0, tag, v); lds[L_PART + 512 + tid] = __uint_as_float(v[0]); } }
  bar();
  STAMP(7);
  if (tid < 32) {
    const int bb = tid >> 4, d = tid & 15;
    const float* pp = lds + L_PART + bb * 16 * 18;
    float M = -INFINITY;
#pragma unroll
    for (int ss = 0; ss < 16; ++ss) M = fmaxf(M, pp[ss * 18]);
    float L = 0.f, o0 = 0.f;
#pragma unroll
    for (int ss = 0; ss < 16; ++ss) { const float wgt = __builtin_amdgcn_exp2f(pp[ss * 18] - M);
      L = fmaf(wgt, pp[ss * 18 + 1], L); o0 = fmaf(wgt, pp[ss * 18 + 2 + d], o0); }
    const unsigned ob = cvtpk(o0 * frcp(L), 0.f) & 0xffffu;
    const unsigned other = __shfl_down(ob, 16);
    if (tid < 16) put(g4 + w * 16 + tid, tag, ob | (other << 16));
  }
  STAMP(8);
  { unsigned ov[2]; sweep<2>(g4 + kq * 1024 + tid, 512, tag, ov);
    STAMP(9);
    float v[16];
#pragma unroll
    for (int j = 0; j < 16; ++j) v[j] = 0.f;
#pragma unroll
    for (int i = 0; i < 2; ++i) {
      const u32x4 wv = *reinterpret_cast<const u32x4*>(lds + L_WST + (i * 512 + tid) * 4);
      const float o0 = bflo(ov[i]), o1 = bfhi(ov[i]);
#pragma unroll
      for (int j = 0; j < 4; ++j) { const float lo = bflo(wv[j]), hi = bfhi(wv[j]);
        v[2 * j] = fmaf(o0, lo, v[2 * j]); v[2 * j + 1] = fmaf(o0, hi, v[2 * j + 1]); v[8 + 2 * j] = fmaf(o1, lo, v[8 + 2 * j]); v[8 + 2 * j + 1] = fmaf(o1, hi, v[8 + 2 * j + 1]); }
    }
#pragma unroll
    for (int h = 0; h < 8; ++h) v[h] = sum32(v[h], v[h + 8]);
#pragma unroll
    for (int h = 0; h < 4; ++h) v[h] = sum16(v[h], v[h + 4]);
#pragma unroll
    for (int h = 0; h < 4; ++h) v[h] = row_sum(v[h]);
    if (cc == 0) *reinterpret_cast<f32x4*>(lds + L_RED + wave * 16 + r * 4) = f32x4{v[0], v[1], v[2], v[3]};
    bar();
    if (tid < 16) {
      float a = 0.f;
#pragma unroll
      for (int ww = 0; ww < NW; ++ww) a += lds[L_RED + ww * 16 + tid];
      put(g5 + (kq * 2 + (tid >> 3)) * 512 + cg * 8 + (tid & 7), tag, __float_as_uint(a));
    } }
  STAMP(10);
  { unsigned pv[8]; sweep<8>(g5 + tid, 512, tag, pv);
    STAMP(11);
    const float a0 = (__uint_as_float(pv[0]) + __uint_as_float(pv[2])) + (__uint_as_float(pv[4]) + __uint_as_float(pv[6]));
    const float a1 = (__uint_as_float(pv[1]) + __uint_as_float(pv[3])) + (__uint_as_float(pv[5]) + __uint_as_float(pv[7]));
    const unsigned ga = ((const unsigned*)(lds + L_MODS))[1024 + tid];
    const float y0 = rbf(x0 + rbf(bflo(ga) * rbf(a0))), y1 = rbf(x1 + rbf(bfhi(ga) * rbf(a1)));
    lds[L_X + tid] = y0; lds[L_X + 512 + tid] = y1;
    bar();
    if (has_gu) {
      float m0, m1; ln_norm(lds, lane, y0, y1, m0, m1);
      const unsigned sh = ((const unsigned*)(lds + L_MODS))[1536 + tid], sc = ((const unsigned*)(lds + L_MODS))[2048 + tid];
      const float h0 = rbf(rbf(m0 * rbf(1.f + bflo(sc))) + bflo(sh)), h1 = rbf(rbf(m1 * rbf(1.f + bfhi(sc))) + bfhi(sh));
      const u32x4 wgv = *reinterpret_cast<const u32x4*>(lds + L_WST + (2 * 512 + tid) * 4), wuv = *reinterpret_cast<const u32x4*>(lds + L_WST + (3 * 512 + tid) * 4);
      float v2[32];
#pragma unroll
      for (int j = 0; j < 4; ++j) {
        v2[2 * j] = h0 * bflo(wgv[j]); v2[2 * j + 1] = h0 * bfhi(wgv[j]); v2[8 + 2 * j] = h0 * bflo(wuv[j]); v2[8 + 2 * j + 1] = h0 * bfhi(wuv[j]);
        v2[16 + 2 * j] = h1 * bflo(wgv[j]); v2[16 + 2 * j + 1] = h1 * bfhi(wgv[j]); v2[24 + 2 * j] = h1 * bflo(wuv[j]); v2[24 + 2 * j + 1] = h1 * bfhi(wuv[j]);
      }
#pragma unroll
      for (int h = 0; h < 16; ++h) v2[h] = sum32(v2[h], v2[h + 16]);
#pragma unroll
      for (int h = 0; h < 8; ++h) v2[h] = sum16(v2[h], v2[h + 8]);
#define TRS(HALF, CTRL, BIT) _Pragma("unroll") for (int h = 0; h < (HALF); ++h) { \
      const float lo_ = v2[h], hi_ = v2[h + (HALF)]; const float t1_ = lo_ + dpp<CTRL>(lo_), t2_ = hi_ + dpp<CTRL>(hi_); \
      v2[h] = (lane & (BIT)) ? t2_ : t1_; }
      TRS(4, XOR8, 8) TRS(2, XOR7, 4) TRS(1, XOR2, 2)
#undef TRS
      v2[0] += dpp<XOR1>(v2[0]);
      if ((lane & 1) == 0) lds[L_RED + wave * 32 + (lane >> 1)] = v2[0];
    }
    bar();
    if (has_gu && tid < 8) {
      float g0 = 0.f, u0 = 0.f, g1s = 0.f, u1 = 0.f;
#pragma unroll
      for (int ww = 0; ww < NW; ++ww) { g0 += lds[L_RED + ww * 32 + tid]; u0 += lds[L_RED + ww * 32 + 8 + tid]; g1s += lds[L_RED + ww * 32 + 16 + tid]; u1 += lds[L_RED + ww * 32 + 24 + tid]; }
      put(g6 + 8 * wgu + tid, tag, cvtpk(rbf(g0) * rbf(u0), rbf(g1s) * rbf(u1)));
    } }
  STAMP(12);
  { unsigned hv[4];
    for (unsigned n = 0;; ++n) {
      bool ok = true;
#pragma unroll
      for (int i = 0; i < 4; ++i) { const int k = tid + 512 * i; const u64 a = __hip_atomic_load(g6 + (k < F ? k : 0), __ATOMIC_RELAXED, __HIP_MEMORY_SCOPE_AGENT); hv[i] = k < F ? (unsigned)a : 0u; ok &= (unsigned)(a >> 32) == tag; }
      if (__all(ok) || n > SPIN_CAP) break;
      __builtin_amdgcn_s_sleep(1);
    }
    STAMP(13);
    float a00 = 0.f, a01 = 0.f, a10 = 0.f, a11 = 0.f;
    const u32x4 wdv = u32x4{((const unsigned*)(lds + L_WST))[4 * 2048 + tid], ((const unsigned*)(lds + L_WST))[4 * 2048 + 512 + tid], ((const unsigned*)(lds + L_WST))[4 * 2048 + 1024 + tid], ((const unsigned*)(lds + L_WST))[4 * 2048 + 1536 + tid]};
#pragma unroll
    for (int i = 0; i < 4; ++i) {
      const float w0 = bflo(wdv[i]), w1 = bfhi(wdv[i]), o0 = bflo(hv[i]), o1 = bfhi(hv[i]);
      a00 = fmaf(o0, w0, a00); a01 = fmaf(o0, w1, a01); a10 = fmaf(o1, w0, a10); a11 = fmaf(o1, w1, a11);
    }
    a00 = wave_sum(a00); a01 = wave_sum(a01); a10 = wave_sum(a10); a11 = wave_sum(a11);
    if (lane == 0) *reinterpret_cast<f32x4*>(lds + L_RED + wave * 4) = f32x4{a00, a01, a10, a11};
    bar();
    if (tid < 2) {
      float a0 = 0.f, a1 = 0.f;
#pragma unroll
      for (int ww = 0; ww < NW; ++ww) { a0 += lds[L_RED + ww * 4 + tid]; a1 += lds[L_RED + ww * 4 + 2 + tid]; }
      const int n = col0 + tid;
      const unsigned gb = ((const unsigned*)(lds + L_MODS))[2560 + n];
      out[n] = __float2bfloat16(lds[L_X + n] + rbf(bflo(gb) * rbf(a0)));
      out[512 + n] = __float2bfloat16(lds[L_X + 512 + n] + rbf(bfhi(gb) * rbf(a1)));
    }
    if (w == 0 && tid == 0) __hip_atomic_store((unsigned*)(ws + WS_EPOCH), tag, __ATOMIC_RELAXED, __HIP_MEMORY_SCOPE_AGENT);
  }
  STAMP(14);
}

extern "C" void kernel_launch(void* const* d_in, const int* in_sizes, int n_in, void* d_out, int out_size, void* d_ws, size_t ws_size, hipStream_t stream) {
  (void)hipFuncSetAttribute((const void*)mega, hipFuncAttributeMaxDynamicSharedMemorySize, L_TOTAL * 4);
  mega<<<NWG, NT, L_TOTAL * 4, stream>>>((const bf16*)d_in[0], (const bf16*)d_in[1], (const bf16*)d_in[2], (const bf16*)d_in[3], (const bf16*)d_in[4],
                                        (const bf16*)d_in[6], (const bf16*)d_in[7], (const bf16*)d_in[8], (const bf16*)d_in[9], (const bf16*)d_in[10],
                                        (bf16*)d_out, (char*)d_ws);
}
```

```cpp
#include <hip/hip_runtime.h>
#include <hip/hip_bf16.h>
#include <cstdint>

using bf16 = __hip_bfloat16;
typedef unsigned long long u64;
constexpr int B = 2, DM = 512, HQ = 32, HKV = 8, G = 4, SK = 32768, DH = 128, F = 1792;
constexpr int NQ = HQ * DH;
constexpr int NWG = 256, NT = 512, NW = 8;
constexpr int NSPLIT = 16, PAGE = 16;
constexpr int PPS = SK / PAGE / NSPLIT;
constexpr unsigned SPIN_CAP = 1u << 22;

using u32x4 = __attribute__((ext_vector_type(4))) unsigned;
using f32x4 = __attribute__((ext_vector_type(4))) float;
using f32x2 = __attribute__((ext_vector_type(2))) float;
using bf16x2 = __attribute__((ext_vector_type(2))) __bf16;

constexpr size_t WS_G1 = 0;
constexpr size_t WS_G2 = 32768;
constexpr size_t WS_G4 = 65536;
constexpr size_t WS_G5 = 98304;
constexpr size_t WS_G6 = 131072;
constexpr size_t WS_EPOCH = 163840;
constexpr size_t WS_G3 = 1u << 20;

__device__ __forceinline__ float bf2f(unsigned h) { return __builtin_bit_cast(float, h << 16); }
__device__ __forceinline__ float bflo(unsigned w) { return __builtin_bit_cast(float, w << 16); }
__device__ __forceinline__ float bfhi(unsigned w) { return __builtin_bit_cast(float, w & 0xffff0000u); }
__device__ __forceinline__ unsigned cvtpk(float lo, float hi) {
  return __builtin_bit_cast(unsigned, __builtin_convertvector(f32x2{lo, hi}, bf16x2));
}
__device__ __forceinline__ float rbf(float f) { return __builtin_bit_cast(float, cvtpk(f, f) << 16); }
__device__ __forceinline__ float frcp(float x) { return __builtin_amdgcn_rcpf(x); }
__device__ __forceinline__ float dot2(unsigned k, unsigned q, float acc) {
  return __builtin_amdgcn_fdot2_f32_bf16(__builtin_bit_cast(bf16x2, k), __builtin_bit_cast(bf16x2, q), acc, false);
}
template <int CTRL> __device__ __forceinline__ float dpp(float x) {
  return __builtin_bit_cast(float, __builtin_amdgcn_mov_dpp(__builtin_bit_cast(int, x), CTRL, 0xf, 0xf, true));
}
constexpr int XOR1 = 0xB1, XOR2 = 0x4E, XOR7 = 0x141, XOR8 = 0x128, ROR4 = 0x124;
__device__ __forceinline__ float sum32(float a, float b) {
  auto s = __builtin_amdgcn_permlane32_swap(__float_as_uint(a), __float_as_uint(b), false, false);
  return __uint_as_float(s[0]) + __uint_as_float(s[1]);
}
__device__ __forceinline__ float sum16(float a, float b) {
  auto s = __builtin_amdgcn_permlane16_swap(__float_as_uint(a), __float_as_uint(b), false, false);
  return __uint_as_float(s[0]) + __uint_as_float(s[1]);
}
__device__ __forceinline__ float xrow16_max(float x) {
  auto s = __builtin_amdgcn_permlane16_swap(__float_as_uint(x), __float_as_uint(x), false, false);
  x = fmaxf(__uint_as_float(s[0]), __uint_as_float(s[1]));
  auto t = __builtin_amdgcn_permlane32_swap(__float_as_uint(x), __float_as_uint(x), false, false);
  return fmaxf(__uint_as_float(t[0]), __uint_as_float(t[1]));
}
__device__ __forceinline__ float xrow16_sum(float x) { x = sum16(x, x); return sum32(x, x); }
__device__ __forceinline__ float row_sum(float x) { x += dpp<XOR1>(x); x += dpp<XOR2>(x); x += dpp<XOR7>(x); x += dpp<XOR8>(x); return x; }
__device__ __forceinline__ float wave_sum(float x) { return xrow16_sum(row_sum(x)); }
__device__ __forceinline__ void bar() { asm volatile("s_waitcnt lgkmcnt(0)\n\ts_barrier" ::: "memory"); }
__device__ __forceinline__ void put(u64* p, unsigned tag, unsigned v) {
  __hip_atomic_store(p, ((u64)tag << 32) | v, __ATOMIC_RELAXED, __HIP_MEMORY_SCOPE_AGENT);
}
template <int N>
__device__ __forceinline__ void sweep(const u64* p, int stride, unsigned tag, unsigned (&v)[N]) {
  for (unsigned n = 0;; ++n) {
    bool ok = true;
#pragma unroll
    for (int k = 0; k < N; ++k) { const u64 a = __hip_atomic_load(p + k * stride, __ATOMIC_RELAXED, __HIP_MEMORY_SCOPE_AGENT); v[k] = (unsigned)a; ok &= (unsigned)(a >> 32) == tag; }
    if (__all(ok) || n > SPIN_CAP) break;
    __builtin_amdgcn_s_sleep(1);
  }
}
__device__ __forceinline__ void glds16(const void* gsrc, unsigned lds_dst) {
  unsigned keep;
  asm volatile("s_mov_b32 %0, m0\n\ts_mov_b32 m0, %2\n\ts_nop 0\n\tglobal_load_lds_dwordx4 %1, off nt\n\ts_mov_b32 m0, %0" : "=&s"(keep) : "v"(gsrc), "s"(lds_dst) : "memory");
}

__device__ __forceinline__ void glds16d(const void* gsrc, unsigned lds_dst) {
  unsigned keep;
  asm volatile("s_mov_b32 %0, m0\n\ts_mov_b32 m0, %2\n\ts_nop 0\n\tglobal_load_lds_dwordx4 %1, off\n\ts_mov_b32 m0, %0" : "=&s"(keep) : "v"(gsrc), "s"(lds_dst) : "memory");
}
__device__ __forceinline__ void glds4d(const void* gsrc, unsigned lds_dst) {
  unsigned keep;
  asm volatile("s_mov_b32 %0, m0\n\ts_mov_b32 m0, %2\n\ts_nop 0\n\tglobal_load_lds_dword %1, off\n\ts_mov_b32 m0, %0" : "=&s"(keep) : "v"(gsrc), "s"(lds_dst) : "memory");
}

constexpr int L_A = 0;
constexpr int L_X = 1024;
constexpr int L_RED = 2048;
constexpr int L_Q = 2304;
constexpr int L_P = 2560;
constexpr int L_MODS = 3072;
constexpr int L_PART = L_MODS + 3072;
constexpr int L_WST = L_PART + 640;
constexpr int L_MRG = L_WST + 20 * 512;
constexpr int L_KV = L_MRG + 4224;
constexpr int L_TOTAL = L_KV + 8 * 2048;

__device__ __forceinline__ void ln_norm(const float* lds, int lane, float x0, float x1, float& n0, float& n1) {
#pragma unroll
  for (int bb = 0; bb < 2; ++bb) {
    float v[8], s = 0.f;
#pragma unroll
    for (int j = 0; j < 8; ++j) { v[j] = lds[L_X + bb * 512 + lane + 64 * j]; s += v[j]; }
    const float mu = wave_sum(s) * (1.f / DM);
    float q = 0.f;
#pragma unroll
    for (int j = 0; j < 8; ++j) { const float d = v[j] - mu; q += d * d; }
    const float var = rbf(wave_sum(q) * (1.f / DM));
    const float sd = rbf(__builtin_amdgcn_sqrtf(rbf(var + 1e-5f)));
    const float t = rbf(rbf((bb ? x1 : x0) - rbf(mu)) * frcp(sd));
    if (bb) n1 = t; else n0 = t;
  }
}
__device__ __forceinline__ void gemv16_part(float* lds, int tid, const u32x4* wv) {
  const int lane = tid & 63, wave = tid >> 6;
  float acc[16];
#pragma unroll
  for (int j = 0; j < 16; ++j) acc[j] = 0.f;
#pragma unroll
  for (int i = 0; i < 2; ++i) {
    const int row = (tid >> 1) + 256 * i;
    const float a0 = lds[L_A + row], a1 = lds[L_A + 512 + row];
#pragma unroll
    for (int j = 0; j < 4; ++j) {
      const float lo = bflo(wv[i][j]), hi = bfhi(wv[i][j]);
      acc[2 * j] = fmaf(a0, lo, acc[2 * j]); acc[2 * j + 1] = fmaf(a0, hi, acc[2 * j + 1]);
      acc[8 + 2 * j] = fmaf(a1, lo, acc[8 + 2 * j]); acc[8 + 2 * j + 1] = fmaf(a1, hi, acc[8 + 2 * j + 1]);
    }
  }
#pragma unroll
  for (int h = 0; h < 8; ++h) acc[h] = sum32(acc[h], acc[h + 8]);
#pragma unroll
  for (int h = 0; h < 4; ++h) acc[h] = sum16(acc[h], acc[h + 4]);
#pragma unroll
  for (int h = 0; h < 4; ++h) { float v = acc[h]; v += dpp<XOR2>(v); v += dpp<ROR4>(v); v += dpp<XOR8>(v); acc[h] = v; }
  if ((lane & 14) == 0) *reinterpret_cast<f32x4*>(lds + L_RED + wave * 32 + (lane & 1) * 16 + (lane >> 4) * 4) = f32x4{acc[0], acc[1], acc[2], acc[3]};
}
__device__ __forceinline__ unsigned gemv16_fin(const float* lds, int t) {
  const int half = t >> 3, j = t & 7;
  float s0 = 0.f, s1 = 0.f;
#pragma unroll
  for (int ww = 0; ww < 8; ++ww) { s0 += lds[L_RED + ww * 32 + half * 16 + j]; s1 += lds[L_RED + ww * 32 + half * 16 + 8 + j]; }
  return cvtpk(s0, s1);
}

__global__ void __launch_bounds__(NT, 2) adaLN_cond__gqa_block_cached__glu_gated_bilinear__decode_kernel(const bf16* __restrict__ x, const bf16* __restrict__ kc, const bf16* __restrict__ vc,
                                              const bf16* __restrict__ wq, const bf16* __restrict__ wo, const bf16* __restrict__ wg,
                                              const bf16* __restrict__ wu, const bf16* __restrict__ wd, const bf16* __restrict__ c,
                                              const bf16* __restrict__ w_mod, bf16* __restrict__ out, char* __restrict__ ws) {
  extern __shared__ __attribute__((aligned(16))) float lds[];
  const int tid = threadIdx.x, lane = tid & 63, wave = __builtin_amdgcn_readfirstlane(tid >> 6);
  const int w = blockIdx.x;
  const unsigned tag = __hip_atomic_load((const unsigned*)(ws + WS_EPOCH), __ATOMIC_RELAXED, __HIP_MEMORY_SCOPE_AGENT) + 1u;
  u64* g1 = (u64*)(ws + WS_G1); u64* g2 = (u64*)(ws + WS_G2); u64* g3 = (u64*)(ws + WS_G3);
  u64* g4 = (u64*)(ws + WS_G4); u64* g5 = (u64*)(ws + WS_G5); u64* g6 = (u64*)(ws + WS_G6);
  const int pair = w >> 4, b = pair >> 3, kvh = pair & 7, s = w & 15;
  const int r = lane >> 4, cc = lane & 15;
  const int wx = w & 7, wi = w >> 3;
  const bool has_mod = wi < 24;
  const int mslice = wi < 8 ? wx * 8 + wi : (wi < 24 ? 64 + wx * 16 + (wi - 8) : 0);
  const int qslice = wx * 32 + wi;
  const int cg = wx * 8 + (wi & 7), kq = wi >> 3;
  const bool has_gu = wi < 28;
  const int wgu = has_gu ? wx * 28 + wi : 0;
  const int col0 = 2 * (wx * 32 + wi);
  const unsigned short* cs = (const unsigned short*)c; const unsigned short* xs = (const unsigned short*)x;
  const float c0 = bf2f(cs[tid]), c1 = bf2f(cs[512 + tid]);
  const float x0 = bf2f(xs[tid]), x1 = bf2f(xs[512 + tid]);
  u32x4 wm[2], wqv[2];
#pragma unroll
  for (int i = 0; i < 2; ++i) {
    const int row = (tid >> 1) + 256 * i;
    wm[i] = *reinterpret_cast<const u32x4*>(w_mod + (size_t)row * (6 * DM) + mslice * 16 + (tid & 1) * 8);
    wqv[i] = *reinterpret_cast<const u32x4*>(wq + (size_t)row * NQ + qslice * 16 + (tid & 1) * 8);
  }
  lds[L_A + tid] = rbf(c0 * frcp(1.f + __expf(-c0))); lds[L_A + 512 + tid] = rbf(c1 * frcp(1.f + __expf(-c1)));
  lds[L_X + tid] = x0; lds[L_X + 512 + tid] = x1;
  bar();
  gemv16_part(lds, tid, wm);
  float n0, n1;
  ln_norm(lds, lane, x0, x1, n0, n1);
  bar();
  if (has_mod && tid < 16) put(g1 + mslice * 16 + tid, tag, gemv16_fin(lds, tid));
  const size_t base = ((size_t)(b * HKV + kvh) * SK + (size_t)s * PPS * PAGE) * DH + (size_t)r * DH + 8 * cc;
  const bf16* kb = kc + base; const bf16* vb = vc + base;
  u32x4 KA[4], VA[4], KB[4], VB[4];
#define ISSUE(KX, VX, pg_) do { const size_t off_ = (size_t)(pg_) * PAGE * DH; \
    _Pragma("unroll") for (int t_ = 0; t_ < 4; ++t_) { KX[t_] = __builtin_nontemporal_load(reinterpret_cast<const u32x4*>(kb + off_ + t_ * 4 * DH)); VX[t_] = __builtin_nontemporal_load(reinterpret_cast<const u32x4*>(vb + off_ + t_ * 4 * DH)); } } while (0)
#define ISSUE_SEL(KX, VX, pg_, ok_) do { const size_t off_ = (size_t)(pg_) * PAGE * DH; \
    const bf16* kp_ = (ok_) ? kb + off_ : kc; const bf16* vp_ = (ok_) ? vb + off_ : vc; \
    _Pragma("unroll") for (int t_ = 0; t_ < 4; ++t_) { KX[t_] = __builtin_nontemporal_load(reinterpret_cast<const u32x4*>(kp_ + t_ * 4 * DH)); VX[t_] = __builtin_nontemporal_load(reinterpret_cast<const u32x4*>(vp_ + t_ * 4 * DH)); } } while (0)
  const unsigned lkv = (unsigned)(size_t)((__attribute__((address_space(3))) float*)(lds + L_KV));
#define DMA_PAGE(slot_) do { const size_t off_ = (size_t)(120 + (slot_)) * PAGE * DH; \
    _Pragma("unroll") for (int t_ = 0; t_ < 4; ++t_) { glds16(kb + off_ + t_ * 4 * DH, lkv + (slot_) * 8192 + t_ * 1024); glds16(vb + off_ + t_ * 4 * DH, lkv + (slot_) * 8192 + 4096 + t_ * 1024); } } while (0)
#define STASH_DMA() do {     \
      const unsigned lst = (unsigned)(size_t)((__attribute__((address_space(3))) float*)(lds + L_WST)) + wave * 1024; \
      glds16d(wo + (size_t)(kq * 1024 + tid) * DM + cg * 8, lst); \
      glds16d(wo + (size_t)(kq * 1024 + 512 + tid) * DM + cg * 8, lst + 8192); \
      glds16d(wg + (size_t)tid * F + 8 * wgu, lst + 2 * 8192); \
      glds16d(wu + (size_t)tid * F + 8 * wgu, lst + 3 * 8192); \
      const unsigned lsd = (unsigned)(size_t)((__attribute__((address_space(3))) float*)(lds + L_WST)) + 4 * 8192 + wave * 256; \
      _Pragma("unroll") for (int i = 0; i < 4; ++i) { const int k_ = tid + 512 * i; glds4d(wd + (size_t)(k_ < F ? k_ : 0) * DM + col0, lsd + i * 2048); } } while (0)
  const int pfirst = wave == 0 ? 106 : (wave <= 5 ? (wave - 1) * 16 : 64 + (wave - 5) * 14), nu = (wave >= 1 && wave <= 4) ? 8 : 7;
  constexpr int pstep = 1;
  if (wave != 0) {
    ISSUE(KA, VA, pfirst);
    DMA_PAGE(wave);
    if (wave == 1) DMA_PAGE(0);
  } else {
    unsigned v[16];
    sweep<16>(g1 + lane, 64, tag, v);
#pragma unroll
    for (int j = 0; j < 16; ++j) ((unsigned*)(lds + L_MODS))[lane + 64 * j] = v[j];
  }
  bar();
  { const unsigned sh = ((const unsigned*)(lds + L_MODS))[tid], sc = ((const unsigned*)(lds + L_MODS))[512 + tid];
    lds[L_A + tid] = rbf(rbf(n0 * rbf(1.f + bflo(sc))) + bflo(sh));
    lds[L_A + 512 + tid] = rbf(rbf(n1 * rbf(1.f + bfhi(sc))) + bfhi(sh)); }
  bar();
  gemv16_part(lds, tid, wqv);
  bar();
  if (wave != 0) {
    ISSUE(KB, VB, pfirst + pstep);
    STASH_DMA();
    asm volatile("s_waitcnt vmcnt(16)" ::: "memory");
  } else {
    if (tid < 16) put(g2 + qslice * 16 + tid, tag, gemv16_fin(lds, tid));
    unsigned qq[8];
    sweep<8>(g2 + kvh * 512 + lane, 64, tag, qq);
#pragma unroll
    for (int j = 0; j < 8; ++j) ((unsigned short*)(lds + L_Q))[lane + 64 * j] = (unsigned short)(b ? (qq[j] >> 16) : (qq[j] & 0xffffu));
    ISSUE(KA, VA, pfirst);
    ISSUE(KB, VB, pfirst + pstep);
  }
  bar();
  u32x4 qpk[4];
#pragma unroll
  for (int g = 0; g < 4; ++g) qpk[g] = *reinterpret_cast<const u32x4*>((const unsigned short*)(lds + L_Q) + g * DH + 8 * cc);
  constexpr float C2 = 0.088388347648318440f * 1.4426950408889634f;
  float m = -INFINITY, l = 0.f, acc[32];
#pragma unroll
  for (int k = 0; k < 32; ++k) acc[k] = 0.f;
  float* prow = lds + L_P + (wave * 4 + r) * 16;
#define TR_STEP(HALF, CTRL, BIT) _Pragma("unroll") for (int h = 0; h < (HALF); ++h) { \
      const float lo_ = xx[h], hi_ = xx[h + (HALF)]; const float t1_ = lo_ + dpp<CTRL>(lo_), t2_ = hi_ + dpp<CTRL>(hi_); \
      xx[h] = (cc & (BIT)) ? t2_ : t1_; }
#define PROCESS(KX, VX) do { \
    float xx[16]; \
    _Pragma("unroll") for (int t = 0; t < 4; ++t) _Pragma("unroll") for (int g = 0; g < 4; ++g) { float d = 0.f; \
        _Pragma("unroll") for (int k = 0; k < 4; ++k) d = dot2(KX[t][k], qpk[g][k], d); xx[t * 4 + g] = d; } \
    TR_STEP(8, XOR8, 8) TR_STEP(4, XOR7, 4) TR_STEP(2, XOR2, 2) TR_STEP(1, XOR1, 1) \
    const float sc = xx[0] * C2; \
    float pm = sc; pm = fmaxf(pm, dpp<ROR4>(pm)); pm = fmaxf(pm, dpp<XOR8>(pm)); pm = xrow16_max(pm); \
    const float mn = fmaxf(m, pm); \
    const float alpha = __builtin_amdgcn_exp2f(m - mn), p = __builtin_amdgcn_exp2f(sc - mn); \
    l = fmaf(l, alpha, p); m = mn; \
    _Pragma("unroll") for (int q4 = 0; q4 < 4; ++q4) { \
      const float al = q4 == 0 ? dpp<0x00>(alpha) : q4 == 1 ? dpp<0x55>(alpha) : q4 == 2 ? dpp<0xAA>(alpha) : dpp<0xFF>(alpha); \
      _Pragma("unroll") for (int k = 0; k < 8; ++k) acc[q4 * 8 + k] *= al; } \
    prow[cc] = p; \
    asm volatile("s_waitcnt lgkmcnt(0)" ::: "memory"); \
    f32x4 pr[4]; \
    _Pragma("unroll") for (int u_ = 0; u_ < 4; ++u_) pr[u_] = *reinterpret_cast<const f32x4*>(prow + 4 * u_); \
    _Pragma("unroll") for (int t = 0; t < 4; ++t) { \
      float vf[8]; \
      _Pragma("unroll") for (int k = 0; k < 4; ++k) { vf[2 * k] = bflo(VX[t][k]); vf[2 * k + 1] = bfhi(VX[t][k]); } \
      _Pragma("unroll") for (int g = 0; g < 4; ++g) { const float pg_ = pr[t][g]; \
        _Pragma("unroll") for (int k = 0; k < 8; ++k) acc[g * 8 + k] = fmaf(pg_, vf[k], acc[g * 8 + k]); } } \
  } while (0)
  for (int u = 0; u < nu; ++u) {
    PROCESS(KA, VA);
    if (u == 0) {
      const float* kl = lds + L_KV + wave * 2048 + lane * 4;
#pragma unroll
      for (int t = 0; t < 4; ++t) { KA[t] = *reinterpret_cast<const u32x4*>(kl + t * 256); VA[t] = *reinterpret_cast<const u32x4*>(kl + 1024 + t * 256); }
      PROCESS(KA, VA);
    }
    ISSUE_SEL(KA, VA, pfirst + (2 * u + 2) * pstep, u + 1 < nu);
    if (u == 1 && wave == 0) {
      STASH_DMA();
    } else if (u == 3 && wave == 0) {
#pragma unroll
      for (int hf = 0; hf < 2; ++hf) { unsigned v[16]; sweep<16>(g1 + 1024 + hf * 1024 + lane, 64, tag, v);
#pragma unroll
        for (int j = 0; j < 16; ++j) ((unsigned*)(lds + L_MODS))[1024 + hf * 1024 + lane + 64 * j] = v[j]; }
    }
    PROCESS(KB, VB);
    ISSUE_SEL(KB, VB, pfirst + (2 * u + 3) * pstep, u + 1 < nu);
  }
#undef PROCESS
#undef TR_STEP
  asm volatile("s_waitcnt vmcnt(0)" ::: "memory");
#pragma unroll
  for (int k = 0; k < 32; ++k) acc[k] = xrow16_sum(acc[k]);
  { float t = l; t += dpp<ROR4>(t); t += dpp<XOR8>(t); l = xrow16_sum(t); }
  float* mrg = lds + L_MRG;
  if (r == 0) {
#pragma unroll
    for (int g = 0; g < 4; ++g) {
      float* rec = mrg + (wave * 4 + g) * 132;
#pragma unroll
      for (int k = 0; k < 8; k += 4) *reinterpret_cast<f32x4*>(rec + 4 + 8 * cc + k) = f32x4{acc[g * 8 + k], acc[g * 8 + k + 1], acc[g * 8 + k + 2], acc[g * 8 + k + 3]};
      if (cc == g) { rec[0] = m; rec[1] = l; } } }
  bar();
  { const int g = tid >> 7, d = tid & 127;
    float M = -INFINITY;
#pragma unroll
    for (int ww = 0; ww < NW; ++ww) M = fmaxf(M, mrg[(ww * 4 + g) * 132]);
    float L = 0.f, o0 = 0.f;
#pragma unroll
    for (int ww = 0; ww < NW; ++ww) { const float* rec = mrg + (ww * 4 + g) * 132;
      const float wgt = __builtin_amdgcn_exp2f(rec[0] - M);
      L = fmaf(wgt, rec[1], L); o0 = fmaf(wgt, rec[4 + d], o0); }
    u64* dst = g3 + (size_t)((kvh * 4 + g) * 8 + (d >> 4)) * 576 + (b * 16 + s) * 18;
    put(dst + 2 + (d & 15), tag, __float_as_uint(o0));
    if ((d & 15) == 0) { put(dst, tag, __float_as_uint(M)); put(dst + 1, tag, __float_as_uint(L)); }
  }
  { unsigned v[1]; sweep<1>(g3 + (size_t)w * 576 + tid, 0, tag, v);
    lds[L_PART + tid] = __uint_as_float(v[0]);
    if (wave == 0) { sweep<1>(g3 + (size_t)w * 576 + 512 + tid, 0, tag, v); lds[L_PART + 512 + tid] = __uint_as_float(v[0]); } }
  bar();
  if (tid < 32) {
    const int bb = tid >> 4, d = tid & 15;
    const float* pp = lds + L_PART + bb * 16 * 18;
    float M = -INFINITY;
#pragma unroll
    for (int ss = 0; ss < 16; ++ss) M = fmaxf(M, pp[ss * 18]);
    float L = 0.f, o0 = 0.f;
#pragma unroll
    for (int ss = 0; ss < 16; ++ss) { const float wgt = __builtin_amdgcn_exp2f(pp[ss * 18] - M);
      L = fmaf(wgt, pp[ss * 18 + 1], L); o0 = fmaf(wgt, pp[ss * 18 + 2 + d], o0); }
    const unsigned ob = cvtpk(o0 * frcp(L), 0.f) & 0xffffu;
    const unsigned other = __shfl_down(ob, 16);
    if (tid < 16) put(g4 + w * 16 + tid, tag, ob | (other << 16));
  }
  { unsigned ov[2]; sweep<2>(g4 + kq * 1024 + tid, 512, tag, ov);
    float v[16];
#pragma unroll
    for (int j = 0; j < 16; ++j) v[j] = 0.f;
#pragma unroll
    for (int i = 0; i < 2; ++i) {
      const u32x4 wv = *reinterpret_cast<const u32x4*>(lds + L_WST + (i * 512 + tid) * 4);
      const float o0 = bflo(ov[i]), o1 = bfhi(ov[i]);
#pragma unroll
      for (int j = 0; j < 4; ++j) { const float lo = bflo(wv[j]), hi = bfhi(wv[j]);
        v[2 * j] = fmaf(o0, lo, v[2 * j]); v[2 * j + 1] = fmaf(o0, hi, v[2 * j + 1]); v[8 + 2 * j] = fmaf(o1, lo, v[8 + 2 * j]); v[8 + 2 * j + 1] = fmaf(o1, hi, v[8 + 2 * j + 1]); }
    }
#pragma unroll
    for (int h = 0; h < 8; ++h) v[h] = sum32(v[h], v[h + 8]);
#pragma unroll
    for (int h = 0; h < 4; ++h) v[h] = sum16(v[h], v[h + 4]);
#pragma unroll
    for (int h = 0; h < 4; ++h) v[h] = row_sum(v[h]);
    if (cc == 0) *reinterpret_cast<f32x4*>(lds + L_RED + wave * 16 + r * 4) = f32x4{v[0], v[1], v[2], v[3]};
    bar();
    if (tid < 16) {
      float a = 0.f;
#pragma unroll
      for (int ww = 0; ww < NW; ++ww) a += lds[L_RED + ww * 16 + tid];
      put(g5 + (kq * 2 + (tid >> 3)) * 512 + cg * 8 + (tid & 7), tag, __float_as_uint(a));
    } }
  { unsigned pv[8]; sweep<8>(g5 + tid, 512, tag, pv);
    const float a0 = (__uint_as_float(pv[0]) + __uint_as_float(pv[2])) + (__uint_as_float(pv[4]) + __uint_as_float(pv[6]));
    const float a1 = (__uint_as_float(pv[1]) + __uint_as_float(pv[3])) + (__uint_as_float(pv[5]) + __uint_as_float(pv[7]));
    const unsigned ga = ((const unsigned*)(lds + L_MODS))[1024 + tid];
    const float y0 = rbf(x0 + rbf(bflo(ga) * rbf(a0))), y1 = rbf(x1 + rbf(bfhi(ga) * rbf(a1)));
    lds[L_X + tid] = y0; lds[L_X + 512 + tid] = y1;
    bar();
    if (has_gu) {
      float m0, m1; ln_norm(lds, lane, y0, y1, m0, m1);
      const unsigned sh = ((const unsigned*)(lds + L_MODS))[1536 + tid], sc = ((const unsigned*)(lds + L_MODS))[2048 + tid];
      const float h0 = rbf(rbf(m0 * rbf(1.f + bflo(sc))) + bflo(sh)), h1 = rbf(rbf(m1 * rbf(1.f + bfhi(sc))) + bfhi(sh));
      const u32x4 wgv = *reinterpret_cast<const u32x4*>(lds + L_WST + (2 * 512 + tid) * 4), wuv = *reinterpret_cast<const u32x4*>(lds + L_WST + (3 * 512 + tid) * 4);
      float v2[32];
#pragma unroll
      for (int j = 0; j < 4; ++j) {
        v2[2 * j] = h0 * bflo(wgv[j]); v2[2 * j + 1] = h0 * bfhi(wgv[j]); v2[8 + 2 * j] = h0 * bflo(wuv[j]); v2[8 + 2 * j + 1] = h0 * bfhi(wuv[j]);
        v2[16 + 2 * j] = h1 * bflo(wgv[j]); v2[16 + 2 * j + 1] = h1 * bfhi(wgv[j]); v2[24 + 2 * j] = h1 * bflo(wuv[j]); v2[24 + 2 * j + 1] = h1 * bfhi(wuv[j]);
      }
#pragma unroll
      for (int h = 0; h < 16; ++h) v2[h] = sum32(v2[h], v2[h + 16]);
#pragma unroll
      for (int h = 0; h < 8; ++h) v2[h] = sum16(v2[h], v2[h + 8]);
#define TRS(HALF, CTRL, BIT) _Pragma("unroll") for (int h = 0; h < (HALF); ++h) { \
      const float lo_ = v2[h], hi_ = v2[h + (HALF)]; const float t1_ = lo_ + dpp<CTRL>(lo_), t2_ = hi_ + dpp<CTRL>(hi_); \
      v2[h] = (lane & (BIT)) ? t2_ : t1_; }
      TRS(4, XOR8, 8) TRS(2, XOR7, 4) TRS(1, XOR2, 2)
#undef TRS
      v2[0] += dpp<XOR1>(v2[0]);
      if ((lane & 1) == 0) lds[L_RED + wave * 32 + (lane >> 1)] = v2[0];
    }
    bar();
    if (has_gu && tid < 8) {
      float g0 = 0.f, u0 = 0.f, g1s = 0.f, u1 = 0.f;
#pragma unroll
      for (int ww = 0; ww < NW; ++ww) { g0 += lds[L_RED + ww * 32 + tid]; u0 += lds[L_RED + ww * 32 + 8 + tid]; g1s += lds[L_RED + ww * 32 + 16 + tid]; u1 += lds[L_RED + ww * 32 + 24 + tid]; }
      put(g6 + 8 * wgu + tid, tag, cvtpk(rbf(g0) * rbf(u0), rbf(g1s) * rbf(u1)));
    } }
  { unsigned hv[4];
    for (unsigned n = 0;; ++n) {
      bool ok = true;
#pragma unroll
      for (int i = 0; i < 4; ++i) { const int k = tid + 512 * i; const u64 a = __hip_atomic_load(g6 + (k < F ? k : 0), __ATOMIC_RELAXED, __HIP_MEMORY_SCOPE_AGENT); hv[i] = k < F ? (unsigned)a : 0u; ok &= (unsigned)(a >> 32) == tag; }
      if (__all(ok) || n > SPIN_CAP) break;
      __builtin_amdgcn_s_sleep(1);
    }
    float a00 = 0.f, a01 = 0.f, a10 = 0.f, a11 = 0.f;
    const u32x4 wdv = u32x4{((const unsigned*)(lds + L_WST))[4 * 2048 + tid], ((const unsigned*)(lds + L_WST))[4 * 2048 + 512 + tid], ((const unsigned*)(lds + L_WST))[4 * 2048 + 1024 + tid], ((const unsigned*)(lds + L_WST))[4 * 2048 + 1536 + tid]};
#pragma unroll
    for (int i = 0; i < 4; ++i) {
      const float w0 = bflo(wdv[i]), w1 = bfhi(wdv[i]), o0 = bflo(hv[i]), o1 = bfhi(hv[i]);
      a00 = fmaf(o0, w0, a00); a01 = fmaf(o0, w1, a01); a10 = fmaf(o1, w0, a10); a11 = fmaf(o1, w1, a11);
    }
    a00 = wave_sum(a00); a01 = wave_sum(a01); a10 = wave_sum(a10); a11 = wave_sum(a11);
    if (lane == 0) *reinterpret_cast<f32x4*>(lds + L_RED + wave * 4) = f32x4{a00, a01, a10, a11};
    bar();
    if (tid < 2) {
      float a0 = 0.f, a1 = 0.f;
#pragma unroll
      for (int ww = 0; ww < NW; ++ww) { a0 += lds[L_RED + ww * 4 + tid]; a1 += lds[L_RED + ww * 4 + 2 + tid]; }
      const int n = col0 + tid;
      const unsigned gb = ((const unsigned*)(lds + L_MODS))[2560 + n];
      out[n] = __float2bfloat16(lds[L_X + n] + rbf(bflo(gb) * rbf(a0)));
      out[512 + n] = __float2bfloat16(lds[L_X + 512 + n] + rbf(bfhi(gb) * rbf(a1)));
    }
    if (w == 0 && tid == 0) __hip_atomic_store((unsigned*)(ws + WS_EPOCH), tag, __ATOMIC_RELAXED, __HIP_MEMORY_SCOPE_AGENT);
  }
}

extern "C" void kernel_launch(void* const* d_in, const int* in_sizes, int n_in, void* d_out, int out_size, void* d_ws, size_t ws_size, hipStream_t stream) {
  (void)hipFuncSetAttribute((const void*)adaLN_cond__gqa_block_cached__glu_gated_bilinear__decode_kernel, hipFuncAttributeMaxDynamicSharedMemorySize, L_TOTAL * 4);
  adaLN_cond__gqa_block_cached__glu_gated_bilinear__decode_kernel<<<NWG, NT, L_TOTAL * 4, stream>>>((const bf16*)d_in[0], (const bf16*)d_in[1], (const bf16*)d_in[2], (const bf16*)d_in[3], (const bf16*)d_in[4],
                                        (const bf16*)d_in[6], (const bf16*)d_in[7], (const bf16*)d_in[8], (const bf16*)d_in[9], (const bf16*)d_in[10],
                                        (bf16*)d_out, (char*)d_ws);
}
```
